```python
import torch

def _softmax(x, axis=-1):
    return torch.softmax(x, dim=axis)
def _gelu(x):
    return torch.nn.functional.gelu(x, approximate="tanh")
def _silu(x):
    return torch.nn.functional.silu(x)
def _sigmoid(x):
    return torch.sigmoid(x)
def _layernorm(x, axis=-1, eps=1e-5):
    m = x.mean(dim=axis, keepdim=True)
    v = x.var(dim=axis, keepdim=True, unbiased=False)
    return (x - m) / torch.sqrt(v + eps)
def _scatter_add(tgt, idx, src, axis=0):
    return tgt.clone().index_add_(axis, idx, src)
def _segment_sum(src, seg, n):
    out = torch.zeros((n,) + src.shape[1:], dtype=src.dtype)
    return out.index_add_(0, seg, src)
def _pad(x, pw):
    flat = tuple(v for pair in reversed(pw) for v in pair)
    return torch.nn.functional.pad(x, flat)

kernel_name = 'sequential_preLN__ssm_conv_s4d__geglu_ffn__prompt'

def setup_inputs():

    _w = (lambda a: (lambda t: t.to(torch.bfloat16) if t.is_floating_point() else t)(torch.as_tensor(a)))
    def _A_setup():
        Bsz, L, D, N = 1, 2048, 576, 128
        return {
            "u": _w((lambda *s: torch.randn(*s, dtype=torch.bfloat16) * 0.05)(Bsz, L, D)),
            "log_A_real": _w((lambda *s: torch.randn(*s, dtype=torch.bfloat16) * 6.4)(D, N) * 0.5),
            "A_imag": _w((lambda *s: torch.randn(*s, dtype=torch.bfloat16) * 0.05)(D, N) * 3.14),
            "C_real": _w((lambda *s: torch.randn(*s, dtype=torch.bfloat16) * 0.05)(D, N) / N ** 0.5),
            "C_imag": _w((lambda *s: torch.randn(*s, dtype=torch.bfloat16) * 0.05)(D, N) / N ** 0.5),
            "log_dt": _w((lambda *s: torch.randn(*s, dtype=torch.bfloat16) * 0.05)(D) * 0.5 - 4.0),
            "D_skip": _w((lambda *s: torch.randn(*s, dtype=torch.bfloat16) * 0.05)(D)),
        }
    _A_in = _A_setup()

    def _B_setup():
        B, S, D, F = 1, 2048, 576, 4608
        return {
            "x": _w((lambda *s: torch.randn(*s, dtype=torch.bfloat16) * 0.05)(B, S, D)),
            "w_gate": _w((lambda *s: torch.randn(*s, dtype=torch.bfloat16) * 0.05)(D, F)),
            "w_up": _w((lambda *s: torch.randn(*s, dtype=torch.bfloat16) * 0.05)(D, F)),
            "w_down": _w((lambda *s: torch.randn(*s, dtype=torch.bfloat16) * 0.05)(F, D)),
        }
    _B_in = _B_setup()

    return {
        "A_u": _A_in["u"],
        "A_log_A_real": _A_in["log_A_real"],
        "A_A_imag": _A_in["A_imag"],
        "A_C_real": _A_in["C_real"],
        "A_C_imag": _A_in["C_imag"],
        "A_log_dt": _A_in["log_dt"],
        "A_D_skip": _A_in["D_skip"],
        "B_x": _B_in["x"],
        "B_w_gate": _B_in["w_gate"],
        "B_w_up": _B_in["w_up"],
        "B_w_down": _B_in["w_down"],
    }

def reference(A_u, A_log_A_real, A_A_imag, A_C_real, A_C_imag, A_log_dt, A_D_skip, B_x, B_w_gate, B_w_up, B_w_down):
    def _A_ref(u, log_A_real, A_imag, C_real, C_imag, log_dt, D_skip):
        L = u.shape[1]
        dt = torch.exp(log_dt)
        A = -torch.exp(log_A_real) + 1j * A_imag
        C = (C_real + 1j * C_imag) * dt[:, None]
        pos = (lambda x, dt: x.to(dt))(torch.arange(L), log_dt.dtype)
        P = torch.exp((A * dt[:, None])[:, :, None] * pos[None, None, :])
        K = 2.0 * torch.einsum("dn,dnl->dl", C, P).real
        L2 = 2 * L
        u_t = (lambda x, *d: x.permute(*d))(u, 0, 2, 1)
        Kf = (lambda x, *a, **k: torch.fft.rfft(x.float(), *a, **k))(K, L2)
        Uf = (lambda x, *a, **k: torch.fft.rfft(x.float(), *a, **k))(u_t, L2)
        y = (lambda x, *a, **k: torch.fft.irfft(x, *a, **k).to(torch.bfloat16))(Uf * Kf[None, :, :], L2)[:, :, :L]
        y = (lambda x, *d: x.permute(*d))(y, 0, 2, 1)
        return y + u * D_skip[None, None, :]

    def _B_ref(x, w_gate, w_up, w_down):
        return (_gelu(x @ w_gate) * (x @ w_up)) @ w_down

    x = A_u
    h = x + _A_ref(_layernorm(x, axis=-1), A_log_A_real, A_A_imag, A_C_real, A_C_imag, A_log_dt, A_D_skip)
    h2 = h + _B_ref(_layernorm(h, axis=-1), B_w_gate, B_w_up, B_w_down)
    return h2
```

```cpp
#include <hip/hip_runtime.h>
#include <hip/hip_bf16.h>
#include <stdint.h>
typedef unsigned short u16;
constexpr int L=2048, D=576, NM=128, F=4608;
__device__ __forceinline__ float bf2f(u16 v){ return __uint_as_float(((unsigned)v)<<16); }
__device__ __forceinline__ u16 f2bf(float f){ __bf16 b=(__bf16)f; return __builtin_bit_cast(u16,b); }
__device__ __forceinline__ float rbf(float f){ return bf2f(f2bf(f)); }

__global__ void ln_k(const u16* in, u16* out){
  int row=blockIdx.x, t=threadIdx.x;
  float v[9]; float s=0;
  for(int i=0;i<9;i++){ v[i]=bf2f(in[row*D+i*64+t]); s+=v[i]; }
  for(int o=32;o;o>>=1) s+=__shfl_xor(s,o);
  float m=s/D; float q=0;
  for(int i=0;i<9;i++){ float d=v[i]-m; q+=d*d; }
  for(int o=32;o;o>>=1) q+=__shfl_xor(q,o);
  float var=rbf(q/D); float mb=rbf(m);
  float sd=rbf(sqrtf(rbf(var+1e-5f)));
  for(int i=0;i<9;i++) out[row*D+i*64+t]=f2bf(rbf(v[i]-mb)/sd);
}
__global__ void kgen_k(const u16* logA,const u16* Aim,const u16* Cr,const u16* Ci,const u16* logdt,float* K){
  int d=blockIdx.x; __shared__ float zr[NM],zi[NM],cr[NM],ci[NM];
  float dt=rbf(expf(bf2f(logdt[d])));
  if(threadIdx.x<NM){int n=threadIdx.x; float ar=-rbf(expf(bf2f(logA[d*NM+n]))); zr[n]=ar*dt; zi[n]=bf2f(Aim[d*NM+n])*dt; cr[n]=bf2f(Cr[d*NM+n])*dt; ci[n]=bf2f(Ci[d*NM+n])*dt;}
  __syncthreads();
  for(int l=threadIdx.x;l<L;l+=blockDim.x){ float p=rbf((float)l); float acc=0;
    for(int n=0;n<NM;n++){ float e=expf(zr[n]*p); float sn,cs; sincosf(zi[n]*p,&sn,&cs); acc+=e*(cr[n]*cs-ci[n]*sn);} K[d*L+l]=2.f*acc; }
}
__global__ void conv_k(const float* K,const u16* u,const u16* x,const u16* Dsk,u16* h){
  int d=blockIdx.x; __shared__ float ks[L],us[L];
  for(int l=threadIdx.x;l<L;l+=blockDim.x){ks[l]=K[d*L+l]; us[l]=bf2f(u[l*D+d]);}
  __syncthreads();
  float dk=bf2f(Dsk[d]);
  for(int l=threadIdx.x;l<L;l+=blockDim.x){ float acc=0; for(int s=0;s<=l;s++) acc+=ks[l-s]*us[s];
    float y=rbf(rbf(acc)+rbf(us[l]*dk)); h[l*D+d]=f2bf(bf2f(x[l*D+d])+y);}
}
__device__ __forceinline__ float gelu(float x){ float y=0.7978845608028654f*(x+0.044715f*x*x*x); return 0.5f*x*(1.f+tanhf(y)); }
__global__ void g1_k(const u16* xn,const u16* wg,const u16* wu,u16* G){
  __shared__ float xs[16][D]; int r0=blockIdx.y*16, f=blockIdx.x*256+threadIdx.x;
  for(int i=threadIdx.x;i<16*D;i+=256) xs[i/D][i%D]=bf2f(xn[r0*D+i]);
  __syncthreads();
  float a[16]={},b[16]={};
  for(int k=0;k<D;k++){ float g=bf2f(wg[k*F+f]),u=bf2f(wu[k*F+f]);
#pragma unroll
    for(int r=0;r<16;r++){a[r]+=xs[r][k]*g;b[r]+=xs[r][k]*u;} }
#pragma unroll
  for(int r=0;r<16;r++) G[(r0+r)*F+f]=f2bf(rbf(gelu(rbf(a[r])))*rbf(b[r]));
}
__global__ void g2_k(const u16* G,const u16* wd,const u16* h,u16* out){
  __shared__ float gs[4][F]; int r0=blockIdx.x*4, c=threadIdx.x;
  for(int i=threadIdx.x;i<4*F;i+=576) gs[i/F][i%F]=bf2f(G[r0*F+i]);
  __syncthreads();
  float a[4]={};
  for(int k=0;k<F;k++){ float w=bf2f(wd[k*D+c]);
#pragma unroll
    for(int r=0;r<4;r++)a[r]+=gs[r][k]*w; }
#pragma unroll
  for(int r=0;r<4;r++) out[(r0+r)*D+c]=f2bf(bf2f(h[(r0+r)*D+c])+rbf(a[r]));
}
extern "C" void kernel_launch(void* const* d_in, const int* in_sizes, int n_in, void* d_out, int out_size, void* d_ws, size_t ws_size, hipStream_t stream){
  const u16* x=(const u16*)d_in[0];
  char* ws=(char*)d_ws;
  u16* u=(u16*)ws; float* K=(float*)(ws+(4<<20)); u16* h=(u16*)(ws+(12<<20)); u16* xn=(u16*)(ws+(16<<20)); u16* G=(u16*)(ws+(20<<20));
  ln_k<<<L,64,0,stream>>>(x,u);
  kgen_k<<<D,256,0,stream>>>((const u16*)d_in[1],(const u16*)d_in[2],(const u16*)d_in[3],(const u16*)d_in[4],(const u16*)d_in[5],K);
  conv_k<<<D,256,0,stream>>>(K,u,x,(const u16*)d_in[6],h);
  ln_k<<<L,64,0,stream>>>(h,xn);
  g1_k<<<dim3(F/256,L/16),256,0,stream>>>(xn,(const u16*)d_in[8],(const u16*)d_in[9],G);
  g2_k<<<L/4,576,0,stream>>>(G,(const u16*)d_in[10],h,(u16*)d_out);
}
```

```cpp
#include <hip/hip_runtime.h>
#include <stdint.h>
typedef unsigned short u16;
typedef unsigned int u32;
typedef u32 u32x2 __attribute__((ext_vector_type(2)));
typedef u32 u32x4 __attribute__((ext_vector_type(4)));
typedef float f32x2 __attribute__((ext_vector_type(2)));
typedef float f32x4 __attribute__((ext_vector_type(4)));
typedef _Float16 f16x2 __attribute__((ext_vector_type(2)));
typedef _Float16 f16x8 __attribute__((ext_vector_type(8)));
typedef __bf16 bf16x2 __attribute__((ext_vector_type(2)));
typedef short s16x4 __attribute__((ext_vector_type(4)));
#define DI __device__ __forceinline__
#define LAS __attribute__((address_space(3)))

constexpr int L_ = 2048, D_ = 576, NM = 128, F_ = 4608;
constexpr u32 WS_UT  = 0;
#define UT_ROW 4224
constexpr u32 UT_ARR = D_ * UT_ROW;
constexpr u32 WS_ER  = 5u << 20;
constexpr u32 ER_ROW = 4160;
constexpr u32 WS_HT  = 8u << 20;
constexpr u32 WS_CS  = 21u << 19;
constexpr u32 WS_FLG = (11u << 20) + (256u << 10);
constexpr u32 WS_WP  = 12u << 20;
constexpr u32 WS_G   = 18u << 20;
constexpr u32 WS_EX  = 37u << 20;
constexpr u32 WS_U = 0, U_BLK = 9216, WS_SM = 4u << 20;
constexpr u32 WS_SYNC = 5u << 20, S_ECHO = 4096, S_U = 8192;
constexpr u32 F_H = 294912, F_W = 315392, F_BYTES = 319488;
constexpr int HREP = 16, HSH = 8, HSP = 128;
#define CS3 1
#define WPOLL 1
constexpr u32 WS_STAMP = 200u << 20;

DI float bf2f(u32 v) { return __uint_as_float(v << 16); }
DI float bflo(u32 v) { return __uint_as_float(v << 16); }
DI float bfhi(u32 v) { return __uint_as_float(v & 0xffff0000u); }
DI u32 pkbf(float lo, float hi) { f32x2 v = {lo, hi}; bf16x2 b = __builtin_convertvector(v, bf16x2); return __builtin_bit_cast(u32, b); }
DI u32 pkh(float lo, float hi) { f32x2 v = {lo, hi}; f16x2 b = __builtin_convertvector(v, f16x2); return __builtin_bit_cast(u32, b); }
DI float rbf(float f) { return bflo(pkbf(f, f) & 0xffffu); }
DI __amdgpu_buffer_rsrc_t mkrs(const void* p, u32 bytes) { return __builtin_amdgcn_make_buffer_rsrc((void*)p, 0, bytes, 0x00020000); }
DI void st128(u32x4 v, __amdgpu_buffer_rsrc_t rs, u32 off) { asm volatile("" : "+v"(off)); __builtin_amdgcn_raw_buffer_store_b128(v, rs, off, 0, 16); }
template <int CTRL> DI float dppf(float v) { return __builtin_bit_cast(float, __builtin_amdgcn_update_dpp(0, __builtin_bit_cast(int, v), CTRL, 0xf, 0xf, true)); }
DI float row16_sum(float v) { v += dppf<0x128>(v); v += dppf<0x124>(v); v += dppf<0x122>(v); v += dppf<0x121>(v); return v; }
DI float row16_max(float v) { v = fmaxf(v, dppf<0x128>(v)); v = fmaxf(v, dppf<0x124>(v)); v = fmaxf(v, dppf<0x122>(v)); v = fmaxf(v, dppf<0x121>(v)); return v; }
DI u32x2 tr_read(u32 addr) { return __builtin_bit_cast(u32x2, __builtin_amdgcn_ds_read_tr16_b64_v4i16((LAS s16x4*)addr)); }
#define CODE_PREFETCH(BEGIN, END, LANE64) do { \
    u32 lo_, hi_, n_; \
    asm volatile("s_getpc_b64 vcc\n.Lpf%=:\n\ts_add_u32 %0, vcc_lo, " BEGIN "-.Lpf%=\n\ts_addc_u32 %1, vcc_hi, 0\n\ts_mov_b32 %2, " END "-" BEGIN : "=s"(lo_), "=s"(hi_), "=s"(n_) :: "vcc", "scc"); \
    const __amdgpu_buffer_rsrc_t rs_ = mkrs((const void*)(((unsigned long long)hi_ << 32) | lo_), n_ & ~3u); \
    u32 x_ = 0; \
    for (u32 o_ = (LANE64) * 128; o_ < n_ + 8191; o_ += 8192 * 4) { \
      x_ ^= __builtin_amdgcn_raw_buffer_load_b32(rs_, o_, 0, 0) ^ __builtin_amdgcn_raw_buffer_load_b32(rs_, o_, 8192, 0) ^ \
            __builtin_amdgcn_raw_buffer_load_b32(rs_, o_, 16384, 0) ^ __builtin_amdgcn_raw_buffer_load_b32(rs_, o_, 24576, 0); } \
    asm volatile("" :: "v"(x_)); \
  } while (0)
#define STAMP_PARAM
#define STAMP_ARG
#define STAMP_DECL do {} while (0)
#define STAMP_CLK(ws, k, slot) do {} while (0)
#define STAMP_AT(ws, k, slot) do {} while (0)
#define STAMP_FLUSH(ws, k) do {} while (0)

typedef f32x2 Cx;
DI Cx cmul(Cx a, Cx b) {
  Cx t, o;
  asm("v_pk_mul_f32 %0, %1, %2 op_sel:[1,1] op_sel_hi:[1,0] neg_lo:[1,0]" : "=v"(t) : "v"(a), "v"(b));
  asm("v_pk_fma_f32 %0, %1, %2, %3 op_sel:[0,0,0] op_sel_hi:[0,1,1]" : "=v"(o) : "v"(a), "v"(b), "v"(t));
  return o;
}
DI Cx cexp_p(float zr2, float zi2) {
  float e = __builtin_amdgcn_exp2f(zr2), t = __builtin_amdgcn_fractf(zi2);
  Cx o = {__builtin_amdgcn_cosf(t), __builtin_amdgcn_sinf(t)}; return o * e;
}
DI u32 pkc(Cx v) { return pkh(v.x, v.y); }
DI float dot2bf(u32 a, u32 b, float c) { return __builtin_amdgcn_fdot2_f32_bf16(__builtin_bit_cast(bf16x2, a), __builtin_bit_cast(bf16x2, b), c, false); }
DI float fexp(float x) { return __builtin_amdgcn_exp2f(x * 1.4426950408889634f); }
DI float rdlane(float v, int l) { return __builtin_bit_cast(float, __builtin_amdgcn_readlane(__builtin_bit_cast(int, v), l)); }
DI u32 perm(u32 hi, u32 lo, u32 sel) { return __builtin_amdgcn_perm(hi, lo, sel); }
typedef __bf16 bf16x8 __attribute__((ext_vector_type(8)));
typedef __attribute__((address_space(1))) u32 gu32;
DI u32 atom_add(char* p, u32 v) { return __hip_atomic_fetch_add((gu32*)p, v, __ATOMIC_RELAXED, __HIP_MEMORY_SCOPE_AGENT); }
DI u32 atom_read(char* p) { u32 z = 0; asm volatile("" : "+v"(z)); return atom_add(p, z); }
#define BAR() asm volatile("s_waitcnt lgkmcnt(0)\n\ts_barrier" ::: "memory")
#define DRAIN() asm volatile("s_waitcnt vmcnt(0)" ::: "memory")
constexpr int SPIN = 1 << 17;
#define KSLEEP 0
#define UREP 16
#define NSH 8
#define USP 128
constexpr u32 UCNT = 256 / NSH;
#define UEARLY 0
#define CBMAP 1
constexpr bool CSF = true;
#define CSV 2
#define TOUCH 8
#define UPAUSE 8
template <int PAUSE> DI void wait_counters(char* p, int lane) {
  const int n = NSH; const u32 want = UCNT;
  for (int it = 0; it < SPIN; it++) {
    u32 v = want;
    if (lane < n) v = atom_read(p + USP * lane);
    if (__all(v == want)) break;
    if (PAUSE) __builtin_amdgcn_s_sleep(PAUSE);
  }
  asm volatile("" ::: "memory");
}
DI int wd_a(int ks) { const int q = ks / 36, s = ks - 36 * q; return 1152 * q + (s < 32 ? 144 * ((s >> 1) & 7) + 64 * (s & 1) + 32 * (s >> 4) : 288 * (s - 32) + 128); }
DI int wd_gap(int ks) { return ks % 36 < 32 ? 0 : 128; }

constexpr int CV_C = 0, CV_CSTR = 4160, CV_U = 12 * CV_CSTR, CV_SM = 65536;
constexpr int KG_ROW = 544, KG_T = 63744, KG_CH = 57 * KG_ROW, KG_C = KG_T + 3 * KG_CH, KG_S = KG_C + 3 * 1312;
constexpr int PK_0 = 16384, PK_ROW = 1184, PK_Q = PK_0 + 64 * PK_ROW, PK_QROW = 352;

#define CV_LO(o) (*(const u32x2*)(pa + (o)))
#define CV_HI(o) (*(const LAS u32x2*)(pa_hi + (o)))
#define CV_B(o) (*(const f16x8*)(pb + 3 * (o)))
#define CV_M(t, l, h, b) acc[t] = __builtin_amdgcn_mfma_f32_16x16x32_f16(__builtin_bit_cast(f16x8, (u32x4){l.x, l.y, h.x, h.y}), b, acc[t], 0, 0, 0);
#define CV_SB __builtin_amdgcn_sched_barrier(0);
#define CV_BAR asm volatile("s_barrier" ::: "memory");
DI void conv_hi(const char* pa, u32 pa_hi, const char* pb, f32x4 (&acc)[8]) {
  const f16x8 B0 = CV_B(0); CV_SB const u32x2 L39 = CV_LO(1536); CV_SB const u32x2 H39 = CV_HI(1536); CV_SB const u32x2 L47 = CV_LO(1024); CV_SB const u32x2 H47 = CV_HI(1024); CV_SB const u32x2 L55 = CV_LO(512); CV_SB const u32x2 H55 = CV_HI(512); CV_SB const u32x2 L63 = CV_LO(0); CV_SB const u32x2 H63 = CV_HI(0); CV_SB
  const f16x8 B1 = CV_B(64); CV_SB const u32x2 L38 = CV_LO(1600); CV_SB const u32x2 H38 = CV_HI(1600); CV_SB const u32x2 L46 = CV_LO(1088); CV_SB const u32x2 H46 = CV_HI(1088); CV_SB const u32x2 L54 = CV_LO(576); CV_SB const u32x2 H54 = CV_HI(576); CV_SB const u32x2 L62 = CV_LO(64); CV_SB const u32x2 H62 = CV_HI(64); CV_SB
  const f16x8 B2 = CV_B(128); CV_SB const u32x2 L37 = CV_LO(1664); CV_SB const u32x2 H37 = CV_HI(1664); CV_SB const u32x2 L45 = CV_LO(1152); CV_SB const u32x2 H45 = CV_HI(1152); CV_SB const u32x2 L53 = CV_LO(640); CV_SB const u32x2 H53 = CV_HI(640); CV_SB const u32x2 L61 = CV_LO(128); CV_SB const u32x2 H61 = CV_HI(128); CV_SB
  CV_M(4, L39, H39, B0) const f16x8 B3 = CV_B(192); const u32x2 L36 = CV_LO(1728); const u32x2 H36 = CV_HI(1728); CV_SB
  CV_M(5, L47, H47, B0) const u32x2 L44 = CV_LO(1216); const u32x2 H44 = CV_HI(1216); CV_SB
  CV_M(6, L55, H55, B0) const u32x2 L52 = CV_LO(704); const u32x2 H52 = CV_HI(704); CV_SB
  CV_M(7, L63, H63, B0) const u32x2 L60 = CV_LO(192); const u32x2 H60 = CV_HI(192); CV_SB
  CV_M(4, L38, H38, B1) const f16x8 B4 = CV_B(256); const u32x2 L35 = CV_LO(1792); const u32x2 H35 = CV_HI(1792); CV_SB
  CV_M(5, L46, H46, B1) const u32x2 L43 = CV_LO(1280); const u32x2 H43 = CV_HI(1280); CV_SB
  CV_M(6, L54, H54, B1) const u32x2 L51 = CV_LO(768); const u32x2 H51 = CV_HI(768); CV_SB
  CV_M(7, L62, H62, B1) const u32x2 L59 = CV_LO(256); const u32x2 H59 = CV_HI(256); CV_SB
  CV_M(4, L37, H37, B2) const f16x8 B5 = CV_B(320); const u32x2 L34 = CV_LO(1856); const u32x2 H34 = CV_HI(1856); CV_SB
  CV_M(5, L45, H45, B2) const u32x2 L42 = CV_LO(1344); const u32x2 H42 = CV_HI(1344); CV_SB
  CV_M(6, L53, H53, B2) const u32x2 L50 = CV_LO(832); const u32x2 H50 = CV_HI(832); CV_SB
  CV_M(7, L61, H61, B2) const u32x2 L58 = CV_LO(320); const u32x2 H58 = CV_HI(320); CV_SB
  CV_M(4, L36, H36, B3) const f16x8 B6 = CV_B(384); const u32x2 L33 = CV_LO(1920); const u32x2 H33 = CV_HI(1920); CV_SB
  CV_M(5, L44, H44, B3) const u32x2 L41 = CV_LO(1408); const u32x2 H41 = CV_HI(1408); CV_SB
  CV_M(6, L52, H52, B3) const u32x2 L49 = CV_LO(896); const u32x2 H49 = CV_HI(896); CV_SB
  CV_M(7, L60, H60, B3) const u32x2 L57 = CV_LO(384); const u32x2 H57 = CV_HI(384); CV_SB
  CV_M(4, L35, H35, B4) const f16x8 B7 = CV_B(448); const u32x2 L32 = CV_LO(1984); const u32x2 H32 = CV_HI(1984); CV_SB
  CV_M(5, L43, H43, B4) const u32x2 L40 = CV_LO(1472); const u32x2 H40 = CV_HI(1472); CV_SB
  CV_M(6, L51, H51, B4) const u32x2 L48 = CV_LO(960); const u32x2 H48 = CV_HI(960); CV_SB
  CV_M(7, L59, H59, B4) const u32x2 L56 = CV_LO(448); const u32x2 H56 = CV_HI(448); CV_SB
  CV_M(4, L34, H34, B5) const f16x8 B8 = CV_B(512); CV_SB
  CV_M(5, L42, H42, B5) const u32x2 L31 = CV_LO(2048); CV_SB
  CV_M(6, L50, H50, B5) const u32x2 H31 = CV_HI(2048); CV_SB
  CV_M(7, L58, H58, B5)  CV_SB
  CV_M(4, L33, H33, B6) const f16x8 B9 = CV_B(576); CV_SB
  CV_M(5, L41, H41, B6) const u32x2 L30 = CV_LO(2112); CV_SB
  CV_M(6, L49, H49, B6) const u32x2 H30 = CV_HI(2112); CV_SB
  CV_M(7, L57, H57, B6)  CV_SB
  CV_M(4, L32, H32, B7) const f16x8 B10 = CV_B(640); CV_SB
  CV_M(5, L40, H40, B7) const u32x2 L29 = CV_LO(2176); CV_SB
  CV_M(6, L48, H48, B7) const u32x2 H29 = CV_HI(2176); CV_SB
  CV_M(7, L56, H56, B7)  CV_SB
  CV_M(4, L31, H31, B8) const f16x8 B11 = CV_B(704); CV_SB
  CV_M(5, L39, H39, B8) const u32x2 L28 = CV_LO(2240); CV_SB
  CV_M(6, L47, H47, B8) const u32x2 H28 = CV_HI(2240); CV_SB
  CV_M(7, L55, H55, B8)  CV_SB
  CV_M(4, L30, H30, B9) const f16x8 B12 = CV_B(768); CV_SB
  CV_M(5, L38, H38, B9) const u32x2 L27 = CV_LO(2304); CV_SB
  CV_M(6, L46, H46, B9) const u32x2 H27 = CV_HI(2304); CV_SB
  CV_M(7, L54, H54, B9)  CV_SB
  CV_M(4, L29, H29, B10) const f16x8 B13 = CV_B(832); CV_SB
  CV_M(5, L37, H37, B10) const u32x2 L26 = CV_LO(2368); CV_SB
  CV_M(6, L45, H45, B10) const u32x2 H26 = CV_HI(2368); CV_SB
  CV_M(7, L53, H53, B10)  CV_SB
  CV_M(4, L28, H28, B11) const f16x8 B14 = CV_B(896); CV_SB
  CV_M(5, L36, H36, B11) const u32x2 L25 = CV_LO(2432); CV_SB
  CV_M(6, L44, H44, B11) const u32x2 H25 = CV_HI(2432); CV_SB
  CV_M(7, L52, H52, B11)  CV_SB
  CV_M(4, L27, H27, B12) const f16x8 B15 = CV_B(960); CV_SB
  CV_M(5, L35, H35, B12) const u32x2 L24 = CV_LO(2496); CV_SB
  CV_M(6, L43, H43, B12) const u32x2 H24 = CV_HI(2496); CV_SB
  CV_M(7, L51, H51, B12)  CV_SB
  CV_BAR
  CV_M(4, L26, H26, B13) const f16x8 B16 = CV_B(1024); CV_SB
  CV_M(5, L34, H34, B13) const u32x2 L23 = CV_LO(2560); CV_SB
  CV_M(6, L42, H42, B13) const u32x2 H23 = CV_HI(2560); CV_SB
  CV_M(7, L50, H50, B13)  CV_SB
  CV_M(4, L25, H25, B14) const f16x8 B17 = CV_B(1088); CV_SB
  CV_M(5, L33, H33, B14) const u32x2 L22 = CV_LO(2624); CV_SB
  CV_M(6, L41, H41, B14) const u32x2 H22 = CV_HI(2624); CV_SB
  CV_M(7, L49, H49, B14)  CV_SB
  CV_M(4, L24, H24, B15) const f16x8 B18 = CV_B(1152); CV_SB
  CV_M(5, L32, H32, B15) const u32x2 L21 = CV_LO(2688); CV_SB
  CV_M(6, L40, H40, B15) const u32x2 H21 = CV_HI(2688); CV_SB
  CV_M(7, L48, H48, B15)  CV_SB
  CV_M(4, L23, H23, B16) const f16x8 B19 = CV_B(1216); CV_SB
  CV_M(5, L31, H31, B16) const u32x2 L20 = CV_LO(2752); CV_SB
  CV_M(6, L39, H39, B16) const u32x2 H20 = CV_HI(2752); CV_SB
  CV_M(7, L47, H47, B16)  CV_SB
  CV_M(4, L22, H22, B17) const f16x8 B20 = CV_B(1280); CV_SB
  CV_M(5, L30, H30, B17) const u32x2 L19 = CV_LO(2816); CV_SB
  CV_M(6, L38, H38, B17) const u32x2 H19 = CV_HI(2816); CV_SB
  CV_M(7, L46, H46, B17)  CV_SB
  CV_M(4, L21, H21, B18) const f16x8 B21 = CV_B(1344); CV_SB
  CV_M(5, L29, H29, B18) const u32x2 L18 = CV_LO(2880); CV_SB
  CV_M(6, L37, H37, B18) const u32x2 H18 = CV_HI(2880); CV_SB
  CV_M(7, L45, H45, B18)  CV_SB
  CV_M(4, L20, H20, B19) const f16x8 B22 = CV_B(1408); CV_SB
  CV_M(5, L28, H28, B19) const u32x2 L17 = CV_LO(2944); CV_SB
  CV_M(6, L36, H36, B19) const u32x2 H17 = CV_HI(2944); CV_SB
  CV_M(7, L44, H44, B19)  CV_SB
  CV_M(4, L19, H19, B20) const f16x8 B23 = CV_B(1472); CV_SB
  CV_M(5, L27, H27, B20) const u32x2 L16 = CV_LO(3008); CV_SB
  CV_M(6, L35, H35, B20) const u32x2 H16 = CV_HI(3008); CV_SB
  CV_M(7, L43, H43, B20)  CV_SB
  CV_M(4, L18, H18, B21) const f16x8 B24 = CV_B(1536); CV_SB
  CV_M(5, L26, H26, B21) const u32x2 L15 = CV_LO(3072); CV_SB
  CV_M(6, L34, H34, B21) const u32x2 H15 = CV_HI(3072); CV_SB
  CV_M(7, L42, H42, B21)  CV_SB
  CV_M(4, L17, H17, B22) const f16x8 B25 = CV_B(1600); CV_SB
  CV_M(5, L25, H25, B22) const u32x2 L14 = CV_LO(3136); CV_SB
  CV_M(6, L33, H33, B22) const u32x2 H14 = CV_HI(3136); CV_SB
  CV_M(7, L41, H41, B22)  CV_SB
  CV_M(4, L16, H16, B23) const f16x8 B26 = CV_B(1664); CV_SB
  CV_M(5, L24, H24, B23) const u32x2 L13 = CV_LO(3200); CV_SB
  CV_M(6, L32, H32, B23) const u32x2 H13 = CV_HI(3200); CV_SB
  CV_M(7, L40, H40, B23)  CV_SB
  CV_M(4, L15, H15, B24) const f16x8 B27 = CV_B(1728); CV_SB
  CV_M(5, L23, H23, B24) const u32x2 L12 = CV_LO(3264); CV_SB
  CV_M(6, L31, H31, B24) const u32x2 H12 = CV_HI(3264); CV_SB
  CV_M(7, L39, H39, B24)  CV_SB
  CV_M(4, L14, H14, B25) const f16x8 B28 = CV_B(1792); CV_SB
  CV_M(5, L22, H22, B25) const u32x2 L11 = CV_LO(3328); CV_SB
  CV_M(6, L30, H30, B25) const u32x2 H11 = CV_HI(3328); CV_SB
  CV_M(7, L38, H38, B25)  CV_SB
  CV_M(4, L13, H13, B26) const f16x8 B29 = CV_B(1856); CV_SB
  CV_M(5, L21, H21, B26) const u32x2 L10 = CV_LO(3392); CV_SB
  CV_M(6, L29, H29, B26) const u32x2 H10 = CV_HI(3392); CV_SB
  CV_M(7, L37, H37, B26)  CV_SB
  CV_M(4, L12, H12, B27) const f16x8 B30 = CV_B(1920); CV_SB
  CV_M(5, L20, H20, B27) const u32x2 L9 = CV_LO(3456); CV_SB
  CV_M(6, L28, H28, B27) const u32x2 H9 = CV_HI(3456); CV_SB
  CV_M(7, L36, H36, B27)  CV_SB
  CV_M(4, L11, H11, B28) const f16x8 B31 = CV_B(1984); CV_SB
  CV_M(5, L19, H19, B28) const u32x2 L8 = CV_LO(3520); CV_SB
  CV_M(6, L27, H27, B28) const u32x2 H8 = CV_HI(3520); CV_SB
  CV_M(7, L35, H35, B28)  CV_SB
  CV_M(4, L10, H10, B29) const f16x8 B32 = CV_B(2048); CV_SB
  CV_M(5, L18, H18, B29) const u32x2 L7 = CV_LO(3584); CV_SB
  CV_M(6, L26, H26, B29) const u32x2 H7 = CV_HI(3584); CV_SB
  CV_M(7, L34, H34, B29)  CV_SB
  CV_M(4, L9, H9, B30) const f16x8 B33 = CV_B(2112); CV_SB
  CV_M(5, L17, H17, B30) const u32x2 L6 = CV_LO(3648); CV_SB
  CV_M(6, L25, H25, B30) const u32x2 H6 = CV_HI(3648); CV_SB
  CV_M(7, L33, H33, B30)  CV_SB
  CV_M(4, L8, H8, B31) const f16x8 B34 = CV_B(2176); CV_SB
  CV_M(5, L16, H16, B31) const u32x2 L5 = CV_LO(3712); CV_SB
  CV_M(6, L24, H24, B31) const u32x2 H5 = CV_HI(3712); CV_SB
  CV_M(7, L32, H32, B31)  CV_SB
  CV_M(4, L7, H7, B32) const f16x8 B35 = CV_B(2240); CV_SB
  CV_M(5, L15, H15, B32) const u32x2 L4 = CV_LO(3776); CV_SB
  CV_M(6, L23, H23, B32) const u32x2 H4 = CV_HI(3776); CV_SB
  CV_M(7, L31, H31, B32)  CV_SB
  CV_M(4, L6, H6, B33) const f16x8 B36 = CV_B(2304); CV_SB
  CV_M(5, L14, H14, B33) const u32x2 L3 = CV_LO(3840); CV_SB
  CV_M(6, L22, H22, B33) const u32x2 H3 = CV_HI(3840); CV_SB
  CV_M(7, L30, H30, B33)  CV_SB
  CV_BAR
  CV_M(4, L5, H5, B34) const f16x8 B37 = CV_B(2368); CV_SB
  CV_M(5, L13, H13, B34) const u32x2 L2 = CV_LO(3904); CV_SB
  CV_M(6, L21, H21, B34) const u32x2 H2 = CV_HI(3904); CV_SB
  CV_M(7, L29, H29, B34)  CV_SB
  CV_M(4, L4, H4, B35) const f16x8 B38 = CV_B(2432); CV_SB
  CV_M(5, L12, H12, B35) const u32x2 L1 = CV_LO(3968); CV_SB
  CV_M(6, L20, H20, B35) const u32x2 H1 = CV_HI(3968); CV_SB
  CV_M(7, L28, H28, B35)  CV_SB
  CV_M(4, L3, H3, B36) const f16x8 B39 = CV_B(2496); CV_SB
  CV_M(5, L11, H11, B36) const u32x2 L0 = CV_LO(4032); CV_SB
  CV_M(6, L19, H19, B36) const u32x2 H0 = CV_HI(4032); CV_SB
  CV_M(7, L27, H27, B36)  CV_SB
  CV_M(4, L2, H2, B37) const f16x8 B40 = CV_B(2560); CV_SB
  CV_M(5, L10, H10, B37)  CV_SB
  CV_M(6, L18, H18, B37)  CV_SB
  CV_M(7, L26, H26, B37)  CV_SB
  CV_M(4, L1, H1, B38) const f16x8 B41 = CV_B(2624); CV_SB
  CV_M(5, L9, H9, B38)  CV_SB
  CV_M(6, L17, H17, B38)  CV_SB
  CV_M(7, L25, H25, B38)  CV_SB
  CV_M(4, L0, H0, B39) const f16x8 B42 = CV_B(2688); CV_SB
  CV_M(5, L8, H8, B39)  CV_SB
  CV_M(6, L16, H16, B39)  CV_SB
  CV_M(7, L24, H24, B39)  CV_SB
  CV_M(5, L7, H7, B40) const f16x8 B43 = CV_B(2752); CV_SB
  CV_M(6, L15, H15, B40)  CV_SB
  CV_M(7, L23, H23, B40)  CV_SB
  CV_M(5, L6, H6, B41) const f16x8 B44 = CV_B(2816); CV_SB
  CV_M(6, L14, H14, B41)  CV_SB
  CV_M(7, L22, H22, B41)  CV_SB
  CV_M(5, L5, H5, B42) const f16x8 B45 = CV_B(2880); CV_SB
  CV_M(6, L13, H13, B42)  CV_SB
  CV_M(7, L21, H21, B42)  CV_SB
  CV_M(5, L4, H4, B43) const f16x8 B46 = CV_B(2944); CV_SB
  CV_M(6, L12, H12, B43)  CV_SB
  CV_M(7, L20, H20, B43)  CV_SB
  CV_M(5, L3, H3, B44) const f16x8 B47 = CV_B(3008); CV_SB
  CV_M(6, L11, H11, B44)  CV_SB
  CV_M(7, L19, H19, B44)  CV_SB
  CV_M(5, L2, H2, B45) const f16x8 B48 = CV_B(3072); CV_SB
  CV_M(6, L10, H10, B45)  CV_SB
  CV_M(7, L18, H18, B45)  CV_SB
  CV_M(5, L1, H1, B46) const f16x8 B49 = CV_B(3136); CV_SB
  CV_M(6, L9, H9, B46)  CV_SB
  CV_M(7, L17, H17, B46)  CV_SB
  CV_M(5, L0, H0, B47) const f16x8 B50 = CV_B(3200); CV_SB
  CV_M(6, L8, H8, B47)  CV_SB
  CV_M(7, L16, H16, B47)  CV_SB
  CV_M(6, L7, H7, B48) const f16x8 B51 = CV_B(3264); CV_SB
  CV_M(7, L15, H15, B48)  CV_SB
  CV_M(6, L6, H6, B49) const f16x8 B52 = CV_B(3328); CV_SB
  CV_M(7, L14, H14, B49)  CV_SB
  CV_M(6, L5, H5, B50) const f16x8 B53 = CV_B(3392); CV_SB
  CV_M(7, L13, H13, B50)  CV_SB
  CV_M(6, L4, H4, B51) const f16x8 B54 = CV_B(3456); CV_SB
  CV_M(7, L12, H12, B51)  CV_SB
  CV_M(6, L3, H3, B52) const f16x8 B55 = CV_B(3520); CV_SB
  CV_M(7, L11, H11, B52)  CV_SB
  CV_M(6, L2, H2, B53) const f16x8 B56 = CV_B(3584); CV_SB
  CV_M(7, L10, H10, B53)  CV_SB
  CV_M(6, L1, H1, B54) const f16x8 B57 = CV_B(3648); CV_SB
  CV_M(7, L9, H9, B54)  CV_SB
  CV_M(6, L0, H0, B55) const f16x8 B58 = CV_B(3712); CV_SB
  CV_M(7, L8, H8, B55)  CV_SB
  CV_M(7, L7, H7, B56) const f16x8 B59 = CV_B(3776); CV_SB
  CV_M(7, L6, H6, B57) const f16x8 B60 = CV_B(3840); CV_SB
  CV_M(7, L5, H5, B58) const f16x8 B61 = CV_B(3904); CV_SB
  CV_M(7, L4, H4, B59) const f16x8 B62 = CV_B(3968); CV_SB
  CV_M(7, L3, H3, B60) const f16x8 B63 = CV_B(4032); CV_SB
  CV_M(7, L2, H2, B61)  CV_SB
  CV_M(7, L1, H1, B62)  CV_SB
  CV_M(7, L0, H0, B63)  CV_SB
}
DI void conv_mid(const char* pa, u32 pa_hi, const char* pb, f32x4 (&acc)[8]) {
  const f16x8 B0 = CV_B(0); CV_SB const u32x2 L23 = CV_LO(2560); CV_SB const u32x2 H23 = CV_HI(2560); CV_SB const u32x2 L31 = CV_LO(2048); CV_SB const u32x2 H31 = CV_HI(2048); CV_SB
  const f16x8 B1 = CV_B(64); CV_SB const u32x2 L22 = CV_LO(2624); CV_SB const u32x2 H22 = CV_HI(2624); CV_SB const u32x2 L30 = CV_LO(2112); CV_SB const u32x2 H30 = CV_HI(2112); CV_SB
  const f16x8 B2 = CV_B(128); CV_SB const u32x2 L21 = CV_LO(2688); CV_SB const u32x2 H21 = CV_HI(2688); CV_SB const u32x2 L29 = CV_LO(2176); CV_SB const u32x2 H29 = CV_HI(2176); CV_SB
  CV_M(2, L23, H23, B0) const f16x8 B3 = CV_B(192); const u32x2 L20 = CV_LO(2752); const u32x2 H20 = CV_HI(2752); CV_SB
  CV_M(3, L31, H31, B0) const u32x2 L28 = CV_LO(2240); const u32x2 H28 = CV_HI(2240); CV_SB
  CV_M(2, L22, H22, B1) const f16x8 B4 = CV_B(256); const u32x2 L19 = CV_LO(2816); const u32x2 H19 = CV_HI(2816); CV_SB
  CV_M(3, L30, H30, B1) const u32x2 L27 = CV_LO(2304); const u32x2 H27 = CV_HI(2304); CV_SB
  CV_M(2, L21, H21, B2) const f16x8 B5 = CV_B(320); const u32x2 L18 = CV_LO(2880); const u32x2 H18 = CV_HI(2880); CV_SB
  CV_M(3, L29, H29, B2) const u32x2 L26 = CV_LO(2368); const u32x2 H26 = CV_HI(2368); CV_SB
  CV_M(2, L20, H20, B3) const f16x8 B6 = CV_B(384); const u32x2 L17 = CV_LO(2944); const u32x2 H17 = CV_HI(2944); CV_SB
  CV_M(3, L28, H28, B3) const u32x2 L25 = CV_LO(2432); const u32x2 H25 = CV_HI(2432); CV_SB
  CV_M(2, L19, H19, B4) const f16x8 B7 = CV_B(448); const u32x2 L16 = CV_LO(3008); const u32x2 H16 = CV_HI(3008); CV_SB
  CV_M(3, L27, H27, B4) const u32x2 L24 = CV_LO(2496); const u32x2 H24 = CV_HI(2496); CV_SB
  CV_M(2, L18, H18, B5) const f16x8 B8 = CV_B(512); const u32x2 L15 = CV_LO(3072); CV_SB
  CV_M(3, L26, H26, B5) const u32x2 H15 = CV_HI(3072); CV_SB
  CV_M(2, L17, H17, B6) const f16x8 B9 = CV_B(576); const u32x2 L14 = CV_LO(3136); CV_SB
  CV_M(3, L25, H25, B6) const u32x2 H14 = CV_HI(3136); CV_SB
  CV_M(2, L16, H16, B7) const f16x8 B10 = CV_B(640); const u32x2 L13 = CV_LO(3200); CV_SB
  CV_M(3, L24, H24, B7) const u32x2 H13 = CV_HI(3200); CV_SB
  CV_M(2, L15, H15, B8) const f16x8 B11 = CV_B(704); const u32x2 L12 = CV_LO(3264); CV_SB
  CV_M(3, L23, H23, B8) const u32x2 H12 = CV_HI(3264); CV_SB
  CV_M(2, L14, H14, B9) const f16x8 B12 = CV_B(768); const u32x2 L11 = CV_LO(3328); CV_SB
  CV_M(3, L22, H22, B9) const u32x2 H11 = CV_HI(3328); CV_SB
  CV_M(2, L13, H13, B10) const f16x8 B13 = CV_B(832); const u32x2 L10 = CV_LO(3392); CV_SB
  CV_M(3, L21, H21, B10) const u32x2 H10 = CV_HI(3392); CV_SB
  CV_M(2, L12, H12, B11) const f16x8 B14 = CV_B(896); const u32x2 L9 = CV_LO(3456); CV_SB
  CV_M(3, L20, H20, B11) const u32x2 H9 = CV_HI(3456); CV_SB
  CV_M(2, L11, H11, B12) const f16x8 B15 = CV_B(960); const u32x2 L8 = CV_LO(3520); CV_SB
  CV_M(3, L19, H19, B12) const u32x2 H8 = CV_HI(3520); CV_SB
  CV_BAR
  CV_M(2, L10, H10, B13) const f16x8 B16 = CV_B(1024); const u32x2 L7 = CV_LO(3584); CV_SB
  CV_M(3, L18, H18, B13) const u32x2 H7 = CV_HI(3584); CV_SB
  CV_M(2, L9, H9, B14) const f16x8 B17 = CV_B(1088); const u32x2 L6 = CV_LO(3648); CV_SB
  CV_M(3, L17, H17, B14) const u32x2 H6 = CV_HI(3648); CV_SB
  CV_M(2, L8, H8, B15) const f16x8 B18 = CV_B(1152); const u32x2 L5 = CV_LO(3712); CV_SB
  CV_M(3, L16, H16, B15) const u32x2 H5 = CV_HI(3712); CV_SB
  CV_M(2, L7, H7, B16) const f16x8 B19 = CV_B(1216); const u32x2 L4 = CV_LO(3776); CV_SB
  CV_M(3, L15, H15, B16) const u32x2 H4 = CV_HI(3776); CV_SB
  CV_M(2, L6, H6, B17) const f16x8 B20 = CV_B(1280); const u32x2 L3 = CV_LO(3840); CV_SB
  CV_M(3, L14, H14, B17) const u32x2 H3 = CV_HI(3840); CV_SB
  CV_M(2, L5, H5, B18) const f16x8 B21 = CV_B(1344); const u32x2 L2 = CV_LO(3904); CV_SB
  CV_M(3, L13, H13, B18) const u32x2 H2 = CV_HI(3904); CV_SB
  CV_M(2, L4, H4, B19) const f16x8 B22 = CV_B(1408); const u32x2 L1 = CV_LO(3968); CV_SB
  CV_M(3, L12, H12, B19) const u32x2 H1 = CV_HI(3968); CV_SB
  CV_M(2, L3, H3, B20) const f16x8 B23 = CV_B(1472); const u32x2 L0 = CV_LO(4032); CV_SB
  CV_M(3, L11, H11, B20) const u32x2 H0 = CV_HI(4032); CV_SB
  CV_M(2, L2, H2, B21) const f16x8 B24 = CV_B(1536); CV_SB
  CV_M(3, L10, H10, B21)  CV_SB
  CV_M(2, L1, H1, B22) const f16x8 B25 = CV_B(1600); CV_SB
  CV_M(3, L9, H9, B22)  CV_SB
  CV_M(2, L0, H0, B23) const f16x8 B26 = CV_B(1664); CV_SB
  CV_M(3, L8, H8, B23)  CV_SB
  CV_M(3, L7, H7, B24) const f16x8 B27 = CV_B(1728); CV_SB
  CV_M(3, L6, H6, B25) const f16x8 B28 = CV_B(1792); CV_SB
  CV_M(3, L5, H5, B26) const f16x8 B29 = CV_B(1856); CV_SB
  CV_M(3, L4, H4, B27) const f16x8 B30 = CV_B(1920); CV_SB
  CV_M(3, L3, H3, B28) const f16x8 B31 = CV_B(1984); CV_SB
  CV_M(3, L2, H2, B29)  CV_SB
  CV_M(3, L1, H1, B30)  CV_SB
  CV_BAR
  CV_M(3, L0, H0, B31)  CV_SB
}
DI void conv_lo(const char* pa, u32 pa_hi, const char* pb, f32x4 (&acc)[8]) {
  const f16x8 B0 = CV_B(0); CV_SB const u32x2 L7 = CV_LO(3584); CV_SB const u32x2 H7 = CV_HI(3584); CV_SB const u32x2 L15 = CV_LO(3072); CV_SB const u32x2 H15 = CV_HI(3072); CV_SB
  const f16x8 B1 = CV_B(64); CV_SB const u32x2 L6 = CV_LO(3648); CV_SB const u32x2 H6 = CV_HI(3648); CV_SB const u32x2 L14 = CV_LO(3136); CV_SB const u32x2 H14 = CV_HI(3136); CV_SB
  const f16x8 B2 = CV_B(128); CV_SB const u32x2 L5 = CV_LO(3712); CV_SB const u32x2 H5 = CV_HI(3712); CV_SB const u32x2 L13 = CV_LO(3200); CV_SB const u32x2 H13 = CV_HI(3200); CV_SB
  const f16x8 B3 = CV_B(192); CV_SB const u32x2 L4 = CV_LO(3776); CV_SB const u32x2 H4 = CV_HI(3776); CV_SB const u32x2 L12 = CV_LO(3264); CV_SB const u32x2 H12 = CV_HI(3264); CV_SB
  CV_M(0, L7, H7, B0) const f16x8 B4 = CV_B(256); const u32x2 L3 = CV_LO(3840); const u32x2 H3 = CV_HI(3840); CV_SB
  CV_M(1, L15, H15, B0) const u32x2 L11 = CV_LO(3328); const u32x2 H11 = CV_HI(3328); CV_SB
  CV_M(0, L6, H6, B1) const f16x8 B5 = CV_B(320); const u32x2 L2 = CV_LO(3904); const u32x2 H2 = CV_HI(3904); CV_SB
  CV_M(1, L14, H14, B1) const u32x2 L10 = CV_LO(3392); const u32x2 H10 = CV_HI(3392); CV_SB
  CV_M(0, L5, H5, B2) const f16x8 B6 = CV_B(384); const u32x2 L1 = CV_LO(3968); const u32x2 H1 = CV_HI(3968); CV_SB
  CV_M(1, L13, H13, B2) const u32x2 L9 = CV_LO(3456); const u32x2 H9 = CV_HI(3456); CV_SB
  CV_M(0, L4, H4, B3) const f16x8 B7 = CV_B(448); const u32x2 L0 = CV_LO(4032); const u32x2 H0 = CV_HI(4032); CV_SB
  CV_M(1, L12, H12, B3) const u32x2 L8 = CV_LO(3520); const u32x2 H8 = CV_HI(3520); CV_SB
  CV_M(0, L3, H3, B4) const f16x8 B8 = CV_B(512); CV_SB
  CV_M(1, L11, H11, B4)  CV_SB
  CV_M(0, L2, H2, B5) const f16x8 B9 = CV_B(576); CV_SB
  CV_M(1, L10, H10, B5)  CV_SB
  CV_M(0, L1, H1, B6) const f16x8 B10 = CV_B(640); CV_SB
  CV_M(1, L9, H9, B6)  CV_SB
  CV_M(0, L0, H0, B7) const f16x8 B11 = CV_B(704); CV_SB
  CV_M(1, L8, H8, B7)  CV_SB
  CV_M(1, L7, H7, B8) const f16x8 B12 = CV_B(768); CV_SB
  CV_M(1, L6, H6, B9) const f16x8 B13 = CV_B(832); CV_SB
  CV_M(1, L5, H5, B10) const f16x8 B14 = CV_B(896); CV_SB
  CV_M(1, L4, H4, B11) const f16x8 B15 = CV_B(960); CV_SB
  CV_M(1, L3, H3, B12)  CV_SB
  CV_M(1, L2, H2, B13)  CV_SB
  CV_M(1, L1, H1, B14)  CV_SB
  CV_M(1, L0, H0, B15)  CV_SB
}
DI void conv_t3(const char* pa, u32 pa_hi, const char* pb, f32x4 (&acc)[8]) {
  const f16x8 B0 = CV_B(0); CV_SB const u32x2 L31 = CV_LO(2048); CV_SB const u32x2 H31 = CV_HI(2048); CV_SB
  const f16x8 B1 = CV_B(64); CV_SB const u32x2 L30 = CV_LO(2112); CV_SB const u32x2 H30 = CV_HI(2112); CV_SB
  const f16x8 B2 = CV_B(128); CV_SB const u32x2 L29 = CV_LO(2176); CV_SB const u32x2 H29 = CV_HI(2176); CV_SB
  const f16x8 B3 = CV_B(192); CV_SB const u32x2 L28 = CV_LO(2240); CV_SB const u32x2 H28 = CV_HI(2240); CV_SB
  CV_M(3, L31, H31, B0) const f16x8 B4 = CV_B(256); const u32x2 L27 = CV_LO(2304); const u32x2 H27 = CV_HI(2304); CV_SB
  CV_M(3, L30, H30, B1) const f16x8 B5 = CV_B(320); const u32x2 L26 = CV_LO(2368); const u32x2 H26 = CV_HI(2368); CV_SB
  CV_M(3, L29, H29, B2) const f16x8 B6 = CV_B(384); const u32x2 L25 = CV_LO(2432); const u32x2 H25 = CV_HI(2432); CV_SB
  CV_M(3, L28, H28, B3) const f16x8 B7 = CV_B(448); const u32x2 L24 = CV_LO(2496); const u32x2 H24 = CV_HI(2496); CV_SB
  CV_M(3, L27, H27, B4) const f16x8 B8 = CV_B(512); const u32x2 L23 = CV_LO(2560); const u32x2 H23 = CV_HI(2560); CV_SB
  CV_M(3, L26, H26, B5) const f16x8 B9 = CV_B(576); const u32x2 L22 = CV_LO(2624); const u32x2 H22 = CV_HI(2624); CV_SB
  CV_M(3, L25, H25, B6) const f16x8 B10 = CV_B(640); const u32x2 L21 = CV_LO(2688); const u32x2 H21 = CV_HI(2688); CV_SB
  CV_M(3, L24, H24, B7) const f16x8 B11 = CV_B(704); const u32x2 L20 = CV_LO(2752); const u32x2 H20 = CV_HI(2752); CV_SB
  CV_M(3, L23, H23, B8) const f16x8 B12 = CV_B(768); const u32x2 L19 = CV_LO(2816); const u32x2 H19 = CV_HI(2816); CV_SB
  CV_M(3, L22, H22, B9) const f16x8 B13 = CV_B(832); const u32x2 L18 = CV_LO(2880); const u32x2 H18 = CV_HI(2880); CV_SB
  CV_M(3, L21, H21, B10) const f16x8 B14 = CV_B(896); const u32x2 L17 = CV_LO(2944); const u32x2 H17 = CV_HI(2944); CV_SB
  CV_M(3, L20, H20, B11) const f16x8 B15 = CV_B(960); const u32x2 L16 = CV_LO(3008); const u32x2 H16 = CV_HI(3008); CV_SB
  CV_M(3, L19, H19, B12) const f16x8 B16 = CV_B(1024); const u32x2 L15 = CV_LO(3072); const u32x2 H15 = CV_HI(3072); CV_SB
  CV_M(3, L18, H18, B13) const f16x8 B17 = CV_B(1088); const u32x2 L14 = CV_LO(3136); const u32x2 H14 = CV_HI(3136); CV_SB
  CV_M(3, L17, H17, B14) const f16x8 B18 = CV_B(1152); const u32x2 L13 = CV_LO(3200); const u32x2 H13 = CV_HI(3200); CV_SB
  CV_M(3, L16, H16, B15) const f16x8 B19 = CV_B(1216); const u32x2 L12 = CV_LO(3264); const u32x2 H12 = CV_HI(3264); CV_SB
  CV_M(3, L15, H15, B16) const f16x8 B20 = CV_B(1280); const u32x2 L11 = CV_LO(3328); const u32x2 H11 = CV_HI(3328); CV_SB
  CV_M(3, L14, H14, B17) const f16x8 B21 = CV_B(1344); const u32x2 L10 = CV_LO(3392); const u32x2 H10 = CV_HI(3392); CV_SB
  CV_M(3, L13, H13, B18) const f16x8 B22 = CV_B(1408); const u32x2 L9 = CV_LO(3456); const u32x2 H9 = CV_HI(3456); CV_SB
  CV_M(3, L12, H12, B19) const f16x8 B23 = CV_B(1472); const u32x2 L8 = CV_LO(3520); const u32x2 H8 = CV_HI(3520); CV_SB
  CV_M(3, L11, H11, B20) const f16x8 B24 = CV_B(1536); const u32x2 L7 = CV_LO(3584); const u32x2 H7 = CV_HI(3584); CV_SB
  CV_M(3, L10, H10, B21) const f16x8 B25 = CV_B(1600); const u32x2 L6 = CV_LO(3648); const u32x2 H6 = CV_HI(3648); CV_SB
  CV_M(3, L9, H9, B22) const f16x8 B26 = CV_B(1664); const u32x2 L5 = CV_LO(3712); const u32x2 H5 = CV_HI(3712); CV_SB
  CV_M(3, L8, H8, B23) const f16x8 B27 = CV_B(1728); const u32x2 L4 = CV_LO(3776); const u32x2 H4 = CV_HI(3776); CV_SB
  CV_M(3, L7, H7, B24) const f16x8 B28 = CV_B(1792); const u32x2 L3 = CV_LO(3840); const u32x2 H3 = CV_HI(3840); CV_SB
  CV_M(3, L6, H6, B25) const f16x8 B29 = CV_B(1856); const u32x2 L2 = CV_LO(3904); const u32x2 H2 = CV_HI(3904); CV_SB
  CV_M(3, L5, H5, B26) const f16x8 B30 = CV_B(1920); const u32x2 L1 = CV_LO(3968); const u32x2 H1 = CV_HI(3968); CV_SB
  CV_M(3, L4, H4, B27) const f16x8 B31 = CV_B(1984); const u32x2 L0 = CV_LO(4032); const u32x2 H0 = CV_HI(4032); CV_SB
  CV_M(3, L3, H3, B28)  CV_SB
  CV_M(3, L2, H2, B29)  CV_SB
  CV_M(3, L1, H1, B30)  CV_SB
  CV_M(3, L0, H0, B31)  CV_SB
}
DI void conv_t21(const char* pa, u32 pa_hi, const char* pb, f32x4 (&acc)[8]) {
  const f16x8 B0 = CV_B(0); CV_SB const u32x2 L23 = CV_LO(2560); CV_SB const u32x2 H23 = CV_HI(2560); CV_SB
  const f16x8 B1 = CV_B(64); CV_SB const u32x2 L22 = CV_LO(2624); CV_SB const u32x2 H22 = CV_HI(2624); CV_SB
  const f16x8 B2 = CV_B(128); CV_SB const u32x2 L21 = CV_LO(2688); CV_SB const u32x2 H21 = CV_HI(2688); CV_SB
  const f16x8 B3 = CV_B(192); CV_SB const u32x2 L20 = CV_LO(2752); CV_SB const u32x2 H20 = CV_HI(2752); CV_SB
  CV_M(2, L23, H23, B0) const f16x8 B4 = CV_B(256); const u32x2 L19 = CV_LO(2816); const u32x2 H19 = CV_HI(2816); CV_SB
  CV_M(2, L22, H22, B1) const f16x8 B5 = CV_B(320); const u32x2 L18 = CV_LO(2880); const u32x2 H18 = CV_HI(2880); CV_SB
  CV_M(2, L21, H21, B2) const f16x8 B6 = CV_B(384); const u32x2 L17 = CV_LO(2944); const u32x2 H17 = CV_HI(2944); CV_SB
  CV_M(2, L20, H20, B3) const f16x8 B7 = CV_B(448); const u32x2 L16 = CV_LO(3008); const u32x2 H16 = CV_HI(3008); CV_SB
  CV_M(2, L19, H19, B4) const f16x8 B8 = CV_B(512); const u32x2 L15 = CV_LO(3072); const u32x2 H15 = CV_HI(3072); CV_SB
  CV_M(2, L18, H18, B5) const f16x8 B9 = CV_B(576); const u32x2 L14 = CV_LO(3136); const u32x2 H14 = CV_HI(3136); CV_SB
  CV_M(2, L17, H17, B6) const f16x8 B10 = CV_B(640); const u32x2 L13 = CV_LO(3200); const u32x2 H13 = CV_HI(3200); CV_SB
  CV_M(2, L16, H16, B7) const f16x8 B11 = CV_B(704); const u32x2 L12 = CV_LO(3264); const u32x2 H12 = CV_HI(3264); CV_SB
  CV_M(2, L15, H15, B8) const f16x8 B12 = CV_B(768); const u32x2 L11 = CV_LO(3328); const u32x2 H11 = CV_HI(3328); CV_SB
  CV_M(2, L14, H14, B9) const f16x8 B13 = CV_B(832); const u32x2 L10 = CV_LO(3392); const u32x2 H10 = CV_HI(3392); CV_SB
  CV_M(2, L13, H13, B10) const f16x8 B14 = CV_B(896); const u32x2 L9 = CV_LO(3456); const u32x2 H9 = CV_HI(3456); CV_SB
  CV_M(2, L12, H12, B11) const f16x8 B15 = CV_B(960); const u32x2 L8 = CV_LO(3520); const u32x2 H8 = CV_HI(3520); CV_SB
  CV_M(2, L11, H11, B12) const f16x8 B16 = CV_B(1024); const u32x2 L7 = CV_LO(3584); const u32x2 H7 = CV_HI(3584); CV_SB
  CV_M(2, L10, H10, B13) const f16x8 B17 = CV_B(1088); const u32x2 L6 = CV_LO(3648); const u32x2 H6 = CV_HI(3648); CV_SB
  CV_M(2, L9, H9, B14) const f16x8 B18 = CV_B(1152); const u32x2 L5 = CV_LO(3712); const u32x2 H5 = CV_HI(3712); CV_SB
  CV_M(2, L8, H8, B15) const f16x8 B19 = CV_B(1216); const u32x2 L4 = CV_LO(3776); const u32x2 H4 = CV_HI(3776); CV_SB
  CV_M(2, L7, H7, B16) const f16x8 B20 = CV_B(1280); const u32x2 L3 = CV_LO(3840); const u32x2 H3 = CV_HI(3840); CV_SB
  CV_M(2, L6, H6, B17) const f16x8 B21 = CV_B(1344); const u32x2 L2 = CV_LO(3904); const u32x2 H2 = CV_HI(3904); CV_SB
  CV_M(2, L5, H5, B18) const f16x8 B22 = CV_B(1408); const u32x2 L1 = CV_LO(3968); const u32x2 H1 = CV_HI(3968); CV_SB
  CV_M(2, L4, H4, B19) const f16x8 B23 = CV_B(1472); const u32x2 L0 = CV_LO(4032); const u32x2 H0 = CV_HI(4032); CV_SB
  CV_M(2, L3, H3, B20)  CV_SB
  CV_M(2, L2, H2, B21)  CV_SB
  CV_M(2, L1, H1, B22)  CV_SB
  CV_M(2, L0, H0, B23)  CV_SB
}
template <int MASK> DI void conv_tiles(const char* smem, char* ws, int b, int c, int lane STAMP_PARAM) {
  constexpr int TMAX = 31 - __builtin_clz(MASK);
  const int m = lane & 15, q = lane >> 4;
  const char* pa = smem + CV_C + (4 * c + ((3 - m) & 3)) * CV_CSTR + 2 * (12 - 4 * (m >> 2) + 8 * q);
  u32 pa_hi = (u32)(size_t)pa + 8;
  asm volatile("" : "+v"(pa_hi));
  const char* pb = smem + CV_U + 48 * (2 + 2 * m + q) + 16 * c;
  const __amdgpu_buffer_rsrc_t rs_h = mkrs(ws + WS_HT, D_ * L_ * 2);
  f32x4 acc[8];
#pragma unroll
  for (int t = 0; t < 8; t++) acc[t] = (f32x4){0.f, 0.f, 0.f, 0.f};
  CV_SB
  if (MASK == 0xF0) conv_hi(pa, pa_hi, pb, acc); else if (MASK == 0x0C) conv_mid(pa, pa_hi, pb, acc); else if (MASK == 0x08) conv_t3(pa, pa_hi, pb, acc); else if (MASK == 0x04) conv_t21(pa, pa_hi, pb, acc); else conv_lo(pa, pa_hi, pb, acc);
  CV_SB
  const float isc = *(const float*)(smem + KG_S + 8 * c);
  const char* pu = smem + CV_U + 48 * (32 + 2 * m + (q >> 1)) + 16 * c + 8 * (q & 1);
#pragma unroll
  for (int t = 0; t <= TMAX; t++)
    if ((MASK >> t) & 1) {
      typedef _Float16 f16x4 __attribute__((ext_vector_type(4)));
      const f16x4 uv = *(const f16x4*)(pu + 1536 * t);
      const u32x4 sm = *(const u32x4*)(smem + CV_SM + 64 * m + 16 * q + 1024 * t);
      float hv[4];
#pragma unroll
      for (int e = 0; e < 4; e++) { hv[e] = fmaf(acc[t][e], isc, fmaf((float)uv[e], bflo(sm[e]), bfhi(sm[e]))); }
      __builtin_amdgcn_raw_buffer_store_b64((u32x2){pkbf(hv[0], hv[1]), pkbf(hv[2], hv[3])}, rs_h, 2 * (16 * m + 4 * q) + 512 * t, (3 * b + c) * 4096, 16);
    }
}
struct ColSum {
  u32x4 raw[18]; u32 off;
  DI void load(const u16* wg, const u16* wu, int b, int un, int lane) {
    const int mat = un / 36, r = un - 36 * mat, part = (r & 1) + 2 * ((b >> 2) & 1), c0 = 1152 * (b & 3) + 64 * (r >> 1);
    const u16* wp = (mat ? wu : wg) + (size_t)(144 * part + (lane >> 3)) * F_ + c0 + 8 * (lane & 7);
#pragma unroll
    for (int j = 0; j < 18; j++) raw[j] = *(const u32x4*)(wp + (size_t)8 * j * F_);
    off = ((part * 2 + mat) * F_ + c0) * 4;
  }
  DI void finish(char* ws, int lane) {
    const int col = lane & 15;
    const u32 one = (col & 1) ? 0x3f800000u : 0x00003f80u;
    const u32x4 sel = {col >> 1 == 0 ? one : 0u, col >> 1 == 1 ? one : 0u, col >> 1 == 2 ? one : 0u, col >> 1 == 3 ? one : 0u};
    f32x4 acc = {0.f, 0.f, 0.f, 0.f};
#pragma unroll
    for (int j = 0; j < 18; j++) acc = __builtin_amdgcn_mfma_f32_16x16x32_bf16(__builtin_bit_cast(bf16x8, raw[j]), __builtin_bit_cast(bf16x8, sel), acc, 0, 0, 0);
    float t[4];
#pragma unroll
    for (int e = 0; e < 4; e++) t[e] = __shfl(acc[e], (lane & 7) + 16 * (lane >> 5)) + __shfl(acc[e], (lane & 7) + 16 * (lane >> 5) + 32);
    const int e = (lane >> 3) & 3;
    __builtin_amdgcn_raw_buffer_store_b32(__float_as_uint(e == 0 ? t[0] : e == 1 ? t[1] : e == 2 ? t[2] : t[3]), mkrs(ws + WS_CS, 4 * 2 * F_ * 4), lane * 4, off, 16);
  }
};
DI u32 ln_tokens(const u16* x, char* ws, char* smem, int b, int lane) {
  const int tk = lane >> 3, i = lane & 7;
  const u32x4* row = (const u32x4*)(x + (size_t)(8 * b + tk) * D_);
  u32x4 raw[9];
#pragma unroll
  for (int j = 0; j < 9; j++) raw[j] = row[i + 8 * j];
  u32 nonce = 1;
  asm volatile("s_mov_b64 exec, 1\n\tglobal_atomic_add %0, %1, %0, %2 sc0\n\ts_mov_b64 exec, -1" : "+v"(nonce) : "v"(16 * b), "s"(ws + WS_SYNC) : "memory");
  const __amdgpu_buffer_rsrc_t rs_f = mkrs(ws + WS_FLG, F_BYTES);
  st128((u32x4){0u, 0u, 0u, 0u}, rs_f, lane * 16 + b * 1024);
  st128((u32x4){0u, 0u, 0u, 0u}, rs_f, lane * 16 + b * 1024 + 262144);
  float s[4] = {}, q[4] = {};
#pragma unroll
  for (int j = 0; j < 9; j++)
#pragma unroll
    for (int e = 0; e < 4; e++) { s[e] = dot2bf(raw[j][e], 0x3f803f80u, s[e]); q[e] = dot2bf(raw[j][e], raw[j][e], q[e]); }
  float s0 = s[0] + s[1], s1 = s[2] + s[3], q0 = q[0] + q[1], q1 = q[2] + q[3];
  asm volatile("s_waitcnt vmcnt(2)" : "+v"(s0), "+v"(q0), "+v"(nonce) :: "memory");
  s0 += s1;
  asm volatile("" : "+v"(s0));
  q0 += q1;
  s0 += dppf<0xB1>(s0); q0 += dppf<0xB1>(q0); s0 += dppf<0x4E>(s0); q0 += dppf<0x4E>(q0); s0 += dppf<0x141>(s0); q0 += dppf<0x141>(q0);
  const float m = s0 * (1.f / D_), mb = rbf(m);
  const float sd = rbf(__builtin_amdgcn_sqrtf(rbf(rbf(fmaxf(fmaf(-m, m, q0 * (1.f / D_)), 0.f)) + 1e-5f)));
  const float rs = __builtin_amdgcn_rcpf(sd), nm = -mb * rs;
#pragma unroll
  for (int j = 0; j < 9; j++) {
    u32x4 o;
#pragma unroll
    for (int e = 0; e < 4; e++) { const f32x2 a = (f32x2){bflo(raw[j][e]), bfhi(raw[j][e])} * rs + nm; o[e] = pkh(a.x, a.y); }
    *(u32x4*)(smem + tk * 1184 + 16 * (i + 8 * j)) = o;
  }
  const __amdgpu_buffer_rsrc_t rs_u = mkrs(ws + WS_U, 256 * U_BLK);
  const u32 smv = __shfl(pkbf(sd, mb), 8 * (lane & 7));
  __builtin_amdgcn_raw_buffer_store_b32(smv, mkrs(ws + WS_SM, L_ * 8), lane < 16 ? 4 * lane : 0x7ffffff0, b * 64, 16);
  asm volatile("s_waitcnt lgkmcnt(0)" ::: "memory");
  const u32 rbase = (u32)(size_t)smem + ((lane & 15) >> 2) * 1184 + 32 * (lane >> 4) + 8 * (lane & 3);
#pragma unroll
  for (int e = 0; e < 9; e++) {
    const u32x2 a0 = tr_read(rbase + 128 * e), a1 = tr_read(rbase + 128 * e + 4 * 1184);
    st128((u32x4){a0.x, a0.y, a1.x, a1.y}, rs_u, lane * 16 + e * 1024 + b * U_BLK);
  }
  return __builtin_amdgcn_readfirstlane(nonce) + 1u;
}
DI void ln_signal(char* ws, int b, u32 nonce, int lane) {
  for (int it = 0; it < SPIN; it++) {
    u32 v = 0;
    if (lane == 0) v = atom_read(ws + WS_SYNC + S_ECHO + 4 * b);
    DRAIN();
    if (__builtin_amdgcn_readfirstlane(v) == nonce) break;
  }
  if (lane < UREP) atom_add(ws + WS_SYNC + S_U + USP * (NSH * lane + (b & (NSH - 1))), 1u);
}

template <int NT> DI void kernel_values(char* smem, int tk0, int lane) {
  const int col = lane & 15, q = lane >> 4;
  f16x8 fa[NT][8], fb[NT][8];
  f32x4 acc[NT];
  float dsc[NT];
#pragma unroll
  for (int k = 0; k < NT; k++) {
    const int tk = tk0 + 7 * k, c3 = (tk * 11) >> 5, tl = tk - 3 * c3;
    const char* pb = smem + KG_T + c3 * KG_CH + col * KG_ROW + 16 * q;
    const char* pa = pb + 16 * (tl + 1) * KG_ROW;
    dsc[k] = *(const float*)(smem + KG_S + 8 * c3 + 4);
#pragma unroll
    for (int s = 0; s < 8; s++) { fa[k][s] = *(const f16x8*)(pa + 64 * s); fb[k][s] = *(const f16x8*)(pb + 64 * s); }
  }
  __builtin_amdgcn_sched_barrier(0);
  int i0[NT], d1[NT], d2[NT];
#pragma unroll
  for (int k = 0; k < NT; k++) {
    const int tk = tk0 + 7 * k, c3 = (tk * 11) >> 5, tl = tk - 3 * c3, lo = q < 2;
    const int a0 = 4 * (q & 1) + 128 * (q >> 1) + 8 * col, a1 = lo ? 256 + 64 * q + 4 * col : 256 + 64 * q + 2 * col, a2 = lo ? 512 + 64 * q + col : (lane == 32 ? 640 : 642);
    i0[k] = 656 * c3 + (tl == 0 ? a0 : tl == 1 ? a1 : a2);
    d1[k] = tl == 2 && lo ? 16 : 1;
    d2[k] = tl == 0 ? 2 : tl == 1 ? (lo ? 2 : 32) : (lo ? 32 : 1);
    acc[k] = (f32x4){tl == 0 && lane == 0 ? dsc[k] : 0.f, 0.f, 0.f, 0.f};
  }
  __builtin_amdgcn_sched_barrier(0);
#pragma unroll
  for (int k = 0; k < NT; k++)
#pragma unroll
    for (int s = 0; s < 8; s++) acc[k] = __builtin_amdgcn_mfma_f32_16x16x32_f16(fa[k][s], fb[k][s], acc[k], 0, 0, 0);
  u16* kc = (u16*)(smem + KG_C);
#pragma unroll
  for (int k = 0; k < NT; k++) {
    const u32 p01 = pkh(acc[k][0], acc[k][1]), p23 = pkh(acc[k][2], acc[k][3]);
    kc[i0[k]] = (u16)p01; kc[i0[k] + d1[k]] = (u16)(p01 >> 16); kc[i0[k] + d2[k]] = (u16)p23; kc[i0[k] + d1[k] + d2[k]] = (u16)(p23 >> 16);
  }
}
#define U_ASK(v) asm volatile("v_mov_b32 %0, 0\n\ts_mov_b64 exec, %3\n\tglobal_atomic_add %0, %1, %0, %2 sc0\n\ts_mov_b64 exec, -1" : "+v"(v) : "v"(uoff), "s"(ws + WS_SYNC), "n"((1 << NSH) - 1) : "memory")
#define U_GET(v) asm volatile("s_waitcnt vmcnt(0)" : "+v"(v) :: "memory")
DI void code_slice(char* smem, int part, int lane) {
  u32 lo, hi, n;
  asm volatile("s_getpc_b64 vcc\n.Lks_pc:\n\ts_add_u32 %0, vcc_lo, .Lk3_begin-.Lks_pc\n\ts_addc_u32 %1, vcc_hi, 0\n\ts_mov_b32 %2, .Lk3_end-.Lk3_begin" : "=s"(lo), "=s"(hi), "=s"(n) :: "vcc", "scc");
  __builtin_amdgcn_raw_ptr_buffer_load_lds(mkrs((const void*)(((unsigned long long)hi << 32) | lo), n & ~3u), (LAS void*)(smem + 161792), 4, lane < 18 ? part * 2304 + lane * 128 : 0x7ffffff0, 0, 0, 0);
}
DI void gather_u(char* ws, char* smem, int b, int lane) {
  const __amdgpu_buffer_rsrc_t rs_u = mkrs(ws + WS_U, 256 * U_BLK);
  u32 uo[3];
#pragma unroll
  for (int i = 0; i < 3; i++) { const u32 id = 64 * i + lane, blk = (id * 21846u) >> 16; uo[i] = blk * (U_BLK - 48) + id * 16 + 48 * b; }
#pragma unroll
  for (int i = 0; i < 12; i++) __builtin_amdgcn_raw_ptr_buffer_load_lds(rs_u, (LAS void*)(smem + CV_U + 1536 + 1024 * i), 16, uo[i % 3], (i / 3) * 64 * U_BLK, 0, 16);
}
DI void gather_sm(char* ws, char* smem, int lane) {
#pragma unroll
  for (int i = 0; i < 8; i++) __builtin_amdgcn_raw_ptr_buffer_load_lds(mkrs(ws + WS_SM, L_ * 8), (LAS void*)(smem + CV_SM + 1024 * i), 16, 64 * (lane >> 1) + 16 * (lane & 1), 2048 * i, 0, 16);
}

__global__ __launch_bounds__(512, 2) void s4d_geglu(const u16* __restrict__ x, const u16* __restrict__ logA, const u16* __restrict__ Aim, const u16* __restrict__ Cr,
                                                    const u16* __restrict__ Ci, const u16* __restrict__ logdt, const u16* __restrict__ Dsk, const u16* __restrict__ wg,
                                                    const u16* __restrict__ wu, const u16* __restrict__ wd, u16* __restrict__ out, char* __restrict__ ws) {
  __shared__ __attribute__((aligned(16))) char smem[163840];
  const u32 sb = (u32)(size_t)smem;
  const int tid = threadIdx.x, lane = tid & 63, w = __builtin_amdgcn_readfirstlane(tid >> 6), b = blockIdx.x;
  {
    u32 t0, off = lane < 20 ? (b >> 3) * 2560 + lane * 128 : 0x7ffffff0;
    asm volatile("s_cmp_lg_u32 %3, 7\n\ts_cbranch_scc1 .Lka_nopf\n\ts_cmp_eq_u32 %2, 255\n\ts_cbranch_scc1 .Lka_nopf\n\t"
                 "s_getpc_b64 s[40:41]\n.Lka_pc:\n\ts_and_b32 s41, s41, 0xffff\n\ts_mov_b32 s42, .Lk3_end-.Lka_pc\n\ts_mov_b32 s43, 0x20000\n\t"
                 "buffer_load_dword %0, %1, s[40:43], 0 offen\n\ts_waitcnt vmcnt(0)\n.Lka_nopf:"
                 : "=&v"(t0) : "v"(off), "s"(b), "s"(w) : "s40", "s41", "s42", "s43", "scc", "memory");
  }
  STAMP_DECL; STAMP_AT(ws, 0, 0);
  if (__builtin_expect(w == 6, 1)) {
    __builtin_amdgcn_s_setprio(3);
    const u32 nonce = ln_tokens(x, ws, smem, b, lane);
    STAMP_AT(ws, 0, 3);
    ln_signal(ws, b, nonce, lane);
    STAMP_AT(ws, 0, 7);
  }
  if (b < 192) {
    const int cb = CBMAP ? 24 * (b & 7) + (b >> 3) : b;
    ColSum cs;
    if (w == 6) {
      const u32 uoff = S_U + USP * (NSH * (b & (UREP - 1)) + (lane & (NSH - 1)));
      u32 v = 0;
      BAR();
      STAMP_AT(ws, 0, 2);
      BAR();
      for (int it = 0; it < SPIN; it++) { U_ASK(v); U_GET(v); if (__all(v == UCNT || lane >= NSH)) break; }
      STAMP_AT(ws, 0, 6);
      gather_u(ws, smem, cb, lane);
      gather_sm(ws, smem, lane);
      asm volatile("s_waitcnt vmcnt(17)" ::: "memory");
    } else {
    if (w < 6) {
      if (KSLEEP) __builtin_amdgcn_s_sleep(KSLEEP);
      const int c3 = w >> 1, d = 3 * cb + c3, n = tid & 127;
      const float la = bf2f(logA[d * NM + n]), ai = bf2f(Aim[d * NM + n]), cr0 = bf2f(Cr[d * NM + n]), ci0 = bf2f(Ci[d * NM + n]);
      const float cr1 = bf2f(Cr[d * NM + (n ^ 64)]), ci1 = bf2f(Ci[d * NM + (n ^ 64)]);
      const float dt = rbf(fexp(bf2f(logdt[d]))), dsk = bf2f(Dsk[d]);
      const float zr2 = (-rbf(fexp(la)) * dt) * 1.4426950408889634f, zi2 = (ai * dt) * 0.15915494309189535f;
      Cx c = (Cx){cr0, ci0} * (2.f * dt);
      float mx = row16_max(fmaxf(fmaxf(fabsf(cr0), fabsf(ci0)), fmaxf(fabsf(cr1), fabsf(ci1))));
      mx = fmaxf(fmaxf(rdlane(mx, 0), rdlane(mx, 16)), fmaxf(rdlane(mx, 32), rdlane(mx, 48))) * (2.f * dt);
      const int eb = min(max((int)(__float_as_uint(fmaxf(mx, fabsf(dsk) * (1.f / 64.f))) >> 23), 8), 253);
      const float sc = __uint_as_float((u32)(261 - eb) << 23);
      if (n == 0) *(f32x2*)(smem + KG_S + 8 * c3) = (f32x2){__uint_as_float((u32)(eb - 7) << 23), dsk * sc};
      c *= sc;
      char* tq = smem + KG_T + c3 * KG_CH + 4 * n;
      char* tr = tq + 16 * KG_ROW;
      const Cx E128 = cexp_p(128.f * zr2, 128.f * zi2), E8 = cexp_p(8.f * zr2, -8.f * zi2), E1 = cexp_p(zr2, zi2), E16 = cmul(E8, E8), E256 = cmul(E128, E128);
      Cx q0 = {1.f, 0.f}, q1 = E8, r = cmul(c, cexp_p(1024.f * zr2, 1024.f * zi2)), t0 = c, t1 = cmul(c, E128);
#pragma unroll
      for (int a = 0; a < 8; a++) {
        *(u32*)(tq + 2 * a * KG_ROW) = pkc(q0); *(u32*)(tq + (2 * a + 1) * KG_ROW) = pkc(q1);
        *(u32*)(tr + (32 + a) * KG_ROW) = pkc(r); *(u32*)(tr + a * KG_ROW) = pkc(t0); *(u32*)(tr + (8 + a) * KG_ROW) = pkc(t1);
        if (!(a & 1)) {
          const Cx u0 = cmul(t0, E256), u1 = cmul(t1, E256);
          *(u32*)(tr + (16 + a / 2) * KG_ROW) = pkc(u0); *(u32*)(tr + (20 + a / 2) * KG_ROW) = pkc(u1);
          if (!(a & 3)) {
            const Cx v0 = cmul(u0, E256), v1 = cmul(u1, E256);
            *(u32*)(tr + (24 + a / 4) * KG_ROW) = pkc(v0); *(u32*)(tr + (26 + a / 4) * KG_ROW) = pkc(v1);
            *(u32*)(tr + (28 + a / 4) * KG_ROW) = pkc(cmul(v0, E256)); *(u32*)(tr + (30 + a / 4) * KG_ROW) = pkc(cmul(v1, E256));
          }
        }
        r = cmul(r, E128);
        if (a < 7) { q0 = a ? cmul(q0, E16) : E16; q1 = cmul(q1, E16); t0 = cmul(t0, E1); t1 = cmul(t1, E1); }
      }
      *(u32*)(tr + 40 * KG_ROW) = pkc(r);
    }
    BAR();
    STAMP_AT(ws, 0, 2);
    if (w == 7) cs.load(wg, wu, b, 3 * (b >> 3) + 2, lane);
    if (w < 2) kernel_values<2>(smem, w, lane);
    else kernel_values<1>(smem, w < 6 ? w : 6, lane);
    STAMP_AT(ws, 0, 6);
    BAR();
    STAMP_AT(ws, 0, 7);
    u32x4 er[2];
    const int cA = w >> 1, yA = 64 * (w & 1) + lane, cB = w < 3 ? w : w - 3, yB = (w < 3 ? 128 : 192) + lane;
    if (w < 6) {
      const u16* kc = (const u16*)(smem + KG_C + 1312 * cA);
      const u32 k0 = kc[639 - yA], k1 = kc[640 - yA];
      const int a = 255 - yB;
      const char* kb = smem + KG_C + 1312 * cB;
      const int st = w < 3 ? 256 + 2 * a : lane < 32 ? 128 + 4 * a : 8 * a;
      const u32x2 p0 = *(const u32x2*)(kb + 2 * (st & ~3)), p1 = *(const u32x2*)(kb + 2 * (st & ~3) + 8);
      u32 nx[2][3];
#pragma unroll
      for (int e = 0; e < 3; e++) {
        const int lA = 2039 - 8 * yA - e, lB = 2039 - 8 * yB - e;
        const u32 pb2 = pkbf((float)lA, (float)lB);
        nx[0][e] = kc[(pb2 & 0xffffu) - 17024];
        const u32 t = ((const u16*)kb)[lB < 128 ? max(lB, 0) : (int)(pb2 >> 16) - 17024];
        nx[1][e] = lB < 0 ? 0u : t;
      }
      const u32 k4 = (yA & 1) ? k0 : k1;
      er[0] = (u32x4){k1 | (k1 << 16), k1 | (k4 << 16), k0 | (k0 << 16), k0 | (k0 << 16)};
      const u32 q0 = (st & 2) ? p0.y : p0.x, q1 = (st & 2) ? p1.x : p0.y;
      const u32x4 o4 = {perm(q1, q1, 0x01000100u), perm(q0, q0, 0x03020302u), perm(q0, q0, 0x01000302u), perm(q0, q0, 0x01000100u)};
      const u32x4 o2 = {perm(p0.y, p1.x, 0x07060100u), perm(p0.y, p0.y, 0x01000100u), perm(p0.x, p0.y, 0x07060100u), perm(p0.x, p0.x, 0x01000100u)};
      const u32x4 o1 = {perm(p1.y, p1.y, 0x01000302u), perm(p1.x, p1.x, 0x01000302u), perm(p0.y, p0.y, 0x01000302u), perm(p0.x, p0.x, 0x01000302u)};
#pragma unroll
      for (int e = 0; e < 4; e++) er[1][e] = w < 3 ? o4[e] : lane < 32 ? o2[e] : o1[e];
#pragma unroll
      for (int k = 0; k < 2; k++) {
        const u32x4 ea = er[k];
        const u32 ebx = nx[k][0] | (nx[k][1] << 16);
        const u32 s0 = __builtin_amdgcn_alignbit(ea.y, ea.x, 16), s1 = __builtin_amdgcn_alignbit(ea.z, ea.y, 16), s2 = __builtin_amdgcn_alignbit(ea.w, ea.z, 16),
                  s3 = __builtin_amdgcn_alignbit(ebx, ea.w, 16), s4 = nx[k][1] | (nx[k][2] << 16);
        char* p = smem + CV_C + 4 * (k ? cB : cA) * CV_CSTR + 16 * (k ? yB : yA);
        *(u32x4*)(p) = ea;
        *(u32x4*)(p + CV_CSTR) = (u32x4){s0, s1, s2, s3};
        *(u32x4*)(p + 2 * CV_CSTR) = (u32x4){ea.y, ea.z, ea.w, ebx};
        *(u32x4*)(p + 3 * CV_CSTR) = (u32x4){s1, s2, s3, s4};
      }
    } else {
      if (lane < 24) *(u32x4*)(smem + CV_C + (lane >> 1) * CV_CSTR + 4096 + 16 * (lane & 1)) = (u32x4){0u, 0u, 0u, 0u};
      *(u32x4*)(smem + CV_U + 16 * lane) = (u32x4){0u, 0u, 0u, 0u};
      if (lane < 32) *(u32x4*)(smem + CV_U + 1024 + 16 * lane) = (u32x4){0u, 0u, 0u, 0u};
      code_slice(smem, b >> 3, lane);
      cs.finish(ws, lane);
    }
    STAMP_AT(ws, 0, 3);
    }
    BAR();
    STAMP_AT(ws, 0, 4);
    if (w < 3) conv_tiles<0xF0>(smem, ws, cb, w, lane STAMP_ARG);
    else if ((w & 3) < 3) {
      if (w < 6) { if (CSF) cs.load(wg, wu, b, 3 * (b >> 3) + w - 4, lane); CV_BAR CV_BAR }
      else asm volatile("s_waitcnt vmcnt(13)\n\ts_barrier\n\ts_waitcnt vmcnt(0)\n\ts_barrier" ::: "memory");
      conv_tiles<0x03>(smem, ws, cb, w - 4, lane STAMP_ARG);
      if (CSF && w < 6) cs.finish(ws, lane);
    } else {
      conv_tiles<0x0C>(smem, ws, cb, w >> 2, lane STAMP_ARG);
      if (w == 7) conv_tiles<0x08>(smem, ws, cb, 2, lane STAMP_ARG); else conv_tiles<0x04>(smem, ws, cb, 2, lane STAMP_ARG);
      if (CSV >= 4 && w == 7) cs.finish(ws, lane);
    }
    STAMP_AT(ws, 0, 5);
    DRAIN();
    BAR();
    if (w == 0 && lane < HREP) atom_add(ws + WS_FLG + F_H + HSP * (HSH * lane + (b & (HSH - 1))), 1u);
  } else {
    const int pb = b - 192, ksq = 128 + (pb >> 2), Cq = pb & 3;
    u32x4 v[2][6], vq[2]; u32 dv[6], dq[2];
    if (w < 6) {
      asm volatile("s_barrier" ::: "memory");
      const int fa0 = wd_a(2 * pb), fa1 = wd_a(2 * pb + 1), faq = wd_a(ksq), g0 = wd_gap(2 * pb), g1 = wd_gap(2 * pb + 1), gq = wd_gap(ksq);
#pragma unroll
      for (int k = 0; k < 2; k++)
#pragma unroll
        for (int j = 0; j < 6; j++) {
          const u32 id = tid + 384 * j, row = (id * 58255u) >> 22, ch = id - 72 * row;
          v[k][j] = *(const u32x4*)(wd + (row + (k ? fa1 : fa0) + (row >> 4) * (k ? g1 : g0)) * D_ + 8 * ch);
          dv[j] = PK_0 + row * PK_ROW + 16 * ch;
        }
#pragma unroll
      for (int j = 0; j < 2; j++) {
        const u32 id = min(tid + 384 * j, 575), row = (id * 58255u) >> 20, ch = id - 18 * row;
        vq[j] = *(const u32x4*)(wd + (row + faq + (row >> 4) * gq) * D_ + 144 * Cq + 8 * ch);
        dq[j] = PK_Q + row * PK_QROW + 16 * ch;
      }
    } else if (w == 6) {
      asm volatile("s_barrier" ::: "memory");
      wait_counters<32>(ws + WS_SYNC + S_U + USP * NSH * (b & (UREP - 1)), lane);
    } else if (b != 255) {
      asm volatile("s_barrier" ::: "memory");
      if (pb < TOUCH) {
        __builtin_amdgcn_raw_ptr_buffer_load_lds(mkrs(ws, 47u << 20), (LAS void*)(smem + 161792), 4, lane << 20, 0, 0, 0);
        __builtin_amdgcn_raw_ptr_buffer_load_lds(mkrs(out, L_ * D_ * 2), (LAS void*)(smem + 162048), 4, lane << 19, 0, 0, 0);
      }
    } else {
      const __amdgpu_buffer_rsrc_t rs_s = mkrs(ws + WS_SYNC, S_U + USP * NSH * UREP);
#pragma unroll
      for (int i = 0; i < USP * NSH * UREP / 1024; i++) st128((u32x4){0u, 0u, 0u, 0u}, rs_s, S_U + 1024 * i + 16 * lane);
      for (int it = 0; it < SPIN; it++) {
        u32x4 r;
#pragma unroll
        for (int e = 0; e < 4; e++) r[e] = atom_read(ws + WS_SYNC + 64 * lane + 16 * e);
        u32 u = UCNT;
        if (lane < NSH) u = atom_read(ws + WS_SYNC + S_U + USP * lane);
        DRAIN();
        st128(r, rs_s, S_ECHO + 16 * lane);
        if (it && __all(u == UCNT)) break;
      }
      asm volatile("s_barrier" ::: "memory");
    }
    const int rr = 4 * (lane >> 4) + ((lane & 15) >> 2), p = lane & 3;
    const __amdgpu_buffer_rsrc_t rs_wp = mkrs(ws + WS_WP, 5308416);
    auto frag = [&](u32 base, u32 stride, int C, int dt, int ks) {
      const int dl0 = dt < 8 ? (C & 1) * 16 + 32 * (dt >> 1) + 4 * (dt & 1) : (C & 1 ? 0 : 128), dlp = dt < 8 ? 16 : 8, q4 = (ks * 57) >> 11;
      const u32 a = base + 2 * dl0 + rr * stride + dlp * p;
      const u32x2 a0 = tr_read(a), a1 = tr_read(a + 16 * stride);
      st128((u32x4){a0.x, a0.y, a1.x, a1.y}, rs_wp, lane * 16 + (((q4 * 4 + C) * 36 + ks - 36 * q4) * 9 + dt) * 1024);
    };
#pragma unroll
    for (int k = 0; k < 2; k++) {
      if (w < 6) {
#pragma unroll
        for (int j = 0; j < 6; j++) *(u32x4*)(smem + k * 32 * PK_ROW + dv[j]) = v[k][j];
      }
      BAR();
      const int C = w >> 1, d0 = (w & 1) * 4;
#pragma unroll
      for (int i = 0; i < 4; i++) frag(sb + PK_0 + k * 32 * PK_ROW + 288 * C, PK_ROW, C, d0 + i, 2 * pb + k);
      if ((w & 1) == k) frag(sb + PK_0 + k * 32 * PK_ROW + 288 * C, PK_ROW, C, 8, 2 * pb + k);
    }
    STAMP_AT(ws, 0, 3);
    if (w < 6) {
#pragma unroll
      for (int j = 0; j < 2; j++) *(u32x4*)(smem + dq[j]) = vq[j];
    }
    BAR();
    frag(sb + PK_Q, PK_QROW, Cq, w, ksq);
    if (w == 1) frag(sb + PK_Q, PK_QROW, Cq, 8, ksq);
    STAMP_AT(ws, 0, 5);
    DRAIN();
    BAR();
    if (w == 0 && lane < 8) atom_add(ws + WS_FLG + F_W + 64 * lane, 1u);
  }
  __builtin_amdgcn_s_setprio(0);
  STAMP_AT(ws, 0, 1); STAMP_FLUSH(ws, 0);
  asm volatile(
".Lk3_begin:\n"
"s_load_dwordx2 s[56:57], %0, 0x58\n"
"s_load_dwordx2 s[58:59], %0, 0x38\n"
"s_load_dwordx2 s[96:97], %0, 0x40\n"
"s_load_dwordx2 s[68:69], %0, 0x50\n"
"s_mov_b32 s48, %1\n"
"s_mov_b32 s49, %2\n"
"s_waitcnt lgkmcnt(0)\n"
"v_mbcnt_lo_u32_b32 v219, -1, 0\n"
"v_mbcnt_hi_u32_b32 v219, -1, v219\n"
"s_add_u32 s20, s56, 0x800000\n"
"s_addc_u32 s21, s57, 0\n"
"s_and_b32 s21, s21, 0xffff\n"
"s_mov_b32 s22, 0x240000\n"
"s_mov_b32 s23, 0x20000\n"
"s_add_u32 s24, s58, 0xfffff000\n"
"s_addc_u32 s25, s59, -1\n"
"s_and_b32 s25, s25, 0xffff\n"
"s_mov_b32 s26, 0x511000\n"
"s_mov_b32 s27, 0x20000\n"
"s_add_u32 s28, s96, 0xfffff000\n"
"s_addc_u32 s29, s97, -1\n"
"s_and_b32 s29, s29, 0xffff\n"
"s_mov_b32 s30, 0x511000\n"
"s_mov_b32 s31, 0x20000\n"
"s_and_b32 s50, s49, 3\n"
"s_and_b32 s53, s48, 3\n"
"s_lshr_b32 s55, s48, 3\n"
"s_and_b32 s51, s55, 3\n"
"s_and_b32 s54, s48, 4\n"
"s_add_u32 s51, s51, s54\n"
"s_lshr_b32 s52, s55, 2\n"
"s_lshl_b32 s54, s53, 3\n"
"s_add_u32 s52, s52, s54\n"
"v_lshlrev_b32 v254, 4, v219\n"
"s_cmp_ge_u32 s49, 4\n"
"s_cbranch_scc1 .Lf_roleB\n"
"v_lshrrev_b32 v220, 5, v219\n"
"v_and_b32 v221, 31, v219\n"
"s_lshl_b32 s54, s50, 15\n"
"s_lshl_b32 s55, s51, 9\n"
"s_add_u32 s54, s54, s55\n"
"v_and_b32 v222, 1, v221\n"
"v_lshlrev_b32 v222, 4, v222\n"
"v_lshl_add_u32 v222, v220, 12, v222\n"
"v_add_u32 v222, s54, v222\n"
"v_lshrrev_b32 v221, 1, v221\n"
"v_xor_b32 v221, v220, v221\n"
"v_xor_b32 v223, 0, v221\n"
"v_lshl_add_u32 v214, v223, 5, v222\n"
"v_xor_b32 v223, 2, v221\n"
"v_lshl_add_u32 v215, v223, 5, v222\n"
"v_add_u32 v215, 0x1c00, v215\n"
"v_xor_b32 v223, 4, v221\n"
"v_lshl_add_u32 v216, v223, 5, v222\n"
"v_add_u32 v216, 0x3800, v216\n"
"v_xor_b32 v223, 6, v221\n"
"v_lshl_add_u32 v217, v223, 5, v222\n"
"v_add_u32 v217, 0x5400, v217\n"
"s_lshl_b32 s61, s50, 12\n"
"s_add_u32 s64, s56, 0xb40000\n"
"s_addc_u32 s65, s57, 0\n"
"s_and_b32 s65, s65, 0xffff\n"
"s_mov_b32 s66, 0x4e000\n"
"s_mov_b32 s67, 0x20000\n"
"s_cmp_lg_u32 s49, 0\n"
"s_cbranch_scc1 .Lf_hskip\n"
"s_and_b32 s54, s48, 15\n"
"s_mul_i32 s54, s54, 1024\n"
"s_add_u32 s60, s54, 294912\n"
"s_movk_i32 s55, 0\n"
"s_mov_b64 exec, 255\n"
"v_lshlrev_b32 v230, 7, v219\n"
".Lf_pwh:\n"
"v_mov_b32 v231, 0\n"
"buffer_atomic_add v231, v230, s[64:67], s60 offen offset:0 sc0\n"
"s_waitcnt vmcnt(0)\n"
"v_cmp_ne_u32 s[96:97], 24, v231\n"
"s_add_u32 s55, s55, 1\n"
"s_nop 0\n"
"s_cmp_lg_u64 s[96:97], 0\n"
"s_cbranch_scc0 .Lf_pdh\n"
"s_cmp_lt_u32 s55, 0x20000\n"
"s_cbranch_scc1 .Lf_pwh\n"
".Lf_pdh:\n"
"s_mov_b64 exec, -1\n"
".Lf_hskip:\n"
"s_barrier\n"
"s_mov_b32 s60, 0\n"
"s_add_u32 m0, s61, 0\n"
"s_nop 0\n"
"buffer_load_dwordx4 v214, s[20:23], s60 offen offset:0 sc1 lds\n"
"buffer_load_dwordx4 v215, s[20:23], s60 offen offset:1024 sc1 lds\n"
"buffer_load_dwordx4 v216, s[20:23], s60 offen offset:2048 sc1 lds\n"
"buffer_load_dwordx4 v217, s[20:23], s60 offen offset:3072 sc1 lds\n"
"s_mov_b32 s60, 131072\n"
"s_add_u32 m0, s61, 16384\n"
"s_nop 0\n"
"buffer_load_dwordx4 v214, s[20:23], s60 offen offset:0 sc1 lds\n"
"buffer_load_dwordx4 v215, s[20:23], s60 offen offset:1024 sc1 lds\n"
"buffer_load_dwordx4 v216, s[20:23], s60 offen offset:2048 sc1 lds\n"
"buffer_load_dwordx4 v217, s[20:23], s60 offen offset:3072 sc1 lds\n"
"s_mov_b32 s60, 262144\n"
"s_add_u32 m0, s61, 32768\n"
"s_nop 0\n"
"buffer_load_dwordx4 v214, s[20:23], s60 offen offset:0 sc1 lds\n"
"buffer_load_dwordx4 v215, s[20:23], s60 offen offset:1024 sc1 lds\n"
"buffer_load_dwordx4 v216, s[20:23], s60 offen offset:2048 sc1 lds\n"
"buffer_load_dwordx4 v217, s[20:23], s60 offen offset:3072 sc1 lds\n"
"s_add_u32 s36, s56, 0xa80000\n"
"s_addc_u32 s37, s57, 0\n"
"s_and_b32 s37, s37, 0xffff\n"
"s_mov_b32 s38, 0x24000\n"
"s_mov_b32 s39, 0x20000\n"
"s_add_u32 s32, s56, 0x1200000\n"
"s_addc_u32 s33, s57, 0\n"
"s_and_b32 s33, s33, 0xffff\n"
"s_mov_b32 s34, 0x1200000\n"
"s_mov_b32 s35, 0x20000\n"
"v_lshrrev_b32 v220, 4, v219\n"
"v_bfe_u32 v221, v219, 2, 2\n"
"v_and_b32 v222, 3, v219\n"
"v_lshlrev_b32 v222, 3, v222\n"
"v_and_b32 v224, 1, v220\n"
"v_lshl_add_u32 v224, v224, 2, v221\n"
"v_lshrrev_b32 v223, 1, v220\n"
"v_lshl_add_u32 v223, v223, 4, v224\n"
"v_mul_u32_u24 v212, 0x120, v223\n"
"v_add_u32 v212, v212, v222\n"
"v_add_u32 v212, 0x10000, v212\n"
"v_add_u32 v213, 0x9000, v212\n"
"v_lshl_add_u32 v225, v223, 9, v222\n"
"s_lshl_b32 s54, s50, 2\n"
"v_xor_b32 v226, s54, v224\n"
"v_xor_b32 v226, 0, v226\n"
"v_lshl_add_u32 v208, v226, 5, v225\n"
"v_xor_b32 v226, s54, v224\n"
"v_xor_b32 v226, 1, v226\n"
"v_lshl_add_u32 v209, v226, 5, v225\n"
"v_xor_b32 v226, s54, v224\n"
"v_xor_b32 v226, 2, v226\n"
"v_lshl_add_u32 v210, v226, 5, v225\n"
"v_xor_b32 v226, s54, v224\n"
"v_xor_b32 v226, 3, v226\n"
"v_lshl_add_u32 v211, v226, 5, v225\n"
"s_waitcnt vmcnt(8)\n"
"s_barrier\n"
"ds_read_b64_tr_b16 v[176:177], v208 offset:0\n"
"ds_read_b64_tr_b16 v[178:179], v208 offset:4096\n"
"ds_read_b64_tr_b16 v[180:181], v209 offset:0\n"
"ds_read_b64_tr_b16 v[182:183], v209 offset:4096\n"
"ds_read_b64_tr_b16 v[184:185], v210 offset:0\n"
"ds_read_b64_tr_b16 v[186:187], v210 offset:4096\n"
"ds_read_b64_tr_b16 v[188:189], v211 offset:0\n"
"ds_read_b64_tr_b16 v[190:191], v211 offset:4096\n"
"ds_read_b64_tr_b16 v[160:161], v212 offset:0\n"
"ds_read_b64_tr_b16 v[162:163], v212 offset:2304\n"
"ds_read_b64_tr_b16 v[164:165], v212 offset:9216\n"
"ds_read_b64_tr_b16 v[166:167], v212 offset:11520\n"
"ds_read_b64_tr_b16 v[168:169], v212 offset:32\n"
"ds_read_b64_tr_b16 v[170:171], v212 offset:2336\n"
"s_waitcnt lgkmcnt(4)\n"
"v_mfma_f32_16x16x32_bf16 v[0:3], v[160:163], v[176:179], 0\n"
"s_mov_b32 s60, 393216\n"
"s_add_u32 m0, s61, 49152\n"
"s_nop 0\n"
"buffer_load_dwordx4 v214, s[20:23], s60 offen offset:0 sc1 lds\n"
"v_mfma_f32_16x16x32_bf16 v[4:7], v[160:163], v[180:183], 0\n"
"ds_read_b64_tr_b16 v[172:173], v212 offset:9248\n"
"v_mfma_f32_16x16x32_bf16 v[8:11], v[160:163], v[184:187], 0\n"
"ds_read_b64_tr_b16 v[174:175], v212 offset:11552\n"
"v_mfma_f32_16x16x32_bf16 v[12:15], v[160:163], v[188:191], 0\n"
"s_waitcnt lgkmcnt(4)\n"
"v_mfma_f32_16x16x32_bf16 v[28:31], v[164:167], v[188:191], 0\n"
"buffer_load_dwordx4 v215, s[20:23], s60 offen offset:1024 sc1 lds\n"
"v_mfma_f32_16x16x32_bf16 v[24:27], v[164:167], v[184:187], 0\n"
"ds_read_b64_tr_b16 v[160:161], v212 offset:64\n"
"v_mfma_f32_16x16x32_bf16 v[20:23], v[164:167], v[180:183], 0\n"
"ds_read_b64_tr_b16 v[162:163], v212 offset:2368\n"
"v_mfma_f32_16x16x32_bf16 v[16:19], v[164:167], v[176:179], 0\n"
"s_waitcnt lgkmcnt(4)\n"
"v_mfma_f32_16x16x32_bf16 v[32:35], v[168:171], v[176:179], 0\n"
"buffer_load_dwordx4 v216, s[20:23], s60 offen offset:2048 sc1 lds\n"
"v_mfma_f32_16x16x32_bf16 v[36:39], v[168:171], v[180:183], 0\n"
"ds_read_b64_tr_b16 v[164:165], v212 offset:9280\n"
"v_mfma_f32_16x16x32_bf16 v[40:43], v[168:171], v[184:187], 0\n"
"ds_read_b64_tr_b16 v[166:167], v212 offset:11584\n"
"v_mfma_f32_16x16x32_bf16 v[44:47], v[168:171], v[188:191], 0\n"
"s_waitcnt lgkmcnt(4)\n"
"v_mfma_f32_16x16x32_bf16 v[60:63], v[172:175], v[188:191], 0\n"
"buffer_load_dwordx4 v217, s[20:23], s60 offen offset:3072 sc1 lds\n"
"v_mfma_f32_16x16x32_bf16 v[56:59], v[172:175], v[184:187], 0\n"
"ds_read_b64_tr_b16 v[168:169], v212 offset:96\n"
"v_mfma_f32_16x16x32_bf16 v[52:55], v[172:175], v[180:183], 0\n"
"ds_read_b64_tr_b16 v[170:171], v212 offset:2400\n"
"v_mfma_f32_16x16x32_bf16 v[48:51], v[172:175], v[176:179], 0\n"
"s_waitcnt lgkmcnt(4)\n"
"v_mfma_f32_16x16x32_bf16 v[64:67], v[160:163], v[176:179], 0\n"
"ds_read_b64_tr_b16 v[172:173], v212 offset:9312\n"
"v_mfma_f32_16x16x32_bf16 v[68:71], v[160:163], v[180:183], 0\n"
"v_mfma_f32_16x16x32_bf16 v[72:75], v[160:163], v[184:187], 0\n"
"ds_read_b64_tr_b16 v[174:175], v212 offset:11616\n"
"v_mfma_f32_16x16x32_bf16 v[76:79], v[160:163], v[188:191], 0\n"
"s_waitcnt lgkmcnt(4)\n"
"v_mfma_f32_16x16x32_bf16 v[92:95], v[164:167], v[188:191], 0\n"
"ds_read_b64_tr_b16 v[160:161], v212 offset:256\n"
"v_mfma_f32_16x16x32_bf16 v[88:91], v[164:167], v[184:187], 0\n"
"v_mfma_f32_16x16x32_bf16 v[84:87], v[164:167], v[180:183], 0\n"
"ds_read_b64_tr_b16 v[162:163], v212 offset:2560\n"
"v_mfma_f32_16x16x32_bf16 v[80:83], v[164:167], v[176:179], 0\n"
"s_waitcnt vmcnt(8)\n"
"s_barrier\n"
"s_waitcnt lgkmcnt(4)\n"
"v_mfma_f32_16x16x32_bf16 v[96:99], v[168:171], v[176:179], 0\n"
"ds_read_b64_tr_b16 v[192:193], v208 offset:16384\n"
"v_mfma_f32_16x16x32_bf16 v[100:103], v[168:171], v[180:183], 0\n"
"ds_read_b64_tr_b16 v[194:195], v208 offset:20480\n"
"v_mfma_f32_16x16x32_bf16 v[104:107], v[168:171], v[184:187], 0\n"
"ds_read_b64_tr_b16 v[164:165], v212 offset:9472\n"
"v_mfma_f32_16x16x32_bf16 v[108:111], v[168:171], v[188:191], 0\n"
"ds_read_b64_tr_b16 v[166:167], v212 offset:11776\n"
"s_waitcnt lgkmcnt(6)\n"
"v_mfma_f32_16x16x32_bf16 v[124:127], v[172:175], v[188:191], 0\n"
"ds_read_b64_tr_b16 v[196:197], v209 offset:16384\n"
"v_mfma_f32_16x16x32_bf16 v[120:123], v[172:175], v[184:187], 0\n"
"ds_read_b64_tr_b16 v[198:199], v209 offset:20480\n"
"v_mfma_f32_16x16x32_bf16 v[116:119], v[172:175], v[180:183], 0\n"
"ds_read_b64_tr_b16 v[168:169], v212 offset:18432\n"
"v_mfma_f32_16x16x32_bf16 v[112:115], v[172:175], v[176:179], 0\n"
"ds_read_b64_tr_b16 v[170:171], v212 offset:20736\n"
"s_waitcnt lgkmcnt(8)\n"
"v_mfma_f32_16x16x32_bf16 v[128:131], v[160:163], v[176:179], 0\n"
"ds_read_b64_tr_b16 v[200:201], v210 offset:16384\n"
"v_mfma_f32_16x16x32_bf16 v[132:135], v[160:163], v[180:183], 0\n"
"ds_read_b64_tr_b16 v[202:203], v210 offset:20480\n"
"v_mfma_f32_16x16x32_bf16 v[136:139], v[160:163], v[184:187], 0\n"
"ds_read_b64_tr_b16 v[172:173], v212 offset:27648\n"
"v_mfma_f32_16x16x32_bf16 v[140:143], v[160:163], v[188:191], 0\n"
"ds_read_b64_tr_b16 v[174:175], v212 offset:29952\n"
"s_waitcnt lgkmcnt(8)\n"
"v_mfma_f32_16x16x32_bf16 v[156:159], v[164:167], v[188:191], 0\n"
"ds_read_b64_tr_b16 v[204:205], v211 offset:16384\n"
"v_mfma_f32_16x16x32_bf16 v[152:155], v[164:167], v[184:187], 0\n"
"ds_read_b64_tr_b16 v[206:207], v211 offset:20480\n"
"v_mfma_f32_16x16x32_bf16 v[148:151], v[164:167], v[180:183], 0\n"
"ds_read_b64_tr_b16 v[160:161], v212 offset:18464\n"
"v_mfma_f32_16x16x32_bf16 v[144:147], v[164:167], v[176:179], 0\n"
"ds_read_b64_tr_b16 v[162:163], v212 offset:20768\n"
"s_waitcnt vmcnt(4)\n"
"s_barrier\n"
"s_waitcnt lgkmcnt(8)\n"
"v_mfma_f32_16x16x32_bf16 v[0:3], v[168:171], v[192:195], v[0:3]\n"
"s_mov_b32 s60, 524288\n"
"s_add_u32 m0, s61, 0\n"
"s_nop 0\n"
"buffer_load_dwordx4 v214, s[20:23], s60 offen offset:0 sc1 lds\n"
"ds_read_b64_tr_b16 v[164:165], v212 offset:27680\n"
"v_mfma_f32_16x16x32_bf16 v[4:7], v[168:171], v[196:199], v[4:7]\n"
"ds_read_b64_tr_b16 v[166:167], v212 offset:29984\n"
"s_waitcnt lgkmcnt(8)\n"
"v_mfma_f32_16x16x32_bf16 v[8:11], v[168:171], v[200:203], v[8:11]\n"
"ds_read_b64_tr_b16 v[176:177], v208 offset:32768\n"
"s_waitcnt lgkmcnt(5)\n"
"v_mfma_f32_16x16x32_bf16 v[12:15], v[168:171], v[204:207], v[12:15]\n"
"ds_read_b64_tr_b16 v[178:179], v208 offset:36864\n"
"v_mfma_f32_16x16x32_bf16 v[28:31], v[172:175], v[204:207], v[28:31]\n"
"buffer_load_dwordx4 v215, s[20:23], s60 offen offset:1024 sc1 lds\n"
"ds_read_b64_tr_b16 v[168:169], v212 offset:18496\n"
"v_mfma_f32_16x16x32_bf16 v[24:27], v[172:175], v[200:203], v[24:27]\n"
"ds_read_b64_tr_b16 v[170:171], v212 offset:20800\n"
"v_mfma_f32_16x16x32_bf16 v[20:23], v[172:175], v[196:199], v[20:23]\n"
"ds_read_b64_tr_b16 v[180:181], v209 offset:32768\n"
"v_mfma_f32_16x16x32_bf16 v[16:19], v[172:175], v[192:195], v[16:19]\n"
"ds_read_b64_tr_b16 v[182:183], v209 offset:36864\n"
"s_waitcnt lgkmcnt(8)\n"
"v_mfma_f32_16x16x32_bf16 v[32:35], v[160:163], v[192:195], v[32:35]\n"
"buffer_load_dwordx4 v216, s[20:23], s60 offen offset:2048 sc1 lds\n"
"ds_read_b64_tr_b16 v[172:173], v212 offset:27712\n"
"v_mfma_f32_16x16x32_bf16 v[36:39], v[160:163], v[196:199], v[36:39]\n"
"ds_read_b64_tr_b16 v[174:175], v212 offset:30016\n"
"v_mfma_f32_16x16x32_bf16 v[40:43], v[160:163], v[200:203], v[40:43]\n"
"ds_read_b64_tr_b16 v[184:185], v210 offset:32768\n"
"v_mfma_f32_16x16x32_bf16 v[44:47], v[160:163], v[204:207], v[44:47]\n"
"ds_read_b64_tr_b16 v[186:187], v210 offset:36864\n"
"s_waitcnt lgkmcnt(10)\n"
"v_mfma_f32_16x16x32_bf16 v[60:63], v[164:167], v[204:207], v[60:63]\n"
"buffer_load_dwordx4 v217, s[20:23], s60 offen offset:3072 sc1 lds\n"
"ds_read_b64_tr_b16 v[160:161], v212 offset:18528\n"
"v_mfma_f32_16x16x32_bf16 v[56:59], v[164:167], v[200:203], v[56:59]\n"
"ds_read_b64_tr_b16 v[162:163], v212 offset:20832\n"
"v_mfma_f32_16x16x32_bf16 v[52:55], v[164:167], v[196:199], v[52:55]\n"
"ds_read_b64_tr_b16 v[188:189], v211 offset:32768\n"
"v_mfma_f32_16x16x32_bf16 v[48:51], v[164:167], v[192:195], v[48:51]\n"
"ds_read_b64_tr_b16 v[190:191], v211 offset:36864\n"
"s_waitcnt lgkmcnt(10)\n"
"v_mfma_f32_16x16x32_bf16 v[64:67], v[168:171], v[192:195], v[64:67]\n"
"ds_read_b64_tr_b16 v[164:165], v212 offset:27744\n"
"v_mfma_f32_16x16x32_bf16 v[68:71], v[168:171], v[196:199], v[68:71]\n"
"v_mfma_f32_16x16x32_bf16 v[72:75], v[168:171], v[200:203], v[72:75]\n"
"ds_read_b64_tr_b16 v[166:167], v212 offset:30048\n"
"v_mfma_f32_16x16x32_bf16 v[76:79], v[168:171], v[204:207], v[76:79]\n"
"s_waitcnt lgkmcnt(8)\n"
"v_mfma_f32_16x16x32_bf16 v[92:95], v[172:175], v[204:207], v[92:95]\n"
"ds_read_b64_tr_b16 v[168:169], v212 offset:18688\n"
"v_mfma_f32_16x16x32_bf16 v[88:91], v[172:175], v[200:203], v[88:91]\n"
"v_mfma_f32_16x16x32_bf16 v[84:87], v[172:175], v[196:199], v[84:87]\n"
"ds_read_b64_tr_b16 v[170:171], v212 offset:20992\n"
"v_mfma_f32_16x16x32_bf16 v[80:83], v[172:175], v[192:195], v[80:83]\n"
"s_waitcnt lgkmcnt(6)\n"
"v_mfma_f32_16x16x32_bf16 v[96:99], v[160:163], v[192:195], v[96:99]\n"
"ds_read_b64_tr_b16 v[172:173], v212 offset:27904\n"
"v_mfma_f32_16x16x32_bf16 v[100:103], v[160:163], v[196:199], v[100:103]\n"
"v_mfma_f32_16x16x32_bf16 v[104:107], v[160:163], v[200:203], v[104:107]\n"
"ds_read_b64_tr_b16 v[174:175], v212 offset:30208\n"
"v_mfma_f32_16x16x32_bf16 v[108:111], v[160:163], v[204:207], v[108:111]\n"
"s_waitcnt lgkmcnt(4)\n"
"v_mfma_f32_16x16x32_bf16 v[124:127], v[164:167], v[204:207], v[124:127]\n"
"ds_read_b64_tr_b16 v[160:161], v213 offset:0\n"
"v_mfma_f32_16x16x32_bf16 v[120:123], v[164:167], v[200:203], v[120:123]\n"
"v_mfma_f32_16x16x32_bf16 v[116:119], v[164:167], v[196:199], v[116:119]\n"
"ds_read_b64_tr_b16 v[162:163], v213 offset:2304\n"
"v_mfma_f32_16x16x32_bf16 v[112:115], v[164:167], v[192:195], v[112:115]\n"
"s_waitcnt lgkmcnt(4)\n"
"v_mfma_f32_16x16x32_bf16 v[128:131], v[168:171], v[192:195], v[128:131]\n"
"ds_read_b64_tr_b16 v[164:165], v213 offset:9216\n"
"v_mfma_f32_16x16x32_bf16 v[132:135], v[168:171], v[196:199], v[132:135]\n"
"v_mfma_f32_16x16x32_bf16 v[136:139], v[168:171], v[200:203], v[136:139]\n"
"ds_read_b64_tr_b16 v[166:167], v213 offset:11520\n"
"v_mfma_f32_16x16x32_bf16 v[140:143], v[168:171], v[204:207], v[140:143]\n"
"s_waitcnt lgkmcnt(4)\n"
"v_mfma_f32_16x16x32_bf16 v[156:159], v[172:175], v[204:207], v[156:159]\n"
"ds_read_b64_tr_b16 v[168:169], v213 offset:32\n"
"v_mfma_f32_16x16x32_bf16 v[152:155], v[172:175], v[200:203], v[152:155]\n"
"v_mfma_f32_16x16x32_bf16 v[148:151], v[172:175], v[196:199], v[148:151]\n"
"ds_read_b64_tr_b16 v[170:171], v213 offset:2336\n"
"v_mfma_f32_16x16x32_bf16 v[144:147], v[172:175], v[192:195], v[144:147]\n"
"s_waitcnt vmcnt(4)\n"
"s_barrier\n"
"s_waitcnt lgkmcnt(4)\n"
"v_mfma_f32_16x16x32_bf16 v[0:3], v[160:163], v[176:179], v[0:3]\n"
"s_mov_b32 s60, 655360\n"
"s_add_u32 m0, s61, 16384\n"
"s_nop 0\n"
"buffer_load_dwordx4 v214, s[20:23], s60 offen offset:0 sc1 lds\n"
"ds_read_b64_tr_b16 v[172:173], v213 offset:9248\n"
"v_mfma_f32_16x16x32_bf16 v[4:7], v[160:163], v[180:183], v[4:7]\n"
"ds_read_b64_tr_b16 v[174:175], v213 offset:11552\n"
"v_mfma_f32_16x16x32_bf16 v[8:11], v[160:163], v[184:187], v[8:11]\n"
"ds_read_b64_tr_b16 v[192:193], v208 offset:49152\n"
"v_mfma_f32_16x16x32_bf16 v[12:15], v[160:163], v[188:191], v[12:15]\n"
"ds_read_b64_tr_b16 v[194:195], v208 offset:53248\n"
"s_waitcnt lgkmcnt(6)\n"
"v_mfma_f32_16x16x32_bf16 v[28:31], v[164:167], v[188:191], v[28:31]\n"
"buffer_load_dwordx4 v215, s[20:23], s60 offen offset:1024 sc1 lds\n"
"ds_read_b64_tr_b16 v[160:161], v213 offset:64\n"
"v_mfma_f32_16x16x32_bf16 v[24:27], v[164:167], v[184:187], v[24:27]\n"
"ds_read_b64_tr_b16 v[162:163], v213 offset:2368\n"
"v_mfma_f32_16x16x32_bf16 v[20:23], v[164:167], v[180:183], v[20:23]\n"
"ds_read_b64_tr_b16 v[196:197], v209 offset:49152\n"
"v_mfma_f32_16x16x32_bf16 v[16:19], v[164:167], v[176:179], v[16:19]\n"
"ds_read_b64_tr_b16 v[198:199], v209 offset:53248\n"
"s_waitcnt lgkmcnt(8)\n"
"v_mfma_f32_16x16x32_bf16 v[32:35], v[168:171], v[176:179], v[32:35]\n"
"buffer_load_dwordx4 v216, s[20:23], s60 offen offset:2048 sc1 lds\n"
"ds_read_b64_tr_b16 v[164:165], v213 offset:9280\n"
"v_mfma_f32_16x16x32_bf16 v[36:39], v[168:171], v[180:183], v[36:39]\n"
"ds_read_b64_tr_b16 v[166:167], v213 offset:11584\n"
"v_mfma_f32_16x16x32_bf16 v[40:43], v[168:171], v[184:187], v[40:43]\n"
"ds_read_b64_tr_b16 v[200:201], v210 offset:49152\n"
"v_mfma_f32_16x16x32_bf16 v[44:47], v[168:171], v[188:191], v[44:47]\n"
"ds_read_b64_tr_b16 v[202:203], v210 offset:53248\n"
"s_waitcnt lgkmcnt(10)\n"
"v_mfma_f32_16x16x32_bf16 v[60:63], v[172:175], v[188:191], v[60:63]\n"
"buffer_load_dwordx4 v217, s[20:23], s60 offen offset:3072 sc1 lds\n"
"ds_read_b64_tr_b16 v[168:169], v213 offset:96\n"
"v_mfma_f32_16x16x32_bf16 v[56:59], v[172:175], v[184:187], v[56:59]\n"
"ds_read_b64_tr_b16 v[170:171], v213 offset:2400\n"
"v_mfma_f32_16x16x32_bf16 v[52:55], v[172:175], v[180:183], v[52:55]\n"
"ds_read_b64_tr_b16 v[204:205], v211 offset:49152\n"
"v_mfma_f32_16x16x32_bf16 v[48:51], v[172:175], v[176:179], v[48:51]\n"
"ds_read_b64_tr_b16 v[206:207], v211 offset:53248\n"
"s_waitcnt lgkmcnt(10)\n"
"v_mfma_f32_16x16x32_bf16 v[64:67], v[160:163], v[176:179], v[64:67]\n"
"ds_read_b64_tr_b16 v[172:173], v213 offset:9312\n"
"v_mfma_f32_16x16x32_bf16 v[68:71], v[160:163], v[180:183], v[68:71]\n"
"v_mfma_f32_16x16x32_bf16 v[72:75], v[160:163], v[184:187], v[72:75]\n"
"ds_read_b64_tr_b16 v[174:175], v213 offset:11616\n"
"v_mfma_f32_16x16x32_bf16 v[76:79], v[160:163], v[188:191], v[76:79]\n"
"s_waitcnt lgkmcnt(8)\n"
"v_mfma_f32_16x16x32_bf16 v[92:95], v[164:167], v[188:191], v[92:95]\n"
"ds_read_b64_tr_b16 v[160:161], v213 offset:256\n"
"v_mfma_f32_16x16x32_bf16 v[88:91], v[164:167], v[184:187], v[88:91]\n"
"v_mfma_f32_16x16x32_bf16 v[84:87], v[164:167], v[180:183], v[84:87]\n"
"ds_read_b64_tr_b16 v[162:163], v213 offset:2560\n"
"v_mfma_f32_16x16x32_bf16 v[80:83], v[164:167], v[176:179], v[80:83]\n"
"s_waitcnt lgkmcnt(6)\n"
"v_mfma_f32_16x16x32_bf16 v[96:99], v[168:171], v[176:179], v[96:99]\n"
"ds_read_b64_tr_b16 v[164:165], v213 offset:9472\n"
"v_mfma_f32_16x16x32_bf16 v[100:103], v[168:171], v[180:183], v[100:103]\n"
"v_mfma_f32_16x16x32_bf16 v[104:107], v[168:171], v[184:187], v[104:107]\n"
"ds_read_b64_tr_b16 v[166:167], v213 offset:11776\n"
"v_mfma_f32_16x16x32_bf16 v[108:111], v[168:171], v[188:191], v[108:111]\n"
"s_waitcnt lgkmcnt(4)\n"
"v_mfma_f32_16x16x32_bf16 v[124:127], v[172:175], v[188:191], v[124:127]\n"
"ds_read_b64_tr_b16 v[168:169], v213 offset:18432\n"
"v_mfma_f32_16x16x32_bf16 v[120:123], v[172:175], v[184:187], v[120:123]\n"
"v_mfma_f32_16x16x32_bf16 v[116:119], v[172:175], v[180:183], v[116:119]\n"
"ds_read_b64_tr_b16 v[170:171], v213 offset:20736\n"
"v_mfma_f32_16x16x32_bf16 v[112:115], v[172:175], v[176:179], v[112:115]\n"
"s_waitcnt lgkmcnt(4)\n"
"v_mfma_f32_16x16x32_bf16 v[128:131], v[160:163], v[176:179], v[128:131]\n"
"ds_read_b64_tr_b16 v[172:173], v213 offset:27648\n"
"v_mfma_f32_16x16x32_bf16 v[132:135], v[160:163], v[180:183], v[132:135]\n"
"v_mfma_f32_16x16x32_bf16 v[136:139], v[160:163], v[184:187], v[136:139]\n"
"ds_read_b64_tr_b16 v[174:175], v213 offset:29952\n"
"v_mfma_f32_16x16x32_bf16 v[140:143], v[160:163], v[188:191], v[140:143]\n"
"s_waitcnt lgkmcnt(4)\n"
"v_mfma_f32_16x16x32_bf16 v[156:159], v[164:167], v[188:191], v[156:159]\n"
"ds_read_b64_tr_b16 v[160:161], v213 offset:18464\n"
"v_mfma_f32_16x16x32_bf16 v[152:155], v[164:167], v[184:187], v[152:155]\n"
"v_mfma_f32_16x16x32_bf16 v[148:151], v[164:167], v[180:183], v[148:151]\n"
"ds_read_b64_tr_b16 v[162:163], v213 offset:20768\n"
"v_mfma_f32_16x16x32_bf16 v[144:147], v[164:167], v[176:179], v[144:147]\n"
"s_waitcnt vmcnt(4)\n"
"s_barrier\n"
"s_waitcnt lgkmcnt(4)\n"
"v_mfma_f32_16x16x32_bf16 v[0:3], v[168:171], v[192:195], v[0:3]\n"
"s_mov_b32 s60, 786432\n"
"s_add_u32 m0, s61, 32768\n"
"s_nop 0\n"
"buffer_load_dwordx4 v214, s[20:23], s60 offen offset:0 sc1 lds\n"
"ds_read_b64_tr_b16 v[164:165], v213 offset:27680\n"
"v_mfma_f32_16x16x32_bf16 v[4:7], v[168:171], v[196:199], v[4:7]\n"
"ds_read_b64_tr_b16 v[166:167], v213 offset:29984\n"
"v_mfma_f32_16x16x32_bf16 v[8:11], v[168:171], v[200:203], v[8:11]\n"
"ds_read_b64_tr_b16 v[176:177], v208 offset:0\n"
"v_mfma_f32_16x16x32_bf16 v[12:15], v[168:171], v[204:207], v[12:15]\n"
"ds_read_b64_tr_b16 v[178:179], v208 offset:4096\n"
"s_waitcnt lgkmcnt(6)\n"
"v_mfma_f32_16x16x32_bf16 v[28:31], v[172:175], v[204:207], v[28:31]\n"
"buffer_load_dwordx4 v215, s[20:23], s60 offen offset:1024 sc1 lds\n"
"ds_read_b64_tr_b16 v[168:169], v213 offset:18496\n"
"v_mfma_f32_16x16x32_bf16 v[24:27], v[172:175], v[200:203], v[24:27]\n"
"ds_read_b64_tr_b16 v[170:171], v213 offset:20800\n"
"v_mfma_f32_16x16x32_bf16 v[20:23], v[172:175], v[196:199], v[20:23]\n"
"ds_read_b64_tr_b16 v[180:181], v209 offset:0\n"
"v_mfma_f32_16x16x32_bf16 v[16:19], v[172:175], v[192:195], v[16:19]\n"
"ds_read_b64_tr_b16 v[182:183], v209 offset:4096\n"
"s_waitcnt lgkmcnt(8)\n"
"v_mfma_f32_16x16x32_bf16 v[32:35], v[160:163], v[192:195], v[32:35]\n"
"buffer_load_dwordx4 v216, s[20:23], s60 offen offset:2048 sc1 lds\n"
"ds_read_b64_tr_b16 v[172:173], v213 offset:27712\n"
"v_mfma_f32_16x16x32_bf16 v[36:39], v[160:163], v[196:199], v[36:39]\n"
"ds_read_b64_tr_b16 v[174:175], v213 offset:30016\n"
"v_mfma_f32_16x16x32_bf16 v[40:43], v[160:163], v[200:203], v[40:43]\n"
"ds_read_b64_tr_b16 v[184:185], v210 offset:0\n"
"v_mfma_f32_16x16x32_bf16 v[44:47], v[160:163], v[204:207], v[44:47]\n"
"ds_read_b64_tr_b16 v[186:187], v210 offset:4096\n"
"s_waitcnt lgkmcnt(10)\n"
"v_mfma_f32_16x16x32_bf16 v[60:63], v[164:167], v[204:207], v[60:63]\n"
"buffer_load_dwordx4 v217, s[20:23], s60 offen offset:3072 sc1 lds\n"
"ds_read_b64_tr_b16 v[160:161], v213 offset:18528\n"
"v_mfma_f32_16x16x32_bf16 v[56:59], v[164:167], v[200:203], v[56:59]\n"
"ds_read_b64_tr_b16 v[162:163], v213 offset:20832\n"
"v_mfma_f32_16x16x32_bf16 v[52:55], v[164:167], v[196:199], v[52:55]\n"
"ds_read_b64_tr_b16 v[188:189], v211 offset:0\n"
"v_mfma_f32_16x16x32_bf16 v[48:51], v[164:167], v[192:195], v[48:51]\n"
"ds_read_b64_tr_b16 v[190:191], v211 offset:4096\n"
"s_waitcnt lgkmcnt(10)\n"
"v_mfma_f32_16x16x32_bf16 v[64:67], v[168:171], v[192:195], v[64:67]\n"
"ds_read_b64_tr_b16 v[164:165], v213 offset:27744\n"
"v_mfma_f32_16x16x32_bf16 v[68:71], v[168:171], v[196:199], v[68:71]\n"
"v_mfma_f32_16x16x32_bf16 v[72:75], v[168:171], v[200:203], v[72:75]\n"
"ds_read_b64_tr_b16 v[166:167], v213 offset:30048\n"
"v_mfma_f32_16x16x32_bf16 v[76:79], v[168:171], v[204:207], v[76:79]\n"
"s_waitcnt lgkmcnt(8)\n"
"v_mfma_f32_16x16x32_bf16 v[92:95], v[172:175], v[204:207], v[92:95]\n"
"ds_read_b64_tr_b16 v[168:169], v213 offset:18688\n"
"v_mfma_f32_16x16x32_bf16 v[88:91], v[172:175], v[200:203], v[88:91]\n"
"v_mfma_f32_16x16x32_bf16 v[84:87], v[172:175], v[196:199], v[84:87]\n"
"ds_read_b64_tr_b16 v[170:171], v213 offset:20992\n"
"v_mfma_f32_16x16x32_bf16 v[80:83], v[172:175], v[192:195], v[80:83]\n"
"s_waitcnt lgkmcnt(6)\n"
"v_mfma_f32_16x16x32_bf16 v[96:99], v[160:163], v[192:195], v[96:99]\n"
"ds_read_b64_tr_b16 v[172:173], v213 offset:27904\n"
"v_mfma_f32_16x16x32_bf16 v[100:103], v[160:163], v[196:199], v[100:103]\n"
"v_mfma_f32_16x16x32_bf16 v[104:107], v[160:163], v[200:203], v[104:107]\n"
"ds_read_b64_tr_b16 v[174:175], v213 offset:30208\n"
"v_mfma_f32_16x16x32_bf16 v[108:111], v[160:163], v[204:207], v[108:111]\n"
"s_waitcnt lgkmcnt(4)\n"
"v_mfma_f32_16x16x32_bf16 v[124:127], v[164:167], v[204:207], v[124:127]\n"
"ds_read_b64_tr_b16 v[160:161], v212 offset:0\n"
"v_mfma_f32_16x16x32_bf16 v[120:123], v[164:167], v[200:203], v[120:123]\n"
"v_mfma_f32_16x16x32_bf16 v[116:119], v[164:167], v[196:199], v[116:119]\n"
"ds_read_b64_tr_b16 v[162:163], v212 offset:2304\n"
"v_mfma_f32_16x16x32_bf16 v[112:115], v[164:167], v[192:195], v[112:115]\n"
"s_waitcnt lgkmcnt(4)\n"
"v_mfma_f32_16x16x32_bf16 v[128:131], v[168:171], v[192:195], v[128:131]\n"
"ds_read_b64_tr_b16 v[164:165], v212 offset:9216\n"
"v_mfma_f32_16x16x32_bf16 v[132:135], v[168:171], v[196:199], v[132:135]\n"
"v_mfma_f32_16x16x32_bf16 v[136:139], v[168:171], v[200:203], v[136:139]\n"
"ds_read_b64_tr_b16 v[166:167], v212 offset:11520\n"
"v_mfma_f32_16x16x32_bf16 v[140:143], v[168:171], v[204:207], v[140:143]\n"
"s_waitcnt lgkmcnt(4)\n"
"v_mfma_f32_16x16x32_bf16 v[156:159], v[172:175], v[204:207], v[156:159]\n"
"ds_read_b64_tr_b16 v[168:169], v212 offset:32\n"
"v_mfma_f32_16x16x32_bf16 v[152:155], v[172:175], v[200:203], v[152:155]\n"
"v_mfma_f32_16x16x32_bf16 v[148:151], v[172:175], v[196:199], v[148:151]\n"
"ds_read_b64_tr_b16 v[170:171], v212 offset:2336\n"
"v_mfma_f32_16x16x32_bf16 v[144:147], v[172:175], v[192:195], v[144:147]\n"
"s_waitcnt vmcnt(4)\n"
"s_barrier\n"
"s_waitcnt lgkmcnt(4)\n"
"v_mfma_f32_16x16x32_bf16 v[0:3], v[160:163], v[176:179], v[0:3]\n"
"s_mov_b32 s60, 917504\n"
"s_add_u32 m0, s61, 49152\n"
"s_nop 0\n"
"buffer_load_dwordx4 v214, s[20:23], s60 offen offset:0 sc1 lds\n"
"ds_read_b64_tr_b16 v[172:173], v212 offset:9248\n"
"v_mfma_f32_16x16x32_bf16 v[4:7], v[160:163], v[180:183], v[4:7]\n"
"ds_read_b64_tr_b16 v[174:175], v212 offset:11552\n"
"v_mfma_f32_16x16x32_bf16 v[8:11], v[160:163], v[184:187], v[8:11]\n"
"ds_read_b64_tr_b16 v[192:193], v208 offset:16384\n"
"v_mfma_f32_16x16x32_bf16 v[12:15], v[160:163], v[188:191], v[12:15]\n"
"ds_read_b64_tr_b16 v[194:195], v208 offset:20480\n"
"s_waitcnt lgkmcnt(6)\n"
"v_mfma_f32_16x16x32_bf16 v[28:31], v[164:167], v[188:191], v[28:31]\n"
"buffer_load_dwordx4 v215, s[20:23], s60 offen offset:1024 sc1 lds\n"
"ds_read_b64_tr_b16 v[160:161], v212 offset:64\n"
"v_mfma_f32_16x16x32_bf16 v[24:27], v[164:167], v[184:187], v[24:27]\n"
"ds_read_b64_tr_b16 v[162:163], v212 offset:2368\n"
"v_mfma_f32_16x16x32_bf16 v[20:23], v[164:167], v[180:183], v[20:23]\n"
"ds_read_b64_tr_b16 v[196:197], v209 offset:16384\n"
"v_mfma_f32_16x16x32_bf16 v[16:19], v[164:167], v[176:179], v[16:19]\n"
"ds_read_b64_tr_b16 v[198:199], v209 offset:20480\n"
"s_waitcnt lgkmcnt(8)\n"
"v_mfma_f32_16x16x32_bf16 v[32:35], v[168:171], v[176:179], v[32:35]\n"
"buffer_load_dwordx4 v216, s[20:23], s60 offen offset:2048 sc1 lds\n"
"ds_read_b64_tr_b16 v[164:165], v212 offset:9280\n"
"v_mfma_f32_16x16x32_bf16 v[36:39], v[168:171], v[180:183], v[36:39]\n"
"ds_read_b64_tr_b16 v[166:167], v212 offset:11584\n"
"v_mfma_f32_16x16x32_bf16 v[40:43], v[168:171], v[184:187], v[40:43]\n"
"ds_read_b64_tr_b16 v[200:201], v210 offset:16384\n"
"v_mfma_f32_16x16x32_bf16 v[44:47], v[168:171], v[188:191], v[44:47]\n"
"ds_read_b64_tr_b16 v[202:203], v210 offset:20480\n"
"s_waitcnt lgkmcnt(10)\n"
"v_mfma_f32_16x16x32_bf16 v[60:63], v[172:175], v[188:191], v[60:63]\n"
"buffer_load_dwordx4 v217, s[20:23], s60 offen offset:3072 sc1 lds\n"
"ds_read_b64_tr_b16 v[168:169], v212 offset:96\n"
"v_mfma_f32_16x16x32_bf16 v[56:59], v[172:175], v[184:187], v[56:59]\n"
"ds_read_b64_tr_b16 v[170:171], v212 offset:2400\n"
"v_mfma_f32_16x16x32_bf16 v[52:55], v[172:175], v[180:183], v[52:55]\n"
"ds_read_b64_tr_b16 v[204:205], v211 offset:16384\n"
"v_mfma_f32_16x16x32_bf16 v[48:51], v[172:175], v[176:179], v[48:51]\n"
"ds_read_b64_tr_b16 v[206:207], v211 offset:20480\n"
"s_waitcnt lgkmcnt(10)\n"
"v_mfma_f32_16x16x32_bf16 v[64:67], v[160:163], v[176:179], v[64:67]\n"
"ds_read_b64_tr_b16 v[172:173], v212 offset:9312\n"
"v_mfma_f32_16x16x32_bf16 v[68:71], v[160:163], v[180:183], v[68:71]\n"
"v_mfma_f32_16x16x32_bf16 v[72:75], v[160:163], v[184:187], v[72:75]\n"
"ds_read_b64_tr_b16 v[174:175], v212 offset:11616\n"
"v_mfma_f32_16x16x32_bf16 v[76:79], v[160:163], v[188:191], v[76:79]\n"
"s_waitcnt lgkmcnt(8)\n"
"v_mfma_f32_16x16x32_bf16 v[92:95], v[164:167], v[188:191], v[92:95]\n"
"ds_read_b64_tr_b16 v[160:161], v212 offset:256\n"
"v_mfma_f32_16x16x32_bf16 v[88:91], v[164:167], v[184:187], v[88:91]\n"
"v_mfma_f32_16x16x32_bf16 v[84:87], v[164:167], v[180:183], v[84:87]\n"
"ds_read_b64_tr_b16 v[162:163], v212 offset:2560\n"
"v_mfma_f32_16x16x32_bf16 v[80:83], v[164:167], v[176:179], v[80:83]\n"
"s_waitcnt lgkmcnt(6)\n"
"v_mfma_f32_16x16x32_bf16 v[96:99], v[168:171], v[176:179], v[96:99]\n"
"ds_read_b64_tr_b16 v[164:165], v212 offset:9472\n"
"v_mfma_f32_16x16x32_bf16 v[100:103], v[168:171], v[180:183], v[100:103]\n"
"v_mfma_f32_16x16x32_bf16 v[104:107], v[168:171], v[184:187], v[104:107]\n"
"ds_read_b64_tr_b16 v[166:167], v212 offset:11776\n"
"v_mfma_f32_16x16x32_bf16 v[108:111], v[168:171], v[188:191], v[108:111]\n"
"s_waitcnt lgkmcnt(4)\n"
"v_mfma_f32_16x16x32_bf16 v[124:127], v[172:175], v[188:191], v[124:127]\n"
"ds_read_b64_tr_b16 v[168:169], v212 offset:18432\n"
"v_mfma_f32_16x16x32_bf16 v[120:123], v[172:175], v[184:187], v[120:123]\n"
"v_mfma_f32_16x16x32_bf16 v[116:119], v[172:175], v[180:183], v[116:119]\n"
"ds_read_b64_tr_b16 v[170:171], v212 offset:20736\n"
"v_mfma_f32_16x16x32_bf16 v[112:115], v[172:175], v[176:179], v[112:115]\n"
"s_waitcnt lgkmcnt(4)\n"
"v_mfma_f32_16x16x32_bf16 v[128:131], v[160:163], v[176:179], v[128:131]\n"
"ds_read_b64_tr_b16 v[172:173], v212 offset:27648\n"
"v_mfma_f32_16x16x32_bf16 v[132:135], v[160:163], v[180:183], v[132:135]\n"
"v_mfma_f32_16x16x32_bf16 v[136:139], v[160:163], v[184:187], v[136:139]\n"
"ds_read_b64_tr_b16 v[174:175], v212 offset:29952\n"
"v_mfma_f32_16x16x32_bf16 v[140:143], v[160:163], v[188:191], v[140:143]\n"
"s_waitcnt lgkmcnt(4)\n"
"v_mfma_f32_16x16x32_bf16 v[156:159], v[164:167], v[188:191], v[156:159]\n"
"ds_read_b64_tr_b16 v[160:161], v212 offset:18464\n"
"v_mfma_f32_16x16x32_bf16 v[152:155], v[164:167], v[184:187], v[152:155]\n"
"v_mfma_f32_16x16x32_bf16 v[148:151], v[164:167], v[180:183], v[148:151]\n"
"ds_read_b64_tr_b16 v[162:163], v212 offset:20768\n"
"v_mfma_f32_16x16x32_bf16 v[144:147], v[164:167], v[176:179], v[144:147]\n"
"s_waitcnt vmcnt(4)\n"
"s_barrier\n"
"s_waitcnt lgkmcnt(4)\n"
"v_mfma_f32_16x16x32_bf16 v[0:3], v[168:171], v[192:195], v[0:3]\n"
"s_mov_b32 s60, 1048576\n"
"s_add_u32 m0, s61, 0\n"
"s_nop 0\n"
"buffer_load_dwordx4 v214, s[20:23], s60 offen offset:0 sc1 lds\n"
"ds_read_b64_tr_b16 v[164:165], v212 offset:27680\n"
"v_mfma_f32_16x16x32_bf16 v[4:7], v[168:171], v[196:199], v[4:7]\n"
"ds_read_b64_tr_b16 v[166:167], v212 offset:29984\n"
"v_mfma_f32_16x16x32_bf16 v[8:11], v[168:171], v[200:203], v[8:11]\n"
"ds_read_b64_tr_b16 v[176:177], v208 offset:32768\n"
"v_mfma_f32_16x16x32_bf16 v[12:15], v[168:171], v[204:207], v[12:15]\n"
"ds_read_b64_tr_b16 v[178:179], v208 offset:36864\n"
"s_waitcnt lgkmcnt(6)\n"
"v_mfma_f32_16x16x32_bf16 v[28:31], v[172:175], v[204:207], v[28:31]\n"
"buffer_load_dwordx4 v215, s[20:23], s60 offen offset:1024 sc1 lds\n"
"ds_read_b64_tr_b16 v[168:169], v212 offset:18496\n"
"v_mfma_f32_16x16x32_bf16 v[24:27], v[172:175], v[200:203], v[24:27]\n"
"ds_read_b64_tr_b16 v[170:171], v212 offset:20800\n"
"v_mfma_f32_16x16x32_bf16 v[20:23], v[172:175], v[196:199], v[20:23]\n"
"ds_read_b64_tr_b16 v[180:181], v209 offset:32768\n"
"v_mfma_f32_16x16x32_bf16 v[16:19], v[172:175], v[192:195], v[16:19]\n"
"ds_read_b64_tr_b16 v[182:183], v209 offset:36864\n"
"s_waitcnt lgkmcnt(8)\n"
"v_mfma_f32_16x16x32_bf16 v[32:35], v[160:163], v[192:195], v[32:35]\n"
"buffer_load_dwordx4 v216, s[20:23], s60 offen offset:2048 sc1 lds\n"
"ds_read_b64_tr_b16 v[172:173], v212 offset:27712\n"
"v_mfma_f32_16x16x32_bf16 v[36:39], v[160:163], v[196:199], v[36:39]\n"
"ds_read_b64_tr_b16 v[174:175], v212 offset:30016\n"
"v_mfma_f32_16x16x32_bf16 v[40:43], v[160:163], v[200:203], v[40:43]\n"
"ds_read_b64_tr_b16 v[184:185], v210 offset:32768\n"
"v_mfma_f32_16x16x32_bf16 v[44:47], v[160:163], v[204:207], v[44:47]\n"
"ds_read_b64_tr_b16 v[186:187], v210 offset:36864\n"
"s_waitcnt lgkmcnt(10)\n"
"v_mfma_f32_16x16x32_bf16 v[60:63], v[164:167], v[204:207], v[60:63]\n"
"buffer_load_dwordx4 v217, s[20:23], s60 offen offset:3072 sc1 lds\n"
"ds_read_b64_tr_b16 v[160:161], v212 offset:18528\n"
"v_mfma_f32_16x16x32_bf16 v[56:59], v[164:167], v[200:203], v[56:59]\n"
"ds_read_b64_tr_b16 v[162:163], v212 offset:20832\n"
"v_mfma_f32_16x16x32_bf16 v[52:55], v[164:167], v[196:199], v[52:55]\n"
"ds_read_b64_tr_b16 v[188:189], v211 offset:32768\n"
"v_mfma_f32_16x16x32_bf16 v[48:51], v[164:167], v[192:195], v[48:51]\n"
"ds_read_b64_tr_b16 v[190:191], v211 offset:36864\n"
"s_waitcnt lgkmcnt(10)\n"
"v_mfma_f32_16x16x32_bf16 v[64:67], v[168:171], v[192:195], v[64:67]\n"
"ds_read_b64_tr_b16 v[164:165], v212 offset:27744\n"
"v_mfma_f32_16x16x32_bf16 v[68:71], v[168:171], v[196:199], v[68:71]\n"
"v_mfma_f32_16x16x32_bf16 v[72:75], v[168:171], v[200:203], v[72:75]\n"
"ds_read_b64_tr_b16 v[166:167], v212 offset:30048\n"
"v_mfma_f32_16x16x32_bf16 v[76:79], v[168:171], v[204:207], v[76:79]\n"
"s_waitcnt lgkmcnt(8)\n"
"v_mfma_f32_16x16x32_bf16 v[92:95], v[172:175], v[204:207], v[92:95]\n"
"ds_read_b64_tr_b16 v[168:169], v212 offset:18688\n"
"v_mfma_f32_16x16x32_bf16 v[88:91], v[172:175], v[200:203], v[88:91]\n"
"v_mfma_f32_16x16x32_bf16 v[84:87], v[172:175], v[196:199], v[84:87]\n"
"ds_read_b64_tr_b16 v[170:171], v212 offset:20992\n"
"v_mfma_f32_16x16x32_bf16 v[80:83], v[172:175], v[192:195], v[80:83]\n"
"s_waitcnt lgkmcnt(6)\n"
"v_mfma_f32_16x16x32_bf16 v[96:99], v[160:163], v[192:195], v[96:99]\n"
"ds_read_b64_tr_b16 v[172:173], v212 offset:27904\n"
"v_mfma_f32_16x16x32_bf16 v[100:103], v[160:163], v[196:199], v[100:103]\n"
"v_mfma_f32_16x16x32_bf16 v[104:107], v[160:163], v[200:203], v[104:107]\n"
"ds_read_b64_tr_b16 v[174:175], v212 offset:30208\n"
"v_mfma_f32_16x16x32_bf16 v[108:111], v[160:163], v[204:207], v[108:111]\n"
"s_waitcnt lgkmcnt(4)\n"
"v_mfma_f32_16x16x32_bf16 v[124:127], v[164:167], v[204:207], v[124:127]\n"
"ds_read_b64_tr_b16 v[160:161], v213 offset:0\n"
"v_mfma_f32_16x16x32_bf16 v[120:123], v[164:167], v[200:203], v[120:123]\n"
"v_mfma_f32_16x16x32_bf16 v[116:119], v[164:167], v[196:199], v[116:119]\n"
"ds_read_b64_tr_b16 v[162:163], v213 offset:2304\n"
"v_mfma_f32_16x16x32_bf16 v[112:115], v[164:167], v[192:195], v[112:115]\n"
"s_waitcnt lgkmcnt(4)\n"
"v_mfma_f32_16x16x32_bf16 v[128:131], v[168:171], v[192:195], v[128:131]\n"
"ds_read_b64_tr_b16 v[164:165], v213 offset:9216\n"
"v_mfma_f32_16x16x32_bf16 v[132:135], v[168:171], v[196:199], v[132:135]\n"
"v_mfma_f32_16x16x32_bf16 v[136:139], v[168:171], v[200:203], v[136:139]\n"
"ds_read_b64_tr_b16 v[166:167], v213 offset:11520\n"
"v_mfma_f32_16x16x32_bf16 v[140:143], v[168:171], v[204:207], v[140:143]\n"
"s_waitcnt lgkmcnt(4)\n"
"v_mfma_f32_16x16x32_bf16 v[156:159], v[172:175], v[204:207], v[156:159]\n"
"ds_read_b64_tr_b16 v[168:169], v213 offset:32\n"
"v_mfma_f32_16x16x32_bf16 v[152:155], v[172:175], v[200:203], v[152:155]\n"
"v_mfma_f32_16x16x32_bf16 v[148:151], v[172:175], v[196:199], v[148:151]\n"
"ds_read_b64_tr_b16 v[170:171], v213 offset:2336\n"
"v_mfma_f32_16x16x32_bf16 v[144:147], v[172:175], v[192:195], v[144:147]\n"
"s_waitcnt vmcnt(4)\n"
"s_barrier\n"
"s_waitcnt lgkmcnt(4)\n"
"v_mfma_f32_16x16x32_bf16 v[0:3], v[160:163], v[176:179], v[0:3]\n"
"s_mov_b32 s60, 1179648\n"
"s_add_u32 m0, s61, 16384\n"
"s_nop 0\n"
"buffer_load_dwordx4 v214, s[20:23], s60 offen offset:0 sc1 lds\n"
"ds_read_b64_tr_b16 v[172:173], v213 offset:9248\n"
"v_mfma_f32_16x16x32_bf16 v[4:7], v[160:163], v[180:183], v[4:7]\n"
"ds_read_b64_tr_b16 v[174:175], v213 offset:11552\n"
"v_mfma_f32_16x16x32_bf16 v[8:11], v[160:163], v[184:187], v[8:11]\n"
"ds_read_b64_tr_b16 v[192:193], v208 offset:49152\n"
"v_mfma_f32_16x16x32_bf16 v[12:15], v[160:163], v[188:191], v[12:15]\n"
"ds_read_b64_tr_b16 v[194:195], v208 offset:53248\n"
"s_waitcnt lgkmcnt(6)\n"
"v_mfma_f32_16x16x32_bf16 v[28:31], v[164:167], v[188:191], v[28:31]\n"
"buffer_load_dwordx4 v215, s[20:23], s60 offen offset:1024 sc1 lds\n"
"ds_read_b64_tr_b16 v[160:161], v213 offset:64\n"
"v_mfma_f32_16x16x32_bf16 v[24:27], v[164:167], v[184:187], v[24:27]\n"
"ds_read_b64_tr_b16 v[162:163], v213 offset:2368\n"
"v_mfma_f32_16x16x32_bf16 v[20:23], v[164:167], v[180:183], v[20:23]\n"
"ds_read_b64_tr_b16 v[196:197], v209 offset:49152\n"
"v_mfma_f32_16x16x32_bf16 v[16:19], v[164:167], v[176:179], v[16:19]\n"
"ds_read_b64_tr_b16 v[198:199], v209 offset:53248\n"
"s_waitcnt lgkmcnt(8)\n"
"v_mfma_f32_16x16x32_bf16 v[32:35], v[168:171], v[176:179], v[32:35]\n"
"buffer_load_dwordx4 v216, s[20:23], s60 offen offset:2048 sc1 lds\n"
"ds_read_b64_tr_b16 v[164:165], v213 offset:9280\n"
"v_mfma_f32_16x16x32_bf16 v[36:39], v[168:171], v[180:183], v[36:39]\n"
"ds_read_b64_tr_b16 v[166:167], v213 offset:11584\n"
"v_mfma_f32_16x16x32_bf16 v[40:43], v[168:171], v[184:187], v[40:43]\n"
"ds_read_b64_tr_b16 v[200:201], v210 offset:49152\n"
"v_mfma_f32_16x16x32_bf16 v[44:47], v[168:171], v[188:191], v[44:47]\n"
"ds_read_b64_tr_b16 v[202:203], v210 offset:53248\n"
"s_waitcnt lgkmcnt(10)\n"
"v_mfma_f32_16x16x32_bf16 v[60:63], v[172:175], v[188:191], v[60:63]\n"
"buffer_load_dwordx4 v217, s[20:23], s60 offen offset:3072 sc1 lds\n"
"ds_read_b64_tr_b16 v[168:169], v213 offset:96\n"
"v_mfma_f32_16x16x32_bf16 v[56:59], v[172:175], v[184:187], v[56:59]\n"
"ds_read_b64_tr_b16 v[170:171], v213 offset:2400\n"
"v_mfma_f32_16x16x32_bf16 v[52:55], v[172:175], v[180:183], v[52:55]\n"
"ds_read_b64_tr_b16 v[204:205], v211 offset:49152\n"
"v_mfma_f32_16x16x32_bf16 v[48:51], v[172:175], v[176:179], v[48:51]\n"
"ds_read_b64_tr_b16 v[206:207], v211 offset:53248\n"
"s_waitcnt lgkmcnt(10)\n"
"v_mfma_f32_16x16x32_bf16 v[64:67], v[160:163], v[176:179], v[64:67]\n"
"ds_read_b64_tr_b16 v[172:173], v213 offset:9312\n"
"v_mfma_f32_16x16x32_bf16 v[68:71], v[160:163], v[180:183], v[68:71]\n"
"v_mfma_f32_16x16x32_bf16 v[72:75], v[160:163], v[184:187], v[72:75]\n"
"ds_read_b64_tr_b16 v[174:175], v213 offset:11616\n"
"v_mfma_f32_16x16x32_bf16 v[76:79], v[160:163], v[188:191], v[76:79]\n"
"s_waitcnt lgkmcnt(8)\n"
"v_mfma_f32_16x16x32_bf16 v[92:95], v[164:167], v[188:191], v[92:95]\n"
"ds_read_b64_tr_b16 v[160:161], v213 offset:256\n"
"v_mfma_f32_16x16x32_bf16 v[88:91], v[164:167], v[184:187], v[88:91]\n"
"v_mfma_f32_16x16x32_bf16 v[84:87], v[164:167], v[180:183], v[84:87]\n"
"ds_read_b64_tr_b16 v[162:163], v213 offset:2560\n"
"v_mfma_f32_16x16x32_bf16 v[80:83], v[164:167], v[176:179], v[80:83]\n"
"s_waitcnt lgkmcnt(6)\n"
"v_mfma_f32_16x16x32_bf16 v[96:99], v[168:171], v[176:179], v[96:99]\n"
"ds_read_b64_tr_b16 v[164:165], v213 offset:9472\n"
"v_mfma_f32_16x16x32_bf16 v[100:103], v[168:171], v[180:183], v[100:103]\n"
"v_mfma_f32_16x16x32_bf16 v[104:107], v[168:171], v[184:187], v[104:107]\n"
"ds_read_b64_tr_b16 v[166:167], v213 offset:11776\n"
"v_mfma_f32_16x16x32_bf16 v[108:111], v[168:171], v[188:191], v[108:111]\n"
"s_waitcnt lgkmcnt(4)\n"
"v_mfma_f32_16x16x32_bf16 v[124:127], v[172:175], v[188:191], v[124:127]\n"
"ds_read_b64_tr_b16 v[168:169], v213 offset:18432\n"
"v_mfma_f32_16x16x32_bf16 v[120:123], v[172:175], v[184:187], v[120:123]\n"
"v_mfma_f32_16x16x32_bf16 v[116:119], v[172:175], v[180:183], v[116:119]\n"
"ds_read_b64_tr_b16 v[170:171], v213 offset:20736\n"
"v_mfma_f32_16x16x32_bf16 v[112:115], v[172:175], v[176:179], v[112:115]\n"
"s_waitcnt lgkmcnt(4)\n"
"v_mfma_f32_16x16x32_bf16 v[128:131], v[160:163], v[176:179], v[128:131]\n"
"ds_read_b64_tr_b16 v[172:173], v213 offset:27648\n"
"v_mfma_f32_16x16x32_bf16 v[132:135], v[160:163], v[180:183], v[132:135]\n"
"v_mfma_f32_16x16x32_bf16 v[136:139], v[160:163], v[184:187], v[136:139]\n"
"ds_read_b64_tr_b16 v[174:175], v213 offset:29952\n"
"v_mfma_f32_16x16x32_bf16 v[140:143], v[160:163], v[188:191], v[140:143]\n"
"s_waitcnt lgkmcnt(4)\n"
"v_mfma_f32_16x16x32_bf16 v[156:159], v[164:167], v[188:191], v[156:159]\n"
"ds_read_b64_tr_b16 v[160:161], v213 offset:18464\n"
"v_mfma_f32_16x16x32_bf16 v[152:155], v[164:167], v[184:187], v[152:155]\n"
"v_mfma_f32_16x16x32_bf16 v[148:151], v[164:167], v[180:183], v[148:151]\n"
"ds_read_b64_tr_b16 v[162:163], v213 offset:20768\n"
"v_mfma_f32_16x16x32_bf16 v[144:147], v[164:167], v[176:179], v[144:147]\n"
"s_waitcnt vmcnt(4)\n"
"s_waitcnt lgkmcnt(0)\n"
"s_barrier\n"
"v_mfma_f32_16x16x32_bf16 v[0:3], v[168:171], v[192:195], v[0:3]\n"
"s_mov_b32 s60, 1310720\n"
"s_add_u32 m0, s61, 32768\n"
"s_nop 0\n"
"buffer_load_dwordx4 v214, s[20:23], s60 offen offset:0 sc1 lds\n"
"ds_read_b64_tr_b16 v[164:165], v213 offset:27680\n"
"v_mfma_f32_16x16x32_bf16 v[4:7], v[168:171], v[196:199], v[4:7]\n"
"ds_read_b64_tr_b16 v[166:167], v213 offset:29984\n"
"v_mfma_f32_16x16x32_bf16 v[8:11], v[168:171], v[200:203], v[8:11]\n"
"ds_read_b64_tr_b16 v[176:177], v208 offset:0\n"
"v_mfma_f32_16x16x32_bf16 v[12:15], v[168:171], v[204:207], v[12:15]\n"
"ds_read_b64_tr_b16 v[178:179], v208 offset:4096\n"
"v_mfma_f32_16x16x32_bf16 v[28:31], v[172:175], v[204:207], v[28:31]\n"
"buffer_load_dwordx4 v215, s[20:23], s60 offen offset:1024 sc1 lds\n"
"ds_read_b64_tr_b16 v[168:169], v213 offset:18496\n"
"v_mfma_f32_16x16x32_bf16 v[24:27], v[172:175], v[200:203], v[24:27]\n"
"ds_read_b64_tr_b16 v[170:171], v213 offset:20800\n"
"v_mfma_f32_16x16x32_bf16 v[20:23], v[172:175], v[196:199], v[20:23]\n"
"ds_read_b64_tr_b16 v[180:181], v209 offset:0\n"
"v_mfma_f32_16x16x32_bf16 v[16:19], v[172:175], v[192:195], v[16:19]\n"
"ds_read_b64_tr_b16 v[182:183], v209 offset:4096\n"
"v_mfma_f32_16x16x32_bf16 v[32:35], v[160:163], v[192:195], v[32:35]\n"
"buffer_load_dwordx4 v216, s[20:23], s60 offen offset:2048 sc1 lds\n"
"ds_read_b64_tr_b16 v[172:173], v213 offset:27712\n"
"v_mfma_f32_16x16x32_bf16 v[36:39], v[160:163], v[196:199], v[36:39]\n"
"ds_read_b64_tr_b16 v[174:175], v213 offset:30016\n"
"v_mfma_f32_16x16x32_bf16 v[40:43], v[160:163], v[200:203], v[40:43]\n"
"ds_read_b64_tr_b16 v[184:185], v210 offset:0\n"
"v_mfma_f32_16x16x32_bf16 v[44:47], v[160:163], v[204:207], v[44:47]\n"
"ds_read_b64_tr_b16 v[186:187], v210 offset:4096\n"
"s_waitcnt lgkmcnt(10)\n"
"v_mfma_f32_16x16x32_bf16 v[60:63], v[164:167], v[204:207], v[60:63]\n"
"buffer_load_dwordx4 v217, s[20:23], s60 offen offset:3072 sc1 lds\n"
"ds_read_b64_tr_b16 v[160:161], v213 offset:18528\n"
"v_mfma_f32_16x16x32_bf16 v[56:59], v[164:167], v[200:203], v[56:59]\n"
"ds_read_b64_tr_b16 v[162:163], v213 offset:20832\n"
"v_mfma_f32_16x16x32_bf16 v[52:55], v[164:167], v[196:199], v[52:55]\n"
"ds_read_b64_tr_b16 v[188:189], v211 offset:0\n"
"v_mfma_f32_16x16x32_bf16 v[48:51], v[164:167], v[192:195], v[48:51]\n"
"ds_read_b64_tr_b16 v[190:191], v211 offset:4096\n"
"s_waitcnt lgkmcnt(10)\n"
"v_mfma_f32_16x16x32_bf16 v[64:67], v[168:171], v[192:195], v[64:67]\n"
"ds_read_b64_tr_b16 v[164:165], v213 offset:27744\n"
"v_mfma_f32_16x16x32_bf16 v[68:71], v[168:171], v[196:199], v[68:71]\n"
"v_mfma_f32_16x16x32_bf16 v[72:75], v[168:171], v[200:203], v[72:75]\n"
"ds_read_b64_tr_b16 v[166:167], v213 offset:30048\n"
"v_mfma_f32_16x16x32_bf16 v[76:79], v[168:171], v[204:207], v[76:79]\n"
"s_waitcnt lgkmcnt(8)\n"
"v_mfma_f32_16x16x32_bf16 v[92:95], v[172:175], v[204:207], v[92:95]\n"
"ds_read_b64_tr_b16 v[168:169], v213 offset:18688\n"
"v_mfma_f32_16x16x32_bf16 v[88:91], v[172:175], v[200:203], v[88:91]\n"
"v_mfma_f32_16x16x32_bf16 v[84:87], v[172:175], v[196:199], v[84:87]\n"
"ds_read_b64_tr_b16 v[170:171], v213 offset:20992\n"
"v_mfma_f32_16x16x32_bf16 v[80:83], v[172:175], v[192:195], v[80:83]\n"
"s_waitcnt lgkmcnt(6)\n"
"v_mfma_f32_16x16x32_bf16 v[96:99], v[160:163], v[192:195], v[96:99]\n"
"ds_read_b64_tr_b16 v[172:173], v213 offset:27904\n"
"v_mfma_f32_16x16x32_bf16 v[100:103], v[160:163], v[196:199], v[100:103]\n"
"v_mfma_f32_16x16x32_bf16 v[104:107], v[160:163], v[200:203], v[104:107]\n"
"ds_read_b64_tr_b16 v[174:175], v213 offset:30208\n"
"v_mfma_f32_16x16x32_bf16 v[108:111], v[160:163], v[204:207], v[108:111]\n"
"s_waitcnt lgkmcnt(4)\n"
"v_mfma_f32_16x16x32_bf16 v[124:127], v[164:167], v[204:207], v[124:127]\n"
"ds_read_b64_tr_b16 v[160:161], v212 offset:0\n"
"v_mfma_f32_16x16x32_bf16 v[120:123], v[164:167], v[200:203], v[120:123]\n"
"v_mfma_f32_16x16x32_bf16 v[116:119], v[164:167], v[196:199], v[116:119]\n"
"ds_read_b64_tr_b16 v[162:163], v212 offset:2304\n"
"v_mfma_f32_16x16x32_bf16 v[112:115], v[164:167], v[192:195], v[112:115]\n"
"s_waitcnt lgkmcnt(4)\n"
"v_mfma_f32_16x16x32_bf16 v[128:131], v[168:171], v[192:195], v[128:131]\n"
"ds_read_b64_tr_b16 v[164:165], v212 offset:9216\n"
"v_mfma_f32_16x16x32_bf16 v[132:135], v[168:171], v[196:199], v[132:135]\n"
"v_mfma_f32_16x16x32_bf16 v[136:139], v[168:171], v[200:203], v[136:139]\n"
"ds_read_b64_tr_b16 v[166:167], v212 offset:11520\n"
"v_mfma_f32_16x16x32_bf16 v[140:143], v[168:171], v[204:207], v[140:143]\n"
"s_waitcnt lgkmcnt(4)\n"
"v_mfma_f32_16x16x32_bf16 v[156:159], v[172:175], v[204:207], v[156:159]\n"
"ds_read_b64_tr_b16 v[168:169], v212 offset:32\n"
"v_mfma_f32_16x16x32_bf16 v[152:155], v[172:175], v[200:203], v[152:155]\n"
"v_mfma_f32_16x16x32_bf16 v[148:151], v[172:175], v[196:199], v[148:151]\n"
"ds_read_b64_tr_b16 v[170:171], v212 offset:2336\n"
"v_mfma_f32_16x16x32_bf16 v[144:147], v[172:175], v[192:195], v[144:147]\n"
"s_waitcnt vmcnt(4)\n"
"s_barrier\n"
"s_waitcnt lgkmcnt(4)\n"
"v_mfma_f32_16x16x32_bf16 v[0:3], v[160:163], v[176:179], v[0:3]\n"
"s_mov_b32 s60, 1441792\n"
"s_add_u32 m0, s61, 49152\n"
"s_nop 0\n"
"buffer_load_dwordx4 v214, s[20:23], s60 offen offset:0 sc1 lds\n"
"ds_read_b64_tr_b16 v[172:173], v212 offset:9248\n"
"v_mfma_f32_16x16x32_bf16 v[4:7], v[160:163], v[180:183], v[4:7]\n"
"ds_read_b64_tr_b16 v[174:175], v212 offset:11552\n"
"v_mfma_f32_16x16x32_bf16 v[8:11], v[160:163], v[184:187], v[8:11]\n"
"ds_read_b64_tr_b16 v[192:193], v208 offset:16384\n"
"v_mfma_f32_16x16x32_bf16 v[12:15], v[160:163], v[188:191], v[12:15]\n"
"ds_read_b64_tr_b16 v[194:195], v208 offset:20480\n"
"s_waitcnt lgkmcnt(6)\n"
"v_mfma_f32_16x16x32_bf16 v[28:31], v[164:167], v[188:191], v[28:31]\n"
"buffer_load_dwordx4 v215, s[20:23], s60 offen offset:1024 sc1 lds\n"
"ds_read_b64_tr_b16 v[160:161], v212 offset:64\n"
"v_mfma_f32_16x16x32_bf16 v[24:27], v[164:167], v[184:187], v[24:27]\n"
"ds_read_b64_tr_b16 v[162:163], v212 offset:2368\n"
"v_mfma_f32_16x16x32_bf16 v[20:23], v[164:167], v[180:183], v[20:23]\n"
"ds_read_b64_tr_b16 v[196:197], v209 offset:16384\n"
"v_mfma_f32_16x16x32_bf16 v[16:19], v[164:167], v[176:179], v[16:19]\n"
"ds_read_b64_tr_b16 v[198:199], v209 offset:20480\n"
"s_waitcnt lgkmcnt(8)\n"
"v_mfma_f32_16x16x32_bf16 v[32:35], v[168:171], v[176:179], v[32:35]\n"
"buffer_load_dwordx4 v216, s[20:23], s60 offen offset:2048 sc1 lds\n"
"ds_read_b64_tr_b16 v[164:165], v212 offset:9280\n"
"v_mfma_f32_16x16x32_bf16 v[36:39], v[168:171], v[180:183], v[36:39]\n"
"ds_read_b64_tr_b16 v[166:167], v212 offset:11584\n"
"v_mfma_f32_16x16x32_bf16 v[40:43], v[168:171], v[184:187], v[40:43]\n"
"ds_read_b64_tr_b16 v[200:201], v210 offset:16384\n"
"v_mfma_f32_16x16x32_bf16 v[44:47], v[168:171], v[188:191], v[44:47]\n"
"ds_read_b64_tr_b16 v[202:203], v210 offset:20480\n"
"s_waitcnt lgkmcnt(10)\n"
"v_mfma_f32_16x16x32_bf16 v[60:63], v[172:175], v[188:191], v[60:63]\n"
"buffer_load_dwordx4 v217, s[20:23], s60 offen offset:3072 sc1 lds\n"
"ds_read_b64_tr_b16 v[168:169], v212 offset:96\n"
"v_mfma_f32_16x16x32_bf16 v[56:59], v[172:175], v[184:187], v[56:59]\n"
"ds_read_b64_tr_b16 v[170:171], v212 offset:2400\n"
"v_mfma_f32_16x16x32_bf16 v[52:55], v[172:175], v[180:183], v[52:55]\n"
"ds_read_b64_tr_b16 v[204:205], v211 offset:16384\n"
"v_mfma_f32_16x16x32_bf16 v[48:51], v[172:175], v[176:179], v[48:51]\n"
"ds_read_b64_tr_b16 v[206:207], v211 offset:20480\n"
"s_waitcnt lgkmcnt(10)\n"
"v_mfma_f32_16x16x32_bf16 v[64:67], v[160:163], v[176:179], v[64:67]\n"
"ds_read_b64_tr_b16 v[172:173], v212 offset:9312\n"
"v_mfma_f32_16x16x32_bf16 v[68:71], v[160:163], v[180:183], v[68:71]\n"
"v_mfma_f32_16x16x32_bf16 v[72:75], v[160:163], v[184:187], v[72:75]\n"
"ds_read_b64_tr_b16 v[174:175], v212 offset:11616\n"
"v_mfma_f32_16x16x32_bf16 v[76:79], v[160:163], v[188:191], v[76:79]\n"
"s_waitcnt lgkmcnt(8)\n"
"v_mfma_f32_16x16x32_bf16 v[92:95], v[164:167], v[188:191], v[92:95]\n"
"ds_read_b64_tr_b16 v[160:161], v212 offset:256\n"
"v_mfma_f32_16x16x32_bf16 v[88:91], v[164:167], v[184:187], v[88:91]\n"
"v_mfma_f32_16x16x32_bf16 v[84:87], v[164:167], v[180:183], v[84:87]\n"
"ds_read_b64_tr_b16 v[162:163], v212 offset:2560\n"
"v_mfma_f32_16x16x32_bf16 v[80:83], v[164:167], v[176:179], v[80:83]\n"
"s_waitcnt lgkmcnt(6)\n"
"v_mfma_f32_16x16x32_bf16 v[96:99], v[168:171], v[176:179], v[96:99]\n"
"ds_read_b64_tr_b16 v[164:165], v212 offset:9472\n"
"v_mfma_f32_16x16x32_bf16 v[100:103], v[168:171], v[180:183], v[100:103]\n"
"v_mfma_f32_16x16x32_bf16 v[104:107], v[168:171], v[184:187], v[104:107]\n"
"ds_read_b64_tr_b16 v[166:167], v212 offset:11776\n"
"v_mfma_f32_16x16x32_bf16 v[108:111], v[168:171], v[188:191], v[108:111]\n"
"s_waitcnt lgkmcnt(4)\n"
"v_mfma_f32_16x16x32_bf16 v[124:127], v[172:175], v[188:191], v[124:127]\n"
"ds_read_b64_tr_b16 v[168:169], v212 offset:18432\n"
"v_mfma_f32_16x16x32_bf16 v[120:123], v[172:175], v[184:187], v[120:123]\n"
"v_mfma_f32_16x16x32_bf16 v[116:119], v[172:175], v[180:183], v[116:119]\n"
"ds_read_b64_tr_b16 v[170:171], v212 offset:20736\n"
"v_mfma_f32_16x16x32_bf16 v[112:115], v[172:175], v[176:179], v[112:115]\n"
"s_waitcnt lgkmcnt(4)\n"
"v_mfma_f32_16x16x32_bf16 v[128:131], v[160:163], v[176:179], v[128:131]\n"
"ds_read_b64_tr_b16 v[172:173], v212 offset:27648\n"
"v_mfma_f32_16x16x32_bf16 v[132:135], v[160:163], v[180:183], v[132:135]\n"
"v_mfma_f32_16x16x32_bf16 v[136:139], v[160:163], v[184:187], v[136:139]\n"
"ds_read_b64_tr_b16 v[174:175], v212 offset:29952\n"
"v_mfma_f32_16x16x32_bf16 v[140:143], v[160:163], v[188:191], v[140:143]\n"
"s_waitcnt lgkmcnt(4)\n"
"v_mfma_f32_16x16x32_bf16 v[156:159], v[164:167], v[188:191], v[156:159]\n"
"ds_read_b64_tr_b16 v[160:161], v212 offset:18464\n"
"v_mfma_f32_16x16x32_bf16 v[152:155], v[164:167], v[184:187], v[152:155]\n"
"v_mfma_f32_16x16x32_bf16 v[148:151], v[164:167], v[180:183], v[148:151]\n"
"ds_read_b64_tr_b16 v[162:163], v212 offset:20768\n"
"v_mfma_f32_16x16x32_bf16 v[144:147], v[164:167], v[176:179], v[144:147]\n"
"s_waitcnt vmcnt(4)\n"
"s_barrier\n"
"s_waitcnt lgkmcnt(4)\n"
"v_mfma_f32_16x16x32_bf16 v[0:3], v[168:171], v[192:195], v[0:3]\n"
"s_mov_b32 s60, 1572864\n"
"s_add_u32 m0, s61, 0\n"
"s_nop 0\n"
"buffer_load_dwordx4 v214, s[20:23], s60 offen offset:0 sc1 lds\n"
"ds_read_b64_tr_b16 v[164:165], v212 offset:27680\n"
"v_mfma_f32_16x16x32_bf16 v[4:7], v[168:171], v[196:199], v[4:7]\n"
"ds_read_b64_tr_b16 v[166:167], v212 offset:29984\n"
"v_mfma_f32_16x16x32_bf16 v[8:11], v[168:171], v[200:203], v[8:11]\n"
"ds_read_b64_tr_b16 v[176:177], v208 offset:32768\n"
"v_mfma_f32_16x16x32_bf16 v[12:15], v[168:171], v[204:207], v[12:15]\n"
"ds_read_b64_tr_b16 v[178:179], v208 offset:36864\n"
"s_waitcnt lgkmcnt(6)\n"
"v_mfma_f32_16x16x32_bf16 v[28:31], v[172:175], v[204:207], v[28:31]\n"
"buffer_load_dwordx4 v215, s[20:23], s60 offen offset:1024 sc1 lds\n"
"ds_read_b64_tr_b16 v[168:169], v212 offset:18496\n"
"v_mfma_f32_16x16x32_bf16 v[24:27], v[172:175], v[200:203], v[24:27]\n"
"ds_read_b64_tr_b16 v[170:171], v212 offset:20800\n"
"v_mfma_f32_16x16x32_bf16 v[20:23], v[172:175], v[196:199], v[20:23]\n"
"ds_read_b64_tr_b16 v[180:181], v209 offset:32768\n"
"v_mfma_f32_16x16x32_bf16 v[16:19], v[172:175], v[192:195], v[16:19]\n"
"ds_read_b64_tr_b16 v[182:183], v209 offset:36864\n"
"s_waitcnt lgkmcnt(8)\n"
"v_mfma_f32_16x16x32_bf16 v[32:35], v[160:163], v[192:195], v[32:35]\n"
"buffer_load_dwordx4 v216, s[20:23], s60 offen offset:2048 sc1 lds\n"
"ds_read_b64_tr_b16 v[172:173], v212 offset:27712\n"
"v_mfma_f32_16x16x32_bf16 v[36:39], v[160:163], v[196:199], v[36:39]\n"
"ds_read_b64_tr_b16 v[174:175], v212 offset:30016\n"
"v_mfma_f32_16x16x32_bf16 v[40:43], v[160:163], v[200:203], v[40:43]\n"
"ds_read_b64_tr_b16 v[184:185], v210 offset:32768\n"
"v_mfma_f32_16x16x32_bf16 v[44:47], v[160:163], v[204:207], v[44:47]\n"
"ds_read_b64_tr_b16 v[186:187], v210 offset:36864\n"
"s_waitcnt lgkmcnt(10)\n"
"v_mfma_f32_16x16x32_bf16 v[60:63], v[164:167], v[204:207], v[60:63]\n"
"buffer_load_dwordx4 v217, s[20:23], s60 offen offset:3072 sc1 lds\n"
"ds_read_b64_tr_b16 v[160:161], v212 offset:18528\n"
"v_mfma_f32_16x16x32_bf16 v[56:59], v[164:167], v[200:203], v[56:59]\n"
"ds_read_b64_tr_b16 v[162:163], v212 offset:20832\n"
"v_mfma_f32_16x16x32_bf16 v[52:55], v[164:167], v[196:199], v[52:55]\n"
"ds_read_b64_tr_b16 v[188:189], v211 offset:32768\n"
"v_mfma_f32_16x16x32_bf16 v[48:51], v[164:167], v[192:195], v[48:51]\n"
"ds_read_b64_tr_b16 v[190:191], v211 offset:36864\n"
"s_waitcnt lgkmcnt(10)\n"
"v_mfma_f32_16x16x32_bf16 v[64:67], v[168:171], v[192:195], v[64:67]\n"
"ds_read_b64_tr_b16 v[164:165], v212 offset:27744\n"
"v_mfma_f32_16x16x32_bf16 v[68:71], v[168:171], v[196:199], v[68:71]\n"
"v_mfma_f32_16x16x32_bf16 v[72:75], v[168:171], v[200:203], v[72:75]\n"
"ds_read_b64_tr_b16 v[166:167], v212 offset:30048\n"
"v_mfma_f32_16x16x32_bf16 v[76:79], v[168:171], v[204:207], v[76:79]\n"
"s_waitcnt lgkmcnt(8)\n"
"v_mfma_f32_16x16x32_bf16 v[92:95], v[172:175], v[204:207], v[92:95]\n"
"ds_read_b64_tr_b16 v[168:169], v212 offset:18688\n"
"v_mfma_f32_16x16x32_bf16 v[88:91], v[172:175], v[200:203], v[88:91]\n"
"v_mfma_f32_16x16x32_bf16 v[84:87], v[172:175], v[196:199], v[84:87]\n"
"ds_read_b64_tr_b16 v[170:171], v212 offset:20992\n"
"v_mfma_f32_16x16x32_bf16 v[80:83], v[172:175], v[192:195], v[80:83]\n"
"s_waitcnt lgkmcnt(6)\n"
"v_mfma_f32_16x16x32_bf16 v[96:99], v[160:163], v[192:195], v[96:99]\n"
"ds_read_b64_tr_b16 v[172:173], v212 offset:27904\n"
"v_mfma_f32_16x16x32_bf16 v[100:103], v[160:163], v[196:199], v[100:103]\n"
"v_mfma_f32_16x16x32_bf16 v[104:107], v[160:163], v[200:203], v[104:107]\n"
"ds_read_b64_tr_b16 v[174:175], v212 offset:30208\n"
"v_mfma_f32_16x16x32_bf16 v[108:111], v[160:163], v[204:207], v[108:111]\n"
"s_waitcnt lgkmcnt(4)\n"
"v_mfma_f32_16x16x32_bf16 v[124:127], v[164:167], v[204:207], v[124:127]\n"
"ds_read_b64_tr_b16 v[160:161], v213 offset:0\n"
"v_mfma_f32_16x16x32_bf16 v[120:123], v[164:167], v[200:203], v[120:123]\n"
"v_mfma_f32_16x16x32_bf16 v[116:119], v[164:167], v[196:199], v[116:119]\n"
"ds_read_b64_tr_b16 v[162:163], v213 offset:2304\n"
"v_mfma_f32_16x16x32_bf16 v[112:115], v[164:167], v[192:195], v[112:115]\n"
"s_waitcnt lgkmcnt(4)\n"
"v_mfma_f32_16x16x32_bf16 v[128:131], v[168:171], v[192:195], v[128:131]\n"
"ds_read_b64_tr_b16 v[164:165], v213 offset:9216\n"
"v_mfma_f32_16x16x32_bf16 v[132:135], v[168:171], v[196:199], v[132:135]\n"
"v_mfma_f32_16x16x32_bf16 v[136:139], v[168:171], v[200:203], v[136:139]\n"
"ds_read_b64_tr_b16 v[166:167], v213 offset:11520\n"
"v_mfma_f32_16x16x32_bf16 v[140:143], v[168:171], v[204:207], v[140:143]\n"
"s_waitcnt lgkmcnt(4)\n"
"v_mfma_f32_16x16x32_bf16 v[156:159], v[172:175], v[204:207], v[156:159]\n"
"ds_read_b64_tr_b16 v[168:169], v213 offset:32\n"
"v_mfma_f32_16x16x32_bf16 v[152:155], v[172:175], v[200:203], v[152:155]\n"
"v_mfma_f32_16x16x32_bf16 v[148:151], v[172:175], v[196:199], v[148:151]\n"
"ds_read_b64_tr_b16 v[170:171], v213 offset:2336\n"
"v_mfma_f32_16x16x32_bf16 v[144:147], v[172:175], v[192:195], v[144:147]\n"
"s_waitcnt vmcnt(4)\n"
"s_barrier\n"
"s_waitcnt lgkmcnt(4)\n"
"v_mfma_f32_16x16x32_bf16 v[0:3], v[160:163], v[176:179], v[0:3]\n"
"s_mov_b32 s60, 1703936\n"
"s_add_u32 m0, s61, 16384\n"
"s_nop 0\n"
"buffer_load_dwordx4 v214, s[20:23], s60 offen offset:0 sc1 lds\n"
"ds_read_b64_tr_b16 v[172:173], v213 offset:9248\n"
"v_mfma_f32_16x16x32_bf16 v[4:7], v[160:163], v[180:183], v[4:7]\n"
"ds_read_b64_tr_b16 v[174:175], v213 offset:11552\n"
"v_mfma_f32_16x16x32_bf16 v[8:11], v[160:163], v[184:187], v[8:11]\n"
"ds_read_b64_tr_b16 v[192:193], v208 offset:49152\n"
"v_mfma_f32_16x16x32_bf16 v[12:15], v[160:163], v[188:191], v[12:15]\n"
"ds_read_b64_tr_b16 v[194:195], v208 offset:53248\n"
"s_waitcnt lgkmcnt(6)\n"
"v_mfma_f32_16x16x32_bf16 v[28:31], v[164:167], v[188:191], v[28:31]\n"
"buffer_load_dwordx4 v215, s[20:23], s60 offen offset:1024 sc1 lds\n"
"ds_read_b64_tr_b16 v[160:161], v213 offset:64\n"
"v_mfma_f32_16x16x32_bf16 v[24:27], v[164:167], v[184:187], v[24:27]\n"
"ds_read_b64_tr_b16 v[162:163], v213 offset:2368\n"
"v_mfma_f32_16x16x32_bf16 v[20:23], v[164:167], v[180:183], v[20:23]\n"
"ds_read_b64_tr_b16 v[196:197], v209 offset:49152\n"
"v_mfma_f32_16x16x32_bf16 v[16:19], v[164:167], v[176:179], v[16:19]\n"
"ds_read_b64_tr_b16 v[198:199], v209 offset:53248\n"
"s_waitcnt lgkmcnt(8)\n"
"v_mfma_f32_16x16x32_bf16 v[32:35], v[168:171], v[176:179], v[32:35]\n"
"buffer_load_dwordx4 v216, s[20:23], s60 offen offset:2048 sc1 lds\n"
"ds_read_b64_tr_b16 v[164:165], v213 offset:9280\n"
"v_mfma_f32_16x16x32_bf16 v[36:39], v[168:171], v[180:183], v[36:39]\n"
"ds_read_b64_tr_b16 v[166:167], v213 offset:11584\n"
"v_mfma_f32_16x16x32_bf16 v[40:43], v[168:171], v[184:187], v[40:43]\n"
"ds_read_b64_tr_b16 v[200:201], v210 offset:49152\n"
"v_mfma_f32_16x16x32_bf16 v[44:47], v[168:171], v[188:191], v[44:47]\n"
"ds_read_b64_tr_b16 v[202:203], v210 offset:53248\n"
"s_waitcnt lgkmcnt(10)\n"
"v_mfma_f32_16x16x32_bf16 v[60:63], v[172:175], v[188:191], v[60:63]\n"
"buffer_load_dwordx4 v217, s[20:23], s60 offen offset:3072 sc1 lds\n"
"ds_read_b64_tr_b16 v[168:169], v213 offset:96\n"
"v_mfma_f32_16x16x32_bf16 v[56:59], v[172:175], v[184:187], v[56:59]\n"
"ds_read_b64_tr_b16 v[170:171], v213 offset:2400\n"
"v_mfma_f32_16x16x32_bf16 v[52:55], v[172:175], v[180:183], v[52:55]\n"
"ds_read_b64_tr_b16 v[204:205], v211 offset:49152\n"
"v_mfma_f32_16x16x32_bf16 v[48:51], v[172:175], v[176:179], v[48:51]\n"
"ds_read_b64_tr_b16 v[206:207], v211 offset:53248\n"
"s_waitcnt lgkmcnt(10)\n"
"v_mfma_f32_16x16x32_bf16 v[64:67], v[160:163], v[176:179], v[64:67]\n"
"ds_read_b64_tr_b16 v[172:173], v213 offset:9312\n"
"v_mfma_f32_16x16x32_bf16 v[68:71], v[160:163], v[180:183], v[68:71]\n"
"v_mfma_f32_16x16x32_bf16 v[72:75], v[160:163], v[184:187], v[72:75]\n"
"ds_read_b64_tr_b16 v[174:175], v213 offset:11616\n"
"v_mfma_f32_16x16x32_bf16 v[76:79], v[160:163], v[188:191], v[76:79]\n"
"s_waitcnt lgkmcnt(8)\n"
"v_mfma_f32_16x16x32_bf16 v[92:95], v[164:167], v[188:191], v[92:95]\n"
"ds_read_b64_tr_b16 v[160:161], v213 offset:256\n"
"v_mfma_f32_16x16x32_bf16 v[88:91], v[164:167], v[184:187], v[88:91]\n"
"v_mfma_f32_16x16x32_bf16 v[84:87], v[164:167], v[180:183], v[84:87]\n"
"ds_read_b64_tr_b16 v[162:163], v213 offset:2560\n"
"v_mfma_f32_16x16x32_bf16 v[80:83], v[164:167], v[176:179], v[80:83]\n"
"s_waitcnt lgkmcnt(6)\n"
"v_mfma_f32_16x16x32_bf16 v[96:99], v[168:171], v[176:179], v[96:99]\n"
"ds_read_b64_tr_b16 v[164:165], v213 offset:9472\n"
"v_mfma_f32_16x16x32_bf16 v[100:103], v[168:171], v[180:183], v[100:103]\n"
"v_mfma_f32_16x16x32_bf16 v[104:107], v[168:171], v[184:187], v[104:107]\n"
"ds_read_b64_tr_b16 v[166:167], v213 offset:11776\n"
"v_mfma_f32_16x16x32_bf16 v[108:111], v[168:171], v[188:191], v[108:111]\n"
"s_waitcnt lgkmcnt(4)\n"
"v_mfma_f32_16x16x32_bf16 v[124:127], v[172:175], v[188:191], v[124:127]\n"
"ds_read_b64_tr_b16 v[168:169], v213 offset:18432\n"
"v_mfma_f32_16x16x32_bf16 v[120:123], v[172:175], v[184:187], v[120:123]\n"
"v_mfma_f32_16x16x32_bf16 v[116:119], v[172:175], v[180:183], v[116:119]\n"
"ds_read_b64_tr_b16 v[170:171], v213 offset:20736\n"
"v_mfma_f32_16x16x32_bf16 v[112:115], v[172:175], v[176:179], v[112:115]\n"
"s_waitcnt lgkmcnt(4)\n"
"v_mfma_f32_16x16x32_bf16 v[128:131], v[160:163], v[176:179], v[128:131]\n"
"ds_read_b64_tr_b16 v[172:173], v213 offset:27648\n"
"v_mfma_f32_16x16x32_bf16 v[132:135], v[160:163], v[180:183], v[132:135]\n"
"v_mfma_f32_16x16x32_bf16 v[136:139], v[160:163], v[184:187], v[136:139]\n"
"ds_read_b64_tr_b16 v[174:175], v213 offset:29952\n"
"v_mfma_f32_16x16x32_bf16 v[140:143], v[160:163], v[188:191], v[140:143]\n"
"s_waitcnt lgkmcnt(4)\n"
"v_mfma_f32_16x16x32_bf16 v[156:159], v[164:167], v[188:191], v[156:159]\n"
"ds_read_b64_tr_b16 v[160:161], v213 offset:18464\n"
"v_mfma_f32_16x16x32_bf16 v[152:155], v[164:167], v[184:187], v[152:155]\n"
"v_mfma_f32_16x16x32_bf16 v[148:151], v[164:167], v[180:183], v[148:151]\n"
"ds_read_b64_tr_b16 v[162:163], v213 offset:20768\n"
"v_mfma_f32_16x16x32_bf16 v[144:147], v[164:167], v[176:179], v[144:147]\n"
"s_waitcnt vmcnt(4)\n"
"s_barrier\n"
"s_waitcnt lgkmcnt(4)\n"
"v_mfma_f32_16x16x32_bf16 v[0:3], v[168:171], v[192:195], v[0:3]\n"
"s_mov_b32 s60, 1835008\n"
"s_add_u32 m0, s61, 32768\n"
"s_nop 0\n"
"buffer_load_dwordx4 v214, s[20:23], s60 offen offset:0 sc1 lds\n"
"ds_read_b64_tr_b16 v[164:165], v213 offset:27680\n"
"v_mfma_f32_16x16x32_bf16 v[4:7], v[168:171], v[196:199], v[4:7]\n"
"ds_read_b64_tr_b16 v[166:167], v213 offset:29984\n"
"v_mfma_f32_16x16x32_bf16 v[8:11], v[168:171], v[200:203], v[8:11]\n"
"ds_read_b64_tr_b16 v[176:177], v208 offset:0\n"
"v_mfma_f32_16x16x32_bf16 v[12:15], v[168:171], v[204:207], v[12:15]\n"
"ds_read_b64_tr_b16 v[178:179], v208 offset:4096\n"
"s_waitcnt lgkmcnt(6)\n"
"v_mfma_f32_16x16x32_bf16 v[28:31], v[172:175], v[204:207], v[28:31]\n"
"buffer_load_dwordx4 v215, s[20:23], s60 offen offset:1024 sc1 lds\n"
"ds_read_b64_tr_b16 v[168:169], v213 offset:18496\n"
"v_mfma_f32_16x16x32_bf16 v[24:27], v[172:175], v[200:203], v[24:27]\n"
"ds_read_b64_tr_b16 v[170:171], v213 offset:20800\n"
"v_mfma_f32_16x16x32_bf16 v[20:23], v[172:175], v[196:199], v[20:23]\n"
"ds_read_b64_tr_b16 v[180:181], v209 offset:0\n"
"v_mfma_f32_16x16x32_bf16 v[16:19], v[172:175], v[192:195], v[16:19]\n"
"ds_read_b64_tr_b16 v[182:183], v209 offset:4096\n"
"s_waitcnt lgkmcnt(8)\n"
"v_mfma_f32_16x16x32_bf16 v[32:35], v[160:163], v[192:195], v[32:35]\n"
"buffer_load_dwordx4 v216, s[20:23], s60 offen offset:2048 sc1 lds\n"
"ds_read_b64_tr_b16 v[172:173], v213 offset:27712\n"
"v_mfma_f32_16x16x32_bf16 v[36:39], v[160:163], v[196:199], v[36:39]\n"
"ds_read_b64_tr_b16 v[174:175], v213 offset:30016\n"
"v_mfma_f32_16x16x32_bf16 v[40:43], v[160:163], v[200:203], v[40:43]\n"
"ds_read_b64_tr_b16 v[184:185], v210 offset:0\n"
"v_mfma_f32_16x16x32_bf16 v[44:47], v[160:163], v[204:207], v[44:47]\n"
"ds_read_b64_tr_b16 v[186:187], v210 offset:4096\n"
"s_waitcnt lgkmcnt(10)\n"
"v_mfma_f32_16x16x32_bf16 v[60:63], v[164:167], v[204:207], v[60:63]\n"
"buffer_load_dwordx4 v217, s[20:23], s60 offen offset:3072 sc1 lds\n"
"ds_read_b64_tr_b16 v[160:161], v213 offset:18528\n"
"v_mfma_f32_16x16x32_bf16 v[56:59], v[164:167], v[200:203], v[56:59]\n"
"ds_read_b64_tr_b16 v[162:163], v213 offset:20832\n"
"v_mfma_f32_16x16x32_bf16 v[52:55], v[164:167], v[196:199], v[52:55]\n"
"ds_read_b64_tr_b16 v[188:189], v211 offset:0\n"
"v_mfma_f32_16x16x32_bf16 v[48:51], v[164:167], v[192:195], v[48:51]\n"
"ds_read_b64_tr_b16 v[190:191], v211 offset:4096\n"
"s_waitcnt lgkmcnt(10)\n"
"v_mfma_f32_16x16x32_bf16 v[64:67], v[168:171], v[192:195], v[64:67]\n"
"ds_read_b64_tr_b16 v[164:165], v213 offset:27744\n"
"v_mfma_f32_16x16x32_bf16 v[68:71], v[168:171], v[196:199], v[68:71]\n"
"v_mfma_f32_16x16x32_bf16 v[72:75], v[168:171], v[200:203], v[72:75]\n"
"ds_read_b64_tr_b16 v[166:167], v213 offset:30048\n"
"v_mfma_f32_16x16x32_bf16 v[76:79], v[168:171], v[204:207], v[76:79]\n"
"s_waitcnt lgkmcnt(8)\n"
"v_mfma_f32_16x16x32_bf16 v[92:95], v[172:175], v[204:207], v[92:95]\n"
"ds_read_b64_tr_b16 v[168:169], v213 offset:18688\n"
"v_mfma_f32_16x16x32_bf16 v[88:91], v[172:175], v[200:203], v[88:91]\n"
"v_mfma_f32_16x16x32_bf16 v[84:87], v[172:175], v[196:199], v[84:87]\n"
"ds_read_b64_tr_b16 v[170:171], v213 offset:20992\n"
"v_mfma_f32_16x16x32_bf16 v[80:83], v[172:175], v[192:195], v[80:83]\n"
"s_waitcnt lgkmcnt(6)\n"
"v_mfma_f32_16x16x32_bf16 v[96:99], v[160:163], v[192:195], v[96:99]\n"
"ds_read_b64_tr_b16 v[172:173], v213 offset:27904\n"
"v_mfma_f32_16x16x32_bf16 v[100:103], v[160:163], v[196:199], v[100:103]\n"
"v_mfma_f32_16x16x32_bf16 v[104:107], v[160:163], v[200:203], v[104:107]\n"
"ds_read_b64_tr_b16 v[174:175], v213 offset:30208\n"
"v_mfma_f32_16x16x32_bf16 v[108:111], v[160:163], v[204:207], v[108:111]\n"
"s_waitcnt lgkmcnt(4)\n"
"v_mfma_f32_16x16x32_bf16 v[124:127], v[164:167], v[204:207], v[124:127]\n"
"ds_read_b64_tr_b16 v[160:161], v212 offset:0\n"
"v_mfma_f32_16x16x32_bf16 v[120:123], v[164:167], v[200:203], v[120:123]\n"
"v_mfma_f32_16x16x32_bf16 v[116:119], v[164:167], v[196:199], v[116:119]\n"
"ds_read_b64_tr_b16 v[162:163], v212 offset:2304\n"
"v_mfma_f32_16x16x32_bf16 v[112:115], v[164:167], v[192:195], v[112:115]\n"
"s_waitcnt lgkmcnt(4)\n"
"v_mfma_f32_16x16x32_bf16 v[128:131], v[168:171], v[192:195], v[128:131]\n"
"ds_read_b64_tr_b16 v[164:165], v212 offset:9216\n"
"v_mfma_f32_16x16x32_bf16 v[132:135], v[168:171], v[196:199], v[132:135]\n"
"v_mfma_f32_16x16x32_bf16 v[136:139], v[168:171], v[200:203], v[136:139]\n"
"ds_read_b64_tr_b16 v[166:167], v212 offset:11520\n"
"v_mfma_f32_16x16x32_bf16 v[140:143], v[168:171], v[204:207], v[140:143]\n"
"s_waitcnt lgkmcnt(4)\n"
"v_mfma_f32_16x16x32_bf16 v[156:159], v[172:175], v[204:207], v[156:159]\n"
"ds_read_b64_tr_b16 v[168:169], v212 offset:32\n"
"v_mfma_f32_16x16x32_bf16 v[152:155], v[172:175], v[200:203], v[152:155]\n"
"v_mfma_f32_16x16x32_bf16 v[148:151], v[172:175], v[196:199], v[148:151]\n"
"ds_read_b64_tr_b16 v[170:171], v212 offset:2336\n"
"v_mfma_f32_16x16x32_bf16 v[144:147], v[172:175], v[192:195], v[144:147]\n"
"s_waitcnt vmcnt(4)\n"
"s_barrier\n"
"s_waitcnt lgkmcnt(4)\n"
"v_mfma_f32_16x16x32_bf16 v[0:3], v[160:163], v[176:179], v[0:3]\n"
"s_mov_b32 s60, 1966080\n"
"s_add_u32 m0, s61, 49152\n"
"s_nop 0\n"
"buffer_load_dwordx4 v214, s[20:23], s60 offen offset:0 sc1 lds\n"
"ds_read_b64_tr_b16 v[172:173], v212 offset:9248\n"
"v_mfma_f32_16x16x32_bf16 v[4:7], v[160:163], v[180:183], v[4:7]\n"
"ds_read_b64_tr_b16 v[174:175], v212 offset:11552\n"
"v_mfma_f32_16x16x32_bf16 v[8:11], v[160:163], v[184:187], v[8:11]\n"
"ds_read_b64_tr_b16 v[192:193], v208 offset:16384\n"
"v_mfma_f32_16x16x32_bf16 v[12:15], v[160:163], v[188:191], v[12:15]\n"
"ds_read_b64_tr_b16 v[194:195], v208 offset:20480\n"
"s_waitcnt lgkmcnt(6)\n"
"v_mfma_f32_16x16x32_bf16 v[28:31], v[164:167], v[188:191], v[28:31]\n"
"buffer_load_dwordx4 v215, s[20:23], s60 offen offset:1024 sc1 lds\n"
"ds_read_b64_tr_b16 v[160:161], v212 offset:64\n"
"v_mfma_f32_16x16x32_bf16 v[24:27], v[164:167], v[184:187], v[24:27]\n"
"ds_read_b64_tr_b16 v[162:163], v212 offset:2368\n"
"v_mfma_f32_16x16x32_bf16 v[20:23], v[164:167], v[180:183], v[20:23]\n"
"ds_read_b64_tr_b16 v[196:197], v209 offset:16384\n"
"v_mfma_f32_16x16x32_bf16 v[16:19], v[164:167], v[176:179], v[16:19]\n"
"ds_read_b64_tr_b16 v[198:199], v209 offset:20480\n"
"s_waitcnt lgkmcnt(8)\n"
"v_mfma_f32_16x16x32_bf16 v[32:35], v[168:171], v[176:179], v[32:35]\n"
"buffer_load_dwordx4 v216, s[20:23], s60 offen offset:2048 sc1 lds\n"
"ds_read_b64_tr_b16 v[164:165], v212 offset:9280\n"
"v_mfma_f32_16x16x32_bf16 v[36:39], v[168:171], v[180:183], v[36:39]\n"
"ds_read_b64_tr_b16 v[166:167], v212 offset:11584\n"
"v_mfma_f32_16x16x32_bf16 v[40:43], v[168:171], v[184:187], v[40:43]\n"
"ds_read_b64_tr_b16 v[200:201], v210 offset:16384\n"
"v_mfma_f32_16x16x32_bf16 v[44:47], v[168:171], v[188:191], v[44:47]\n"
"ds_read_b64_tr_b16 v[202:203], v210 offset:20480\n"
"s_waitcnt lgkmcnt(10)\n"
"v_mfma_f32_16x16x32_bf16 v[60:63], v[172:175], v[188:191], v[60:63]\n"
"buffer_load_dwordx4 v217, s[20:23], s60 offen offset:3072 sc1 lds\n"
"ds_read_b64_tr_b16 v[168:169], v212 offset:96\n"
"v_mfma_f32_16x16x32_bf16 v[56:59], v[172:175], v[184:187], v[56:59]\n"
"ds_read_b64_tr_b16 v[170:171], v212 offset:2400\n"
"v_mfma_f32_16x16x32_bf16 v[52:55], v[172:175], v[180:183], v[52:55]\n"
"ds_read_b64_tr_b16 v[204:205], v211 offset:16384\n"
"v_mfma_f32_16x16x32_bf16 v[48:51], v[172:175], v[176:179], v[48:51]\n"
"ds_read_b64_tr_b16 v[206:207], v211 offset:20480\n"
"s_waitcnt lgkmcnt(10)\n"
"v_mfma_f32_16x16x32_bf16 v[64:67], v[160:163], v[176:179], v[64:67]\n"
"ds_read_b64_tr_b16 v[172:173], v212 offset:9312\n"
"v_mfma_f32_16x16x32_bf16 v[68:71], v[160:163], v[180:183], v[68:71]\n"
"v_mfma_f32_16x16x32_bf16 v[72:75], v[160:163], v[184:187], v[72:75]\n"
"ds_read_b64_tr_b16 v[174:175], v212 offset:11616\n"
"v_mfma_f32_16x16x32_bf16 v[76:79], v[160:163], v[188:191], v[76:79]\n"
"s_waitcnt lgkmcnt(8)\n"
"v_mfma_f32_16x16x32_bf16 v[92:95], v[164:167], v[188:191], v[92:95]\n"
"ds_read_b64_tr_b16 v[160:161], v212 offset:256\n"
"v_mfma_f32_16x16x32_bf16 v[88:91], v[164:167], v[184:187], v[88:91]\n"
"v_mfma_f32_16x16x32_bf16 v[84:87], v[164:167], v[180:183], v[84:87]\n"
"ds_read_b64_tr_b16 v[162:163], v212 offset:2560\n"
"v_mfma_f32_16x16x32_bf16 v[80:83], v[164:167], v[176:179], v[80:83]\n"
"s_waitcnt lgkmcnt(6)\n"
"v_mfma_f32_16x16x32_bf16 v[96:99], v[168:171], v[176:179], v[96:99]\n"
"ds_read_b64_tr_b16 v[164:165], v212 offset:9472\n"
"v_mfma_f32_16x16x32_bf16 v[100:103], v[168:171], v[180:183], v[100:103]\n"
"v_mfma_f32_16x16x32_bf16 v[104:107], v[168:171], v[184:187], v[104:107]\n"
"ds_read_b64_tr_b16 v[166:167], v212 offset:11776\n"
"v_mfma_f32_16x16x32_bf16 v[108:111], v[168:171], v[188:191], v[108:111]\n"
"s_waitcnt lgkmcnt(4)\n"
"v_mfma_f32_16x16x32_bf16 v[124:127], v[172:175], v[188:191], v[124:127]\n"
"ds_read_b64_tr_b16 v[168:169], v212 offset:18432\n"
"v_mfma_f32_16x16x32_bf16 v[120:123], v[172:175], v[184:187], v[120:123]\n"
"v_mfma_f32_16x16x32_bf16 v[116:119], v[172:175], v[180:183], v[116:119]\n"
"ds_read_b64_tr_b16 v[170:171], v212 offset:20736\n"
"v_mfma_f32_16x16x32_bf16 v[112:115], v[172:175], v[176:179], v[112:115]\n"
"s_waitcnt lgkmcnt(4)\n"
"v_mfma_f32_16x16x32_bf16 v[128:131], v[160:163], v[176:179], v[128:131]\n"
"ds_read_b64_tr_b16 v[172:173], v212 offset:27648\n"
"v_mfma_f32_16x16x32_bf16 v[132:135], v[160:163], v[180:183], v[132:135]\n"
"v_mfma_f32_16x16x32_bf16 v[136:139], v[160:163], v[184:187], v[136:139]\n"
"ds_read_b64_tr_b16 v[174:175], v212 offset:29952\n"
"v_mfma_f32_16x16x32_bf16 v[140:143], v[160:163], v[188:191], v[140:143]\n"
"s_waitcnt lgkmcnt(4)\n"
"v_mfma_f32_16x16x32_bf16 v[156:159], v[164:167], v[188:191], v[156:159]\n"
"ds_read_b64_tr_b16 v[160:161], v212 offset:18464\n"
"v_mfma_f32_16x16x32_bf16 v[152:155], v[164:167], v[184:187], v[152:155]\n"
"v_mfma_f32_16x16x32_bf16 v[148:151], v[164:167], v[180:183], v[148:151]\n"
"ds_read_b64_tr_b16 v[162:163], v212 offset:20768\n"
"v_mfma_f32_16x16x32_bf16 v[144:147], v[164:167], v[176:179], v[144:147]\n"
"s_waitcnt vmcnt(4)\n"
"s_barrier\n"
"s_waitcnt lgkmcnt(4)\n"
"v_mfma_f32_16x16x32_bf16 v[0:3], v[168:171], v[192:195], v[0:3]\n"
"s_mov_b32 s60, 2097152\n"
"s_add_u32 m0, s61, 0\n"
"s_nop 0\n"
"buffer_load_dwordx4 v214, s[20:23], s60 offen offset:0 sc1 lds\n"
"ds_read_b64_tr_b16 v[164:165], v212 offset:27680\n"
"v_mfma_f32_16x16x32_bf16 v[4:7], v[168:171], v[196:199], v[4:7]\n"
"ds_read_b64_tr_b16 v[166:167], v212 offset:29984\n"
"v_mfma_f32_16x16x32_bf16 v[8:11], v[168:171], v[200:203], v[8:11]\n"
"ds_read_b64_tr_b16 v[176:177], v208 offset:32768\n"
"v_mfma_f32_16x16x32_bf16 v[12:15], v[168:171], v[204:207], v[12:15]\n"
"ds_read_b64_tr_b16 v[178:179], v208 offset:36864\n"
"s_waitcnt lgkmcnt(6)\n"
"v_mfma_f32_16x16x32_bf16 v[28:31], v[172:175], v[204:207], v[28:31]\n"
"buffer_load_dwordx4 v215, s[20:23], s60 offen offset:1024 sc1 lds\n"
"ds_read_b64_tr_b16 v[168:169], v212 offset:18496\n"
"v_mfma_f32_16x16x32_bf16 v[24:27], v[172:175], v[200:203], v[24:27]\n"
"ds_read_b64_tr_b16 v[170:171], v212 offset:20800\n"
"v_mfma_f32_16x16x32_bf16 v[20:23], v[172:175], v[196:199], v[20:23]\n"
"ds_read_b64_tr_b16 v[180:181], v209 offset:32768\n"
"v_mfma_f32_16x16x32_bf16 v[16:19], v[172:175], v[192:195], v[16:19]\n"
"ds_read_b64_tr_b16 v[182:183], v209 offset:36864\n"
"s_waitcnt lgkmcnt(8)\n"
"v_mfma_f32_16x16x32_bf16 v[32:35], v[160:163], v[192:195], v[32:35]\n"
"buffer_load_dwordx4 v216, s[20:23], s60 offen offset:2048 sc1 lds\n"
"ds_read_b64_tr_b16 v[172:173], v212 offset:27712\n"
"v_mfma_f32_16x16x32_bf16 v[36:39], v[160:163], v[196:199], v[36:39]\n"
"ds_read_b64_tr_b16 v[174:175], v212 offset:30016\n"
"v_mfma_f32_16x16x32_bf16 v[40:43], v[160:163], v[200:203], v[40:43]\n"
"ds_read_b64_tr_b16 v[184:185], v210 offset:32768\n"
"v_mfma_f32_16x16x32_bf16 v[44:47], v[160:163], v[204:207], v[44:47]\n"
"ds_read_b64_tr_b16 v[186:187], v210 offset:36864\n"
"s_waitcnt lgkmcnt(10)\n"
"v_mfma_f32_16x16x32_bf16 v[60:63], v[164:167], v[204:207], v[60:63]\n"
"buffer_load_dwordx4 v217, s[20:23], s60 offen offset:3072 sc1 lds\n"
"ds_read_b64_tr_b16 v[160:161], v212 offset:18528\n"
"v_mfma_f32_16x16x32_bf16 v[56:59], v[164:167], v[200:203], v[56:59]\n"
"ds_read_b64_tr_b16 v[162:163], v212 offset:20832\n"
"v_mfma_f32_16x16x32_bf16 v[52:55], v[164:167], v[196:199], v[52:55]\n"
"ds_read_b64_tr_b16 v[188:189], v211 offset:32768\n"
"v_mfma_f32_16x16x32_bf16 v[48:51], v[164:167], v[192:195], v[48:51]\n"
"ds_read_b64_tr_b16 v[190:191], v211 offset:36864\n"
"s_waitcnt lgkmcnt(10)\n"
"v_mfma_f32_16x16x32_bf16 v[64:67], v[168:171], v[192:195], v[64:67]\n"
"ds_read_b64_tr_b16 v[164:165], v212 offset:27744\n"
"v_mfma_f32_16x16x32_bf16 v[68:71], v[168:171], v[196:199], v[68:71]\n"
"v_mfma_f32_16x16x32_bf16 v[72:75], v[168:171], v[200:203], v[72:75]\n"
"ds_read_b64_tr_b16 v[166:167], v212 offset:30048\n"
"v_mfma_f32_16x16x32_bf16 v[76:79], v[168:171], v[204:207], v[76:79]\n"
"s_waitcnt lgkmcnt(8)\n"
"v_mfma_f32_16x16x32_bf16 v[92:95], v[172:175], v[204:207], v[92:95]\n"
"ds_read_b64_tr_b16 v[168:169], v212 offset:18688\n"
"v_mfma_f32_16x16x32_bf16 v[88:91], v[172:175], v[200:203], v[88:91]\n"
"v_mfma_f32_16x16x32_bf16 v[84:87], v[172:175], v[196:199], v[84:87]\n"
"ds_read_b64_tr_b16 v[170:171], v212 offset:20992\n"
"v_mfma_f32_16x16x32_bf16 v[80:83], v[172:175], v[192:195], v[80:83]\n"
"s_waitcnt lgkmcnt(6)\n"
"v_mfma_f32_16x16x32_bf16 v[96:99], v[160:163], v[192:195], v[96:99]\n"
"ds_read_b64_tr_b16 v[172:173], v212 offset:27904\n"
"v_mfma_f32_16x16x32_bf16 v[100:103], v[160:163], v[196:199], v[100:103]\n"
"v_mfma_f32_16x16x32_bf16 v[104:107], v[160:163], v[200:203], v[104:107]\n"
"ds_read_b64_tr_b16 v[174:175], v212 offset:30208\n"
"v_mfma_f32_16x16x32_bf16 v[108:111], v[160:163], v[204:207], v[108:111]\n"
"s_waitcnt lgkmcnt(4)\n"
"v_mfma_f32_16x16x32_bf16 v[124:127], v[164:167], v[204:207], v[124:127]\n"
"ds_read_b64_tr_b16 v[160:161], v213 offset:0\n"
"v_mfma_f32_16x16x32_bf16 v[120:123], v[164:167], v[200:203], v[120:123]\n"
"v_mfma_f32_16x16x32_bf16 v[116:119], v[164:167], v[196:199], v[116:119]\n"
"ds_read_b64_tr_b16 v[162:163], v213 offset:2304\n"
"v_mfma_f32_16x16x32_bf16 v[112:115], v[164:167], v[192:195], v[112:115]\n"
"s_waitcnt lgkmcnt(4)\n"
"v_mfma_f32_16x16x32_bf16 v[128:131], v[168:171], v[192:195], v[128:131]\n"
"ds_read_b64_tr_b16 v[164:165], v213 offset:9216\n"
"v_mfma_f32_16x16x32_bf16 v[132:135], v[168:171], v[196:199], v[132:135]\n"
"v_mfma_f32_16x16x32_bf16 v[136:139], v[168:171], v[200:203], v[136:139]\n"
"ds_read_b64_tr_b16 v[166:167], v213 offset:11520\n"
"v_mfma_f32_16x16x32_bf16 v[140:143], v[168:171], v[204:207], v[140:143]\n"
"s_waitcnt lgkmcnt(4)\n"
"v_mfma_f32_16x16x32_bf16 v[156:159], v[172:175], v[204:207], v[156:159]\n"
"ds_read_b64_tr_b16 v[168:169], v213 offset:32\n"
"v_mfma_f32_16x16x32_bf16 v[152:155], v[172:175], v[200:203], v[152:155]\n"
"v_mfma_f32_16x16x32_bf16 v[148:151], v[172:175], v[196:199], v[148:151]\n"
"ds_read_b64_tr_b16 v[170:171], v213 offset:2336\n"
"v_mfma_f32_16x16x32_bf16 v[144:147], v[172:175], v[192:195], v[144:147]\n"
"s_waitcnt vmcnt(4)\n"
"s_barrier\n"
"s_waitcnt lgkmcnt(4)\n"
"v_mfma_f32_16x16x32_bf16 v[0:3], v[160:163], v[176:179], v[0:3]\n"
"s_mov_b32 s60, 2228224\n"
"s_add_u32 m0, s61, 16384\n"
"s_nop 0\n"
"buffer_load_dwordx4 v214, s[20:23], s60 offen offset:0 sc1 lds\n"
"ds_read_b64_tr_b16 v[172:173], v213 offset:9248\n"
"v_mfma_f32_16x16x32_bf16 v[4:7], v[160:163], v[180:183], v[4:7]\n"
"ds_read_b64_tr_b16 v[174:175], v213 offset:11552\n"
"v_mfma_f32_16x16x32_bf16 v[8:11], v[160:163], v[184:187], v[8:11]\n"
"ds_read_b64_tr_b16 v[192:193], v208 offset:49152\n"
"v_mfma_f32_16x16x32_bf16 v[12:15], v[160:163], v[188:191], v[12:15]\n"
"ds_read_b64_tr_b16 v[194:195], v208 offset:53248\n"
"s_waitcnt lgkmcnt(6)\n"
"v_mfma_f32_16x16x32_bf16 v[28:31], v[164:167], v[188:191], v[28:31]\n"
"buffer_load_dwordx4 v215, s[20:23], s60 offen offset:1024 sc1 lds\n"
"ds_read_b64_tr_b16 v[160:161], v213 offset:64\n"
"v_mfma_f32_16x16x32_bf16 v[24:27], v[164:167], v[184:187], v[24:27]\n"
"ds_read_b64_tr_b16 v[162:163], v213 offset:2368\n"
"v_mfma_f32_16x16x32_bf16 v[20:23], v[164:167], v[180:183], v[20:23]\n"
"ds_read_b64_tr_b16 v[196:197], v209 offset:49152\n"
"v_mfma_f32_16x16x32_bf16 v[16:19], v[164:167], v[176:179], v[16:19]\n"
"ds_read_b64_tr_b16 v[198:199], v209 offset:53248\n"
"s_waitcnt lgkmcnt(8)\n"
"v_mfma_f32_16x16x32_bf16 v[32:35], v[168:171], v[176:179], v[32:35]\n"
"buffer_load_dwordx4 v216, s[20:23], s60 offen offset:2048 sc1 lds\n"
"ds_read_b64_tr_b16 v[164:165], v213 offset:9280\n"
"v_mfma_f32_16x16x32_bf16 v[36:39], v[168:171], v[180:183], v[36:39]\n"
"ds_read_b64_tr_b16 v[166:167], v213 offset:11584\n"
"v_mfma_f32_16x16x32_bf16 v[40:43], v[168:171], v[184:187], v[40:43]\n"
"ds_read_b64_tr_b16 v[200:201], v210 offset:49152\n"
"v_mfma_f32_16x16x32_bf16 v[44:47], v[168:171], v[188:191], v[44:47]\n"
"ds_read_b64_tr_b16 v[202:203], v210 offset:53248\n"
"s_waitcnt lgkmcnt(10)\n"
"v_mfma_f32_16x16x32_bf16 v[60:63], v[172:175], v[188:191], v[60:63]\n"
"buffer_load_dwordx4 v217, s[20:23], s60 offen offset:3072 sc1 lds\n"
"ds_read_b64_tr_b16 v[168:169], v213 offset:96\n"
"v_mfma_f32_16x16x32_bf16 v[56:59], v[172:175], v[184:187], v[56:59]\n"
"ds_read_b64_tr_b16 v[170:171], v213 offset:2400\n"
"v_mfma_f32_16x16x32_bf16 v[52:55], v[172:175], v[180:183], v[52:55]\n"
"ds_read_b64_tr_b16 v[204:205], v211 offset:49152\n"
"v_mfma_f32_16x16x32_bf16 v[48:51], v[172:175], v[176:179], v[48:51]\n"
"ds_read_b64_tr_b16 v[206:207], v211 offset:53248\n"
"s_waitcnt lgkmcnt(10)\n"
"v_mfma_f32_16x16x32_bf16 v[64:67], v[160:163], v[176:179], v[64:67]\n"
"ds_read_b64_tr_b16 v[172:173], v213 offset:9312\n"
"v_mfma_f32_16x16x32_bf16 v[68:71], v[160:163], v[180:183], v[68:71]\n"
"v_mfma_f32_16x16x32_bf16 v[72:75], v[160:163], v[184:187], v[72:75]\n"
"ds_read_b64_tr_b16 v[174:175], v213 offset:11616\n"
"v_mfma_f32_16x16x32_bf16 v[76:79], v[160:163], v[188:191], v[76:79]\n"
"s_waitcnt lgkmcnt(8)\n"
"v_mfma_f32_16x16x32_bf16 v[92:95], v[164:167], v[188:191], v[92:95]\n"
"ds_read_b64_tr_b16 v[160:161], v213 offset:256\n"
"v_mfma_f32_16x16x32_bf16 v[88:91], v[164:167], v[184:187], v[88:91]\n"
"v_mfma_f32_16x16x32_bf16 v[84:87], v[164:167], v[180:183], v[84:87]\n"
"ds_read_b64_tr_b16 v[162:163], v213 offset:2560\n"
"v_mfma_f32_16x16x32_bf16 v[80:83], v[164:167], v[176:179], v[80:83]\n"
"s_waitcnt lgkmcnt(6)\n"
"v_mfma_f32_16x16x32_bf16 v[96:99], v[168:171], v[176:179], v[96:99]\n"
"ds_read_b64_tr_b16 v[164:165], v213 offset:9472\n"
"v_mfma_f32_16x16x32_bf16 v[100:103], v[168:171], v[180:183], v[100:103]\n"
"v_mfma_f32_16x16x32_bf16 v[104:107], v[168:171], v[184:187], v[104:107]\n"
"ds_read_b64_tr_b16 v[166:167], v213 offset:11776\n"
"v_mfma_f32_16x16x32_bf16 v[108:111], v[168:171], v[188:191], v[108:111]\n"
"s_waitcnt lgkmcnt(4)\n"
"v_mfma_f32_16x16x32_bf16 v[124:127], v[172:175], v[188:191], v[124:127]\n"
"ds_read_b64_tr_b16 v[168:169], v213 offset:18432\n"
"v_mfma_f32_16x16x32_bf16 v[120:123], v[172:175], v[184:187], v[120:123]\n"
"v_mfma_f32_16x16x32_bf16 v[116:119], v[172:175], v[180:183], v[116:119]\n"
"ds_read_b64_tr_b16 v[170:171], v213 offset:20736\n"
"v_mfma_f32_16x16x32_bf16 v[112:115], v[172:175], v[176:179], v[112:115]\n"
"s_waitcnt lgkmcnt(4)\n"
"v_mfma_f32_16x16x32_bf16 v[128:131], v[160:163], v[176:179], v[128:131]\n"
"ds_read_b64_tr_b16 v[172:173], v213 offset:27648\n"
"v_mfma_f32_16x16x32_bf16 v[132:135], v[160:163], v[180:183], v[132:135]\n"
"v_mfma_f32_16x16x32_bf16 v[136:139], v[160:163], v[184:187], v[136:139]\n"
"ds_read_b64_tr_b16 v[174:175], v213 offset:29952\n"
"v_mfma_f32_16x16x32_bf16 v[140:143], v[160:163], v[188:191], v[140:143]\n"
"s_waitcnt lgkmcnt(4)\n"
"v_mfma_f32_16x16x32_bf16 v[156:159], v[164:167], v[188:191], v[156:159]\n"
"ds_read_b64_tr_b16 v[160:161], v213 offset:18464\n"
"v_mfma_f32_16x16x32_bf16 v[152:155], v[164:167], v[184:187], v[152:155]\n"
"v_mfma_f32_16x16x32_bf16 v[148:151], v[164:167], v[180:183], v[148:151]\n"
"ds_read_b64_tr_b16 v[162:163], v213 offset:20768\n"
"v_mfma_f32_16x16x32_bf16 v[144:147], v[164:167], v[176:179], v[144:147]\n"
"s_waitcnt vmcnt(4)\n"
"s_barrier\n"
"s_waitcnt lgkmcnt(4)\n"
"v_mfma_f32_16x16x32_bf16 v[0:3], v[168:171], v[192:195], v[0:3]\n"
"ds_read_b64_tr_b16 v[164:165], v213 offset:27680\n"
"v_mfma_f32_16x16x32_bf16 v[4:7], v[168:171], v[196:199], v[4:7]\n"
"ds_read_b64_tr_b16 v[166:167], v213 offset:29984\n"
"v_mfma_f32_16x16x32_bf16 v[8:11], v[168:171], v[200:203], v[8:11]\n"
"ds_read_b64_tr_b16 v[176:177], v208 offset:0\n"
"v_mfma_f32_16x16x32_bf16 v[12:15], v[168:171], v[204:207], v[12:15]\n"
"ds_read_b64_tr_b16 v[178:179], v208 offset:4096\n"
"s_waitcnt lgkmcnt(6)\n"
"v_mfma_f32_16x16x32_bf16 v[28:31], v[172:175], v[204:207], v[28:31]\n"
"ds_read_b64_tr_b16 v[168:169], v213 offset:18496\n"
"ds_read_b64_tr_b16 v[170:171], v213 offset:20800\n"
"v_mfma_f32_16x16x32_bf16 v[24:27], v[172:175], v[200:203], v[24:27]\n"
"ds_read_b64_tr_b16 v[180:181], v209 offset:0\n"
"v_mfma_f32_16x16x32_bf16 v[20:23], v[172:175], v[196:199], v[20:23]\n"
"ds_read_b64_tr_b16 v[182:183], v209 offset:4096\n"
"s_add_u32 s24, s56, 0xc00000\n"
"s_addc_u32 s25, s57, 0\n"
"s_and_b32 s25, s25, 0xffff\n"
"s_mov_b32 s26, 0x510000\n"
"s_mov_b32 s27, 0x20000\n"
"v_mfma_f32_16x16x32_bf16 v[16:19], v[172:175], v[192:195], v[16:19]\n"
"s_add_u32 s28, s56, 0x2500000\n"
"s_addc_u32 s29, s57, 0\n"
"s_and_b32 s29, s29, 0xffff\n"
"s_mov_b32 s30, 0x900000\n"
"s_mov_b32 s31, 0x20000\n"
"s_waitcnt lgkmcnt(8)\n"
"v_mfma_f32_16x16x32_bf16 v[32:35], v[160:163], v[192:195], v[32:35]\n"
"ds_read_b64_tr_b16 v[172:173], v213 offset:27712\n"
"ds_read_b64_tr_b16 v[174:175], v213 offset:30016\n"
"v_mfma_f32_16x16x32_bf16 v[36:39], v[160:163], v[196:199], v[36:39]\n"
"ds_read_b64_tr_b16 v[184:185], v210 offset:0\n"
"v_mfma_f32_16x16x32_bf16 v[40:43], v[160:163], v[200:203], v[40:43]\n"
"ds_read_b64_tr_b16 v[186:187], v210 offset:4096\n"
"s_add_u32 s40, s68, 0x0\n"
"s_addc_u32 s41, s69, 0\n"
"s_and_b32 s41, s41, 0xffff\n"
"s_mov_b32 s42, 0x240000\n"
"s_mov_b32 s43, 0x20000\n"
"v_mfma_f32_16x16x32_bf16 v[44:47], v[160:163], v[204:207], v[44:47]\n"
"s_bfe_u32 s70, s48, 0x30003\n"
"s_and_b32 s54, s48, 4\n"
"s_lshl_b32 s54, s54, 1\n"
"s_add_u32 s70, s70, s54\n"
"s_lshr_b32 s71, s48, 6\n"
"s_waitcnt lgkmcnt(10)\n"
"v_mfma_f32_16x16x32_bf16 v[60:63], v[164:167], v[204:207], v[60:63]\n"
"ds_read_b64_tr_b16 v[160:161], v213 offset:18528\n"
"ds_read_b64_tr_b16 v[162:163], v213 offset:20832\n"
"v_mfma_f32_16x16x32_bf16 v[56:59], v[164:167], v[200:203], v[56:59]\n"
"ds_read_b64_tr_b16 v[188:189], v211 offset:0\n"
"v_mfma_f32_16x16x32_bf16 v[52:55], v[164:167], v[196:199], v[52:55]\n"
"ds_read_b64_tr_b16 v[190:191], v211 offset:4096\n"
"s_lshl_b32 s72, s70, 2\n"
"s_add_u32 s72, s72, s71\n"
"s_lshl_b32 s73, s70, 7\n"
"s_lshl_b32 s54, s50, 5\n"
"s_add_u32 s73, s73, s54\n"
"v_mfma_f32_16x16x32_bf16 v[48:51], v[164:167], v[192:195], v[48:51]\n"
"s_mul_i32 s75, s71, 144\n"
"s_and_b32 s54, s71, 1\n"
"s_lshl_b32 s55, s54, 4\n"
"s_add_u32 s74, s75, s55\n"
"s_xor_b32 s54, s54, 1\n"
"s_lshl_b32 s54, s54, 7\n"
"s_add_u32 s75, s75, s54\n"
"s_waitcnt lgkmcnt(10)\n"
"v_mfma_f32_16x16x32_bf16 v[64:67], v[168:171], v[192:195], v[64:67]\n"
"ds_read_b64_tr_b16 v[164:165], v213 offset:27744\n"
"v_mfma_f32_16x16x32_bf16 v[68:71], v[168:171], v[196:199], v[68:71]\n"
"ds_read_b64_tr_b16 v[166:167], v213 offset:30048\n"
"v_mfma_f32_16x16x32_bf16 v[72:75], v[168:171], v[200:203], v[72:75]\n"
"s_and_b32 s55, s53, 1\n"
"s_lshl_b32 s55, s55, 5\n"
"s_add_u32 s74, s74, s55\n"
"s_add_u32 s74, s74, 0\n"
"v_mfma_f32_16x16x32_bf16 v[76:79], v[168:171], v[204:207], v[76:79]\n"
"s_lshr_b32 s54, s53, 1\n"
"s_lshl_b32 s54, s54, 4\n"
"s_add_u32 s76, s73, s54\n"
"s_lshl_b32 s54, s49, 10\n"
"s_add_u32 s77, s54, 149504\n"
"s_waitcnt lgkmcnt(8)\n"
"v_mfma_f32_16x16x32_bf16 v[92:95], v[172:175], v[204:207], v[92:95]\n"
"ds_read_b64_tr_b16 v[168:169], v213 offset:18688\n"
"ds_read_b64_tr_b16 v[170:171], v213 offset:20992\n"
"v_mfma_f32_16x16x32_bf16 v[88:91], v[172:175], v[200:203], v[88:91]\n"
"s_lshl_b32 s54, s70, 3\n"
"s_lshl_b32 s55, s50, 1\n"
"s_add_u32 s54, s54, s55\n"
"s_mul_i32 s63, s54, 147456\n"
"v_mfma_f32_16x16x32_bf16 v[84:87], v[172:175], v[196:199], v[84:87]\n"
"v_add_u32 v216, s63, v254\n"
"v_mfma_f32_16x16x32_bf16 v[80:83], v[172:175], v[192:195], v[80:83]\n"
"v_add_u32 v217, 0x23800, v216\n"
"s_waitcnt lgkmcnt(6)\n"
"v_mfma_f32_16x16x32_bf16 v[96:99], v[160:163], v[192:195], v[96:99]\n"
"ds_read_b64_tr_b16 v[172:173], v213 offset:27904\n"
"v_mfma_f32_16x16x32_bf16 v[100:103], v[160:163], v[196:199], v[100:103]\n"
"ds_read_b64_tr_b16 v[174:175], v213 offset:30208\n"
"v_mfma_f32_16x16x32_bf16 v[104:107], v[160:163], v[200:203], v[104:107]\n"
"s_mul_i32 s47, s53, 36864\n"
"s_lshl_b32 s44, s50, 12\n"
"v_mfma_f32_16x16x32_bf16 v[108:111], v[160:163], v[204:207], v[108:111]\n"
"v_lshrrev_b32 v244, 1, v219\n"
"s_waitcnt lgkmcnt(4)\n"
"v_mfma_f32_16x16x32_bf16 v[124:127], v[164:167], v[204:207], v[124:127]\n"
"ds_read_b64_tr_b16 v[160:161], v212 offset:0\n"
"v_mfma_f32_16x16x32_bf16 v[120:123], v[164:167], v[200:203], v[120:123]\n"
"ds_read_b64_tr_b16 v[162:163], v212 offset:2304\n"
"v_mfma_f32_16x16x32_bf16 v[116:119], v[164:167], v[196:199], v[116:119]\n"
"v_lshlrev_b32 v244, 12, v244\n"
"v_mfma_f32_16x16x32_bf16 v[112:115], v[164:167], v[192:195], v[112:115]\n"
"v_and_b32 v245, 1, v219\n"
"s_waitcnt lgkmcnt(4)\n"
"v_mfma_f32_16x16x32_bf16 v[128:131], v[168:171], v[192:195], v[128:131]\n"
"ds_read_b64_tr_b16 v[164:165], v212 offset:9216\n"
"v_mfma_f32_16x16x32_bf16 v[132:135], v[168:171], v[196:199], v[132:135]\n"
"ds_read_b64_tr_b16 v[166:167], v212 offset:11520\n"
"v_mfma_f32_16x16x32_bf16 v[136:139], v[168:171], v[200:203], v[136:139]\n"
"v_lshl_add_u32 v244, v245, 4, v244\n"
"v_mfma_f32_16x16x32_bf16 v[140:143], v[168:171], v[204:207], v[140:143]\n"
"v_lshrrev_b32 v245, 2, v219\n"
"s_waitcnt lgkmcnt(4)\n"
"v_mfma_f32_16x16x32_bf16 v[156:159], v[172:175], v[204:207], v[156:159]\n"
"ds_read_b64_tr_b16 v[168:169], v212 offset:32\n"
"ds_read_b64_tr_b16 v[170:171], v212 offset:2336\n"
"v_mfma_f32_16x16x32_bf16 v[152:155], v[172:175], v[200:203], v[152:155]\n"
"v_lshlrev_b32 v245, 12, v245\n"
"v_mfma_f32_16x16x32_bf16 v[148:151], v[172:175], v[196:199], v[148:151]\n"
"v_and_b32 v246, 3, v219\n"
"v_mfma_f32_16x16x32_bf16 v[144:147], v[172:175], v[192:195], v[144:147]\n"
"v_lshl_add_u32 v245, v246, 4, v245\n"
"s_waitcnt vmcnt(0)\n"
"s_waitcnt lgkmcnt(0)\n"
"s_barrier\n"
"v_mfma_f32_16x16x32_bf16 v[0:3], v[160:163], v[176:179], v[0:3]\n"
"ds_read_b64_tr_b16 v[172:173], v212 offset:9248\n"
"v_mfma_f32_16x16x32_bf16 v[4:7], v[160:163], v[180:183], v[4:7]\n"
"ds_read_b64_tr_b16 v[174:175], v212 offset:11552\n"
"v_mfma_f32_16x16x32_bf16 v[8:11], v[160:163], v[184:187], v[8:11]\n"
"ds_read_b64_tr_b16 v[192:193], v208 offset:16384\n"
"v_mfma_f32_16x16x32_bf16 v[12:15], v[160:163], v[188:191], v[12:15]\n"
"ds_read_b64_tr_b16 v[194:195], v208 offset:20480\n"
"v_mfma_f32_16x16x32_bf16 v[28:31], v[164:167], v[188:191], v[28:31]\n"
"ds_read_b64_tr_b16 v[160:161], v212 offset:64\n"
"v_mfma_f32_16x16x32_bf16 v[24:27], v[164:167], v[184:187], v[24:27]\n"
"ds_read_b64_tr_b16 v[162:163], v212 offset:2368\n"
"v_mfma_f32_16x16x32_bf16 v[20:23], v[164:167], v[180:183], v[20:23]\n"
"ds_read_b64_tr_b16 v[196:197], v209 offset:16384\n"
"v_mfma_f32_16x16x32_bf16 v[16:19], v[164:167], v[176:179], v[16:19]\n"
"ds_read_b64_tr_b16 v[198:199], v209 offset:20480\n"
"v_mfma_f32_16x16x32_bf16 v[32:35], v[168:171], v[176:179], v[32:35]\n"
"ds_read_b64_tr_b16 v[164:165], v212 offset:9280\n"
"v_mfma_f32_16x16x32_bf16 v[36:39], v[168:171], v[180:183], v[36:39]\n"
"ds_read_b64_tr_b16 v[166:167], v212 offset:11584\n"
"v_mfma_f32_16x16x32_bf16 v[40:43], v[168:171], v[184:187], v[40:43]\n"
"ds_read_b64_tr_b16 v[200:201], v210 offset:16384\n"
"v_mfma_f32_16x16x32_bf16 v[44:47], v[168:171], v[188:191], v[44:47]\n"
"ds_read_b64_tr_b16 v[202:203], v210 offset:20480\n"
"s_waitcnt lgkmcnt(10)\n"
"v_mfma_f32_16x16x32_bf16 v[60:63], v[172:175], v[188:191], v[60:63]\n"
"ds_read_b64_tr_b16 v[168:169], v212 offset:96\n"
"v_mfma_f32_16x16x32_bf16 v[56:59], v[172:175], v[184:187], v[56:59]\n"
"ds_read_b64_tr_b16 v[170:171], v212 offset:2400\n"
"v_mfma_f32_16x16x32_bf16 v[52:55], v[172:175], v[180:183], v[52:55]\n"
"ds_read_b64_tr_b16 v[204:205], v211 offset:16384\n"
"v_mfma_f32_16x16x32_bf16 v[48:51], v[172:175], v[176:179], v[48:51]\n"
"ds_read_b64_tr_b16 v[206:207], v211 offset:20480\n"
"s_waitcnt lgkmcnt(10)\n"
"v_mfma_f32_16x16x32_bf16 v[64:67], v[160:163], v[176:179], v[64:67]\n"
"ds_read_b64_tr_b16 v[172:173], v212 offset:9312\n"
"v_mfma_f32_16x16x32_bf16 v[68:71], v[160:163], v[180:183], v[68:71]\n"
"v_mfma_f32_16x16x32_bf16 v[72:75], v[160:163], v[184:187], v[72:75]\n"
"ds_read_b64_tr_b16 v[174:175], v212 offset:11616\n"
"v_mfma_f32_16x16x32_bf16 v[76:79], v[160:163], v[188:191], v[76:79]\n"
"s_waitcnt lgkmcnt(8)\n"
"v_mfma_f32_16x16x32_bf16 v[92:95], v[164:167], v[188:191], v[92:95]\n"
"ds_read_b64_tr_b16 v[160:161], v212 offset:256\n"
"v_mfma_f32_16x16x32_bf16 v[88:91], v[164:167], v[184:187], v[88:91]\n"
"v_mfma_f32_16x16x32_bf16 v[84:87], v[164:167], v[180:183], v[84:87]\n"
"ds_read_b64_tr_b16 v[162:163], v212 offset:2560\n"
"v_mfma_f32_16x16x32_bf16 v[80:83], v[164:167], v[176:179], v[80:83]\n"
"s_waitcnt lgkmcnt(6)\n"
"v_mfma_f32_16x16x32_bf16 v[96:99], v[168:171], v[176:179], v[96:99]\n"
"ds_read_b64_tr_b16 v[164:165], v212 offset:9472\n"
"v_mfma_f32_16x16x32_bf16 v[100:103], v[168:171], v[180:183], v[100:103]\n"
"v_mfma_f32_16x16x32_bf16 v[104:107], v[168:171], v[184:187], v[104:107]\n"
"ds_read_b64_tr_b16 v[166:167], v212 offset:11776\n"
"v_mfma_f32_16x16x32_bf16 v[108:111], v[168:171], v[188:191], v[108:111]\n"
"s_waitcnt lgkmcnt(4)\n"
"v_mfma_f32_16x16x32_bf16 v[124:127], v[172:175], v[188:191], v[124:127]\n"
"ds_read_b64_tr_b16 v[168:169], v212 offset:18432\n"
"v_mfma_f32_16x16x32_bf16 v[120:123], v[172:175], v[184:187], v[120:123]\n"
"ds_read_b64_tr_b16 v[170:171], v212 offset:20736\n"
"v_mfma_f32_16x16x32_bf16 v[116:119], v[172:175], v[180:183], v[116:119]\n"
"s_lshl_b32 s54, s74, 12\n"
"s_lshl_b32 s55, s76, 1\n"
"s_add_u32 s60, s54, s55\n"
"s_mov_b32 m0, s77\n"
"s_nop 0\n"
"buffer_load_dwordx4 v244, s[20:23], s60 offen offset:0 sc1 lds\n"
"v_mfma_f32_16x16x32_bf16 v[112:115], v[172:175], v[176:179], v[112:115]\n"
"s_waitcnt lgkmcnt(4)\n"
"v_mfma_f32_16x16x32_bf16 v[128:131], v[160:163], v[176:179], v[128:131]\n"
"ds_read_b64_tr_b16 v[172:173], v212 offset:27648\n"
"v_mfma_f32_16x16x32_bf16 v[132:135], v[160:163], v[180:183], v[132:135]\n"
"ds_read_b64_tr_b16 v[174:175], v212 offset:29952\n"
"v_mfma_f32_16x16x32_bf16 v[136:139], v[160:163], v[184:187], v[136:139]\n"
"s_lshl_b32 s54, s75, 12\n"
"s_lshl_b32 s55, s73, 1\n"
"s_add_u32 s60, s54, s55\n"
"s_add_u32 m0, s77, 8192\n"
"s_nop 0\n"
"buffer_load_dwordx4 v245, s[20:23], s60 offen offset:0 sc1 lds\n"
"v_mfma_f32_16x16x32_bf16 v[140:143], v[160:163], v[188:191], v[140:143]\n"
"s_waitcnt lgkmcnt(4)\n"
"v_mfma_f32_16x16x32_bf16 v[156:159], v[164:167], v[188:191], v[156:159]\n"
"ds_read_b64_tr_b16 v[160:161], v212 offset:18464\n"
"v_mfma_f32_16x16x32_bf16 v[152:155], v[164:167], v[184:187], v[152:155]\n"
"ds_read_b64_tr_b16 v[162:163], v212 offset:20768\n"
"v_mfma_f32_16x16x32_bf16 v[148:151], v[164:167], v[180:183], v[148:151]\n"
"s_lshl_b32 s54, s50, 10\n"
"s_add_u32 s54, s54, 141312\n"
"v_and_b32 v238, 0xf0, v254\n"
"v_add_u32 v238, s54, v238\n"
"v_mfma_f32_16x16x32_bf16 v[144:147], v[164:167], v[176:179], v[144:147]\n"
"s_waitcnt lgkmcnt(4)\n"
"v_mfma_f32_16x16x32_bf16 v[0:3], v[168:171], v[192:195], v[0:3]\n"
"ds_read_b64_tr_b16 v[164:165], v212 offset:27680\n"
"v_mfma_f32_16x16x32_bf16 v[4:7], v[168:171], v[196:199], v[4:7]\n"
"v_mfma_f32_16x16x32_bf16 v[8:11], v[168:171], v[200:203], v[8:11]\n"
"ds_read_b64_tr_b16 v[166:167], v212 offset:29984\n"
"v_mfma_f32_16x16x32_bf16 v[12:15], v[168:171], v[204:207], v[12:15]\n"
"s_waitcnt lgkmcnt(4)\n"
"v_mfma_f32_16x16x32_bf16 v[28:31], v[172:175], v[204:207], v[28:31]\n"
"ds_read_b64_tr_b16 v[168:169], v212 offset:18496\n"
"v_mfma_f32_16x16x32_bf16 v[24:27], v[172:175], v[200:203], v[24:27]\n"
"v_mfma_f32_16x16x32_bf16 v[20:23], v[172:175], v[196:199], v[20:23]\n"
"ds_read_b64_tr_b16 v[170:171], v212 offset:20800\n"
"v_mfma_f32_16x16x32_bf16 v[16:19], v[172:175], v[192:195], v[16:19]\n"
"s_waitcnt lgkmcnt(4)\n"
"v_mfma_f32_16x16x32_bf16 v[32:35], v[160:163], v[192:195], v[32:35]\n"
"ds_read_b64_tr_b16 v[172:173], v212 offset:27712\n"
"v_mfma_f32_16x16x32_bf16 v[36:39], v[160:163], v[196:199], v[36:39]\n"
"v_mfma_f32_16x16x32_bf16 v[40:43], v[160:163], v[200:203], v[40:43]\n"
"ds_read_b64_tr_b16 v[174:175], v212 offset:30016\n"
"v_mfma_f32_16x16x32_bf16 v[44:47], v[160:163], v[204:207], v[44:47]\n"
"s_waitcnt lgkmcnt(4)\n"
"v_mfma_f32_16x16x32_bf16 v[60:63], v[164:167], v[204:207], v[60:63]\n"
"ds_read_b64_tr_b16 v[160:161], v212 offset:18528\n"
"v_mfma_f32_16x16x32_bf16 v[56:59], v[164:167], v[200:203], v[56:59]\n"
"v_mfma_f32_16x16x32_bf16 v[52:55], v[164:167], v[196:199], v[52:55]\n"
"ds_read_b64_tr_b16 v[162:163], v212 offset:20832\n"
"v_mfma_f32_16x16x32_bf16 v[48:51], v[164:167], v[192:195], v[48:51]\n"
"s_waitcnt lgkmcnt(4)\n"
"v_mfma_f32_16x16x32_bf16 v[64:67], v[168:171], v[192:195], v[64:67]\n"
"ds_read_b64_tr_b16 v[164:165], v212 offset:27744\n"
"v_mfma_f32_16x16x32_bf16 v[68:71], v[168:171], v[196:199], v[68:71]\n"
"v_mfma_f32_16x16x32_bf16 v[72:75], v[168:171], v[200:203], v[72:75]\n"
"ds_read_b64_tr_b16 v[166:167], v212 offset:30048\n"
"v_mfma_f32_16x16x32_bf16 v[76:79], v[168:171], v[204:207], v[76:79]\n"
"s_waitcnt lgkmcnt(4)\n"
"v_mfma_f32_16x16x32_bf16 v[92:95], v[172:175], v[204:207], v[92:95]\n"
"ds_read_b64_tr_b16 v[168:169], v212 offset:18688\n"
"v_mfma_f32_16x16x32_bf16 v[88:91], v[172:175], v[200:203], v[88:91]\n"
"v_mfma_f32_16x16x32_bf16 v[84:87], v[172:175], v[196:199], v[84:87]\n"
"ds_read_b64_tr_b16 v[170:171], v212 offset:20992\n"
"v_mfma_f32_16x16x32_bf16 v[80:83], v[172:175], v[192:195], v[80:83]\n"
"s_waitcnt lgkmcnt(4)\n"
"v_mfma_f32_16x16x32_bf16 v[96:99], v[160:163], v[192:195], v[96:99]\n"
"ds_read_b64_tr_b16 v[172:173], v212 offset:27904\n"
"v_mfma_f32_16x16x32_bf16 v[100:103], v[160:163], v[196:199], v[100:103]\n"
"v_mfma_f32_16x16x32_bf16 v[104:107], v[160:163], v[200:203], v[104:107]\n"
"ds_read_b64_tr_b16 v[174:175], v212 offset:30208\n"
"v_mfma_f32_16x16x32_bf16 v[108:111], v[160:163], v[204:207], v[108:111]\n"
"s_waitcnt lgkmcnt(4)\n"
"v_mfma_f32_16x16x32_bf16 v[124:127], v[164:167], v[204:207], v[124:127]\n"
"v_mfma_f32_16x16x32_bf16 v[120:123], v[164:167], v[200:203], v[120:123]\n"
"v_mfma_f32_16x16x32_bf16 v[116:119], v[164:167], v[196:199], v[116:119]\n"
"v_mfma_f32_16x16x32_bf16 v[112:115], v[164:167], v[192:195], v[112:115]\n"
"s_waitcnt lgkmcnt(2)\n"
"v_mfma_f32_16x16x32_bf16 v[128:131], v[168:171], v[192:195], v[128:131]\n"
"v_mfma_f32_16x16x32_bf16 v[132:135], v[168:171], v[196:199], v[132:135]\n"
"v_mfma_f32_16x16x32_bf16 v[136:139], v[168:171], v[200:203], v[136:139]\n"
"v_mfma_f32_16x16x32_bf16 v[140:143], v[168:171], v[204:207], v[140:143]\n"
"s_waitcnt lgkmcnt(0)\n"
"v_mfma_f32_16x16x32_bf16 v[156:159], v[172:175], v[204:207], v[156:159]\n"
"v_mfma_f32_16x16x32_bf16 v[152:155], v[172:175], v[200:203], v[152:155]\n"
"v_mfma_f32_16x16x32_bf16 v[148:151], v[172:175], v[196:199], v[148:151]\n"
"v_mfma_f32_16x16x32_bf16 v[144:147], v[172:175], v[192:195], v[144:147]\n"
"ds_read_b128 v[160:163], v238 offset:0\n"
"ds_read_b128 v[164:167], v238 offset:256\n"
"ds_read_b128 v[168:171], v238 offset:512\n"
"ds_read_b128 v[172:175], v238 offset:768\n"
"v_and_b32 v221, 48, v219\n"
"v_add_u32 v221, 0x22000, v221\n"
"s_lshl_b32 s54, s51, 4\n"
"s_lshl_b32 s55, s50, 2\n"
"s_add_u32 s54, s54, s55\n"
"s_mul_i32 s78, s54, 147456\n"
"s_mul_i32 s54, s53, 36864\n"
"s_add_u32 s78, s78, s54\n"
"s_and_b32 s55, s52, 7\n"
"s_lshr_b32 s54, s55, 1\n"
"s_lshl_b32 s54, s54, 10\n"
"s_add_u32 s79, s78, s54\n"
"s_add_u32 s79, s79, 32768\n"
"s_lshl_b32 s54, s55, 11\n"
"s_add_u32 s78, s78, s54\n"
"s_and_b32 s54, s52, 1\n"
"s_lshl_b32 s54, s54, 9\n"
"v_lshlrev_b32 v223, 3, v219\n"
"v_add_u32 v223, s54, v223\n"
"s_lshl_b32 s54, s51, 2\n"
"s_add_u32 s54, s54, s50\n"
"s_lshl_b32 s54, s54, 2\n"
"s_add_u32 s54, s54, s53\n"
"s_mul_i32 s98, s54, 192\n"
"s_add_u32 s98, s98, 262144\n"
"v_mov_b32 v224, 1\n"
"v_lshlrev_b32 v225, 6, v219\n"
"s_lshl_b32 s54, s70, 1\n"
"s_lshr_b32 s55, s50, 1\n"
"s_add_u32 s54, s54, s55\n"
"s_lshl_b32 s54, s54, 2\n"
"s_add_u32 s54, s54, s53\n"
"s_mul_i32 s99, s54, 192\n"
"s_add_u32 s99, s99, 262144\n"
"v_mov_b32 v226, 0\n"
"s_barrier\n"
"s_setprio 3\n"
"ds_read_b128 v[176:179], v221 offset:0\n"
"ds_read_b128 v[180:183], v221 offset:1024\n"
"ds_read_b128 v[184:187], v221 offset:64\n"
"ds_read_b128 v[188:191], v221 offset:1088\n"
"s_waitcnt lgkmcnt(3)\n"
"v_pk_fma_f32 v[0:1], v[176:177], v[160:161], v[0:1] op_sel_hi:[1,0,1]\n"
"v_pk_fma_f32 v[2:3], v[178:179], v[160:161], v[2:3] op_sel_hi:[1,0,1]\n"
"s_waitcnt lgkmcnt(1)\n"
"v_pk_fma_f32 v[32:33], v[184:185], v[160:161], v[32:33] op_sel_hi:[1,0,1]\n"
"v_pk_fma_f32 v[34:35], v[186:187], v[160:161], v[34:35] op_sel_hi:[1,0,1]\n"
"v_pk_mul_f32 v[200:201], v[0:1], v[0:1]\n"
"v_pk_mul_f32 v[202:203], v[2:3], v[2:3]\n"
"v_pk_mul_f32 v[204:205], v[32:33], v[32:33]\n"
"v_pk_mul_f32 v[206:207], v[34:35], v[34:35]\n"
"v_pk_fma_f32 v[200:201], v[200:201], v[162:163], v[160:161] op_sel:[0,0,1] op_sel_hi:[1,0,1]\n"
"v_pk_fma_f32 v[202:203], v[202:203], v[162:163], v[160:161] op_sel:[0,0,1] op_sel_hi:[1,0,1]\n"
"v_pk_fma_f32 v[204:205], v[204:205], v[162:163], v[160:161] op_sel:[0,0,1] op_sel_hi:[1,0,1]\n"
"v_pk_fma_f32 v[206:207], v[206:207], v[162:163], v[160:161] op_sel:[0,0,1] op_sel_hi:[1,0,1]\n"
"v_pk_mul_f32 v[200:201], v[0:1], v[200:201]\n"
"v_pk_mul_f32 v[202:203], v[2:3], v[202:203]\n"
"v_pk_mul_f32 v[204:205], v[32:33], v[204:205]\n"
"v_pk_mul_f32 v[206:207], v[34:35], v[206:207]\n"
"v_exp_f32 v200, v200\n"
"v_exp_f32 v201, v201\n"
"v_pk_fma_f32 v[16:17], v[180:181], v[160:161], v[16:17] op_sel_hi:[1,0,1]\n"
"v_exp_f32 v202, v202\n"
"v_exp_f32 v203, v203\n"
"v_pk_fma_f32 v[18:19], v[182:183], v[160:161], v[18:19] op_sel_hi:[1,0,1]\n"
"v_exp_f32 v204, v204\n"
"v_exp_f32 v205, v205\n"
"s_waitcnt lgkmcnt(0)\n"
"v_pk_fma_f32 v[48:49], v[188:189], v[160:161], v[48:49] op_sel_hi:[1,0,1]\n"
"v_exp_f32 v206, v206\n"
"v_exp_f32 v207, v207\n"
"v_pk_fma_f32 v[50:51], v[190:191], v[160:161], v[50:51] op_sel_hi:[1,0,1]\n"
"v_pk_fma_f32 v[200:201], v[162:163], v[200:201], v[162:163] op_sel:[1,0,1] op_sel_hi:[1,1,1]\n"
"v_pk_fma_f32 v[202:203], v[162:163], v[202:203], v[162:163] op_sel:[1,0,1] op_sel_hi:[1,1,1]\n"
"v_pk_fma_f32 v[204:205], v[162:163], v[204:205], v[162:163] op_sel:[1,0,1] op_sel_hi:[1,1,1]\n"
"v_pk_fma_f32 v[206:207], v[162:163], v[206:207], v[162:163] op_sel:[1,0,1] op_sel_hi:[1,1,1]\n"
"v_rcp_f32 v200, v200\n"
"v_rcp_f32 v201, v201\n"
"v_pk_mul_f32 v[16:17], v[0:1], v[16:17]\n"
"v_rcp_f32 v202, v202\n"
"v_rcp_f32 v203, v203\n"
"v_pk_mul_f32 v[18:19], v[2:3], v[18:19]\n"
"v_rcp_f32 v204, v204\n"
"v_rcp_f32 v205, v205\n"
"v_pk_mul_f32 v[48:49], v[32:33], v[48:49]\n"
"v_rcp_f32 v206, v206\n"
"v_rcp_f32 v207, v207\n"
"v_pk_mul_f32 v[50:51], v[34:35], v[50:51]\n"
"v_pk_mul_f32 v[16:17], v[16:17], v[200:201]\n"
"v_pk_mul_f32 v[18:19], v[18:19], v[202:203]\n"
"v_pk_mul_f32 v[48:49], v[48:49], v[204:205]\n"
"v_pk_mul_f32 v[50:51], v[50:51], v[206:207]\n"
"v_cvt_pk_bf16_f32 v192, v16, v17\n"
"v_cvt_pk_bf16_f32 v193, v18, v19\n"
"v_cvt_pk_bf16_f32 v194, v48, v49\n"
"v_cvt_pk_bf16_f32 v195, v50, v51\n"
"s_add_u32 s60, s78, 0\n"
"buffer_store_dwordx4 v[192:195], v254, s[32:35], s60 offen offset:0 sc1\n"
"v_pk_fma_f32 v[4:5], v[176:177], v[164:165], v[4:5] op_sel_hi:[1,0,1]\n"
"v_pk_fma_f32 v[6:7], v[178:179], v[164:165], v[6:7] op_sel_hi:[1,0,1]\n"
"v_pk_fma_f32 v[36:37], v[184:185], v[164:165], v[36:37] op_sel_hi:[1,0,1]\n"
"v_pk_fma_f32 v[38:39], v[186:187], v[164:165], v[38:39] op_sel_hi:[1,0,1]\n"
"v_pk_mul_f32 v[200:201], v[4:5], v[4:5]\n"
"v_pk_mul_f32 v[202:203], v[6:7], v[6:7]\n"
"v_pk_mul_f32 v[204:205], v[36:37], v[36:37]\n"
"v_pk_mul_f32 v[206:207], v[38:39], v[38:39]\n"
"v_pk_fma_f32 v[200:201], v[200:201], v[166:167], v[164:165] op_sel:[0,0,1] op_sel_hi:[1,0,1]\n"
"v_pk_fma_f32 v[202:203], v[202:203], v[166:167], v[164:165] op_sel:[0,0,1] op_sel_hi:[1,0,1]\n"
"v_pk_fma_f32 v[204:205], v[204:205], v[166:167], v[164:165] op_sel:[0,0,1] op_sel_hi:[1,0,1]\n"
"v_pk_fma_f32 v[206:207], v[206:207], v[166:167], v[164:165] op_sel:[0,0,1] op_sel_hi:[1,0,1]\n"
"v_pk_mul_f32 v[200:201], v[4:5], v[200:201]\n"
"v_pk_mul_f32 v[202:203], v[6:7], v[202:203]\n"
"v_pk_mul_f32 v[204:205], v[36:37], v[204:205]\n"
"v_pk_mul_f32 v[206:207], v[38:39], v[206:207]\n"
"v_exp_f32 v200, v200\n"
"v_exp_f32 v201, v201\n"
"v_pk_fma_f32 v[20:21], v[180:181], v[164:165], v[20:21] op_sel_hi:[1,0,1]\n"
"v_exp_f32 v202, v202\n"
"v_exp_f32 v203, v203\n"
"v_pk_fma_f32 v[22:23], v[182:183], v[164:165], v[22:23] op_sel_hi:[1,0,1]\n"
"v_exp_f32 v204, v204\n"
"v_exp_f32 v205, v205\n"
"v_pk_fma_f32 v[52:53], v[188:189], v[164:165], v[52:53] op_sel_hi:[1,0,1]\n"
"v_exp_f32 v206, v206\n"
"v_exp_f32 v207, v207\n"
"v_pk_fma_f32 v[54:55], v[190:191], v[164:165], v[54:55] op_sel_hi:[1,0,1]\n"
"v_pk_fma_f32 v[200:201], v[166:167], v[200:201], v[166:167] op_sel:[1,0,1] op_sel_hi:[1,1,1]\n"
"v_pk_fma_f32 v[202:203], v[166:167], v[202:203], v[166:167] op_sel:[1,0,1] op_sel_hi:[1,1,1]\n"
"v_pk_fma_f32 v[204:205], v[166:167], v[204:205], v[166:167] op_sel:[1,0,1] op_sel_hi:[1,1,1]\n"
"v_pk_fma_f32 v[206:207], v[166:167], v[206:207], v[166:167] op_sel:[1,0,1] op_sel_hi:[1,1,1]\n"
"v_rcp_f32 v200, v200\n"
"v_rcp_f32 v201, v201\n"
"v_pk_mul_f32 v[20:21], v[4:5], v[20:21]\n"
"v_rcp_f32 v202, v202\n"
"v_rcp_f32 v203, v203\n"
"v_pk_mul_f32 v[22:23], v[6:7], v[22:23]\n"
"v_rcp_f32 v204, v204\n"
"v_rcp_f32 v205, v205\n"
"v_pk_mul_f32 v[52:53], v[36:37], v[52:53]\n"
"v_rcp_f32 v206, v206\n"
"v_rcp_f32 v207, v207\n"
"v_pk_mul_f32 v[54:55], v[38:39], v[54:55]\n"
"v_pk_mul_f32 v[20:21], v[20:21], v[200:201]\n"
"v_pk_mul_f32 v[22:23], v[22:23], v[202:203]\n"
"v_pk_mul_f32 v[52:53], v[52:53], v[204:205]\n"
"v_pk_mul_f32 v[54:55], v[54:55], v[206:207]\n"
"v_cvt_pk_bf16_f32 v196, v20, v21\n"
"v_cvt_pk_bf16_f32 v197, v22, v23\n"
"v_cvt_pk_bf16_f32 v198, v52, v53\n"
"v_cvt_pk_bf16_f32 v199, v54, v55\n"
"s_add_u32 s60, s78, 147456\n"
"buffer_store_dwordx4 v[196:199], v254, s[32:35], s60 offen offset:0 sc1\n"
"v_pk_fma_f32 v[8:9], v[176:177], v[168:169], v[8:9] op_sel_hi:[1,0,1]\n"
"v_pk_fma_f32 v[10:11], v[178:179], v[168:169], v[10:11] op_sel_hi:[1,0,1]\n"
"v_pk_fma_f32 v[40:41], v[184:185], v[168:169], v[40:41] op_sel_hi:[1,0,1]\n"
"v_pk_fma_f32 v[42:43], v[186:187], v[168:169], v[42:43] op_sel_hi:[1,0,1]\n"
"v_pk_mul_f32 v[200:201], v[8:9], v[8:9]\n"
"v_pk_mul_f32 v[202:203], v[10:11], v[10:11]\n"
"v_pk_mul_f32 v[204:205], v[40:41], v[40:41]\n"
"v_pk_mul_f32 v[206:207], v[42:43], v[42:43]\n"
"v_pk_fma_f32 v[200:201], v[200:201], v[170:171], v[168:169] op_sel:[0,0,1] op_sel_hi:[1,0,1]\n"
"v_pk_fma_f32 v[202:203], v[202:203], v[170:171], v[168:169] op_sel:[0,0,1] op_sel_hi:[1,0,1]\n"
"v_pk_fma_f32 v[204:205], v[204:205], v[170:171], v[168:169] op_sel:[0,0,1] op_sel_hi:[1,0,1]\n"
"v_pk_fma_f32 v[206:207], v[206:207], v[170:171], v[168:169] op_sel:[0,0,1] op_sel_hi:[1,0,1]\n"
"v_pk_mul_f32 v[200:201], v[8:9], v[200:201]\n"
"v_pk_mul_f32 v[202:203], v[10:11], v[202:203]\n"
"v_pk_mul_f32 v[204:205], v[40:41], v[204:205]\n"
"v_pk_mul_f32 v[206:207], v[42:43], v[206:207]\n"
"v_exp_f32 v200, v200\n"
"v_exp_f32 v201, v201\n"
"v_pk_fma_f32 v[24:25], v[180:181], v[168:169], v[24:25] op_sel_hi:[1,0,1]\n"
"v_exp_f32 v202, v202\n"
"v_exp_f32 v203, v203\n"
"v_pk_fma_f32 v[26:27], v[182:183], v[168:169], v[26:27] op_sel_hi:[1,0,1]\n"
"v_exp_f32 v204, v204\n"
"v_exp_f32 v205, v205\n"
"v_pk_fma_f32 v[56:57], v[188:189], v[168:169], v[56:57] op_sel_hi:[1,0,1]\n"
"v_exp_f32 v206, v206\n"
"v_exp_f32 v207, v207\n"
"v_pk_fma_f32 v[58:59], v[190:191], v[168:169], v[58:59] op_sel_hi:[1,0,1]\n"
"v_pk_fma_f32 v[200:201], v[170:171], v[200:201], v[170:171] op_sel:[1,0,1] op_sel_hi:[1,1,1]\n"
"v_pk_fma_f32 v[202:203], v[170:171], v[202:203], v[170:171] op_sel:[1,0,1] op_sel_hi:[1,1,1]\n"
"v_pk_fma_f32 v[204:205], v[170:171], v[204:205], v[170:171] op_sel:[1,0,1] op_sel_hi:[1,1,1]\n"
"v_pk_fma_f32 v[206:207], v[170:171], v[206:207], v[170:171] op_sel:[1,0,1] op_sel_hi:[1,1,1]\n"
"v_rcp_f32 v200, v200\n"
"v_rcp_f32 v201, v201\n"
"v_pk_mul_f32 v[24:25], v[8:9], v[24:25]\n"
"v_rcp_f32 v202, v202\n"
"v_rcp_f32 v203, v203\n"
"v_pk_mul_f32 v[26:27], v[10:11], v[26:27]\n"
"v_rcp_f32 v204, v204\n"
"v_rcp_f32 v205, v205\n"
"v_pk_mul_f32 v[56:57], v[40:41], v[56:57]\n"
"v_rcp_f32 v206, v206\n"
"v_rcp_f32 v207, v207\n"
"v_pk_mul_f32 v[58:59], v[42:43], v[58:59]\n"
"v_pk_mul_f32 v[24:25], v[24:25], v[200:201]\n"
"v_pk_mul_f32 v[26:27], v[26:27], v[202:203]\n"
"v_pk_mul_f32 v[56:57], v[56:57], v[204:205]\n"
"v_pk_mul_f32 v[58:59], v[58:59], v[206:207]\n"
"v_cvt_pk_bf16_f32 v192, v24, v25\n"
"v_cvt_pk_bf16_f32 v193, v26, v27\n"
"v_cvt_pk_bf16_f32 v194, v56, v57\n"
"v_cvt_pk_bf16_f32 v195, v58, v59\n"
"s_add_u32 s60, s78, 294912\n"
"buffer_store_dwordx4 v[192:195], v254, s[32:35], s60 offen offset:0 sc1\n"
"v_pk_fma_f32 v[12:13], v[176:177], v[172:173], v[12:13] op_sel_hi:[1,0,1]\n"
"v_pk_fma_f32 v[14:15], v[178:179], v[172:173], v[14:15] op_sel_hi:[1,0,1]\n"
"v_pk_fma_f32 v[44:45], v[184:185], v[172:173], v[44:45] op_sel_hi:[1,0,1]\n"
"v_pk_fma_f32 v[46:47], v[186:187], v[172:173], v[46:47] op_sel_hi:[1,0,1]\n"
"v_pk_mul_f32 v[200:201], v[12:13], v[12:13]\n"
"v_pk_mul_f32 v[202:203], v[14:15], v[14:15]\n"
"v_pk_mul_f32 v[204:205], v[44:45], v[44:45]\n"
"v_pk_mul_f32 v[206:207], v[46:47], v[46:47]\n"
"v_pk_fma_f32 v[200:201], v[200:201], v[174:175], v[172:173] op_sel:[0,0,1] op_sel_hi:[1,0,1]\n"
"v_pk_fma_f32 v[202:203], v[202:203], v[174:175], v[172:173] op_sel:[0,0,1] op_sel_hi:[1,0,1]\n"
"v_pk_fma_f32 v[204:205], v[204:205], v[174:175], v[172:173] op_sel:[0,0,1] op_sel_hi:[1,0,1]\n"
"v_pk_fma_f32 v[206:207], v[206:207], v[174:175], v[172:173] op_sel:[0,0,1] op_sel_hi:[1,0,1]\n"
"v_pk_mul_f32 v[200:201], v[12:13], v[200:201]\n"
"v_pk_mul_f32 v[202:203], v[14:15], v[202:203]\n"
"v_pk_mul_f32 v[204:205], v[44:45], v[204:205]\n"
"v_pk_mul_f32 v[206:207], v[46:47], v[206:207]\n"
"v_exp_f32 v200, v200\n"
"v_exp_f32 v201, v201\n"
"v_pk_fma_f32 v[28:29], v[180:181], v[172:173], v[28:29] op_sel_hi:[1,0,1]\n"
"v_exp_f32 v202, v202\n"
"v_exp_f32 v203, v203\n"
"v_pk_fma_f32 v[30:31], v[182:183], v[172:173], v[30:31] op_sel_hi:[1,0,1]\n"
"v_exp_f32 v204, v204\n"
"v_exp_f32 v205, v205\n"
"v_pk_fma_f32 v[60:61], v[188:189], v[172:173], v[60:61] op_sel_hi:[1,0,1]\n"
"v_exp_f32 v206, v206\n"
"v_exp_f32 v207, v207\n"
"v_pk_fma_f32 v[62:63], v[190:191], v[172:173], v[62:63] op_sel_hi:[1,0,1]\n"
"v_pk_fma_f32 v[200:201], v[174:175], v[200:201], v[174:175] op_sel:[1,0,1] op_sel_hi:[1,1,1]\n"
"v_pk_fma_f32 v[202:203], v[174:175], v[202:203], v[174:175] op_sel:[1,0,1] op_sel_hi:[1,1,1]\n"
"v_pk_fma_f32 v[204:205], v[174:175], v[204:205], v[174:175] op_sel:[1,0,1] op_sel_hi:[1,1,1]\n"
"v_pk_fma_f32 v[206:207], v[174:175], v[206:207], v[174:175] op_sel:[1,0,1] op_sel_hi:[1,1,1]\n"
"v_rcp_f32 v200, v200\n"
"v_rcp_f32 v201, v201\n"
"v_pk_mul_f32 v[28:29], v[12:13], v[28:29]\n"
"v_rcp_f32 v202, v202\n"
"v_rcp_f32 v203, v203\n"
"v_pk_mul_f32 v[30:31], v[14:15], v[30:31]\n"
"v_rcp_f32 v204, v204\n"
"v_rcp_f32 v205, v205\n"
"v_pk_mul_f32 v[60:61], v[44:45], v[60:61]\n"
"v_rcp_f32 v206, v206\n"
"v_rcp_f32 v207, v207\n"
"v_pk_mul_f32 v[62:63], v[46:47], v[62:63]\n"
"v_pk_mul_f32 v[28:29], v[28:29], v[200:201]\n"
"v_pk_mul_f32 v[30:31], v[30:31], v[202:203]\n"
"v_pk_mul_f32 v[60:61], v[60:61], v[204:205]\n"
"v_pk_mul_f32 v[62:63], v[62:63], v[206:207]\n"
"v_cvt_pk_bf16_f32 v196, v28, v29\n"
"v_cvt_pk_bf16_f32 v197, v30, v31\n"
"v_cvt_pk_bf16_f32 v198, v60, v61\n"
"v_cvt_pk_bf16_f32 v199, v62, v63\n"
"s_add_u32 s60, s78, 442368\n"
"buffer_store_dwordx4 v[196:199], v254, s[32:35], s60 offen offset:0 sc1\n"
"s_setprio 1\n"
"ds_read_b128 v[176:179], v221 offset:128\n"
"ds_read_b128 v[180:183], v221 offset:1152\n"
"ds_read_b128 v[184:187], v221 offset:192\n"
"ds_read_b128 v[188:191], v221 offset:1216\n"
"s_waitcnt lgkmcnt(3)\n"
"v_pk_fma_f32 v[64:65], v[176:177], v[160:161], v[64:65] op_sel_hi:[1,0,1]\n"
"v_pk_fma_f32 v[66:67], v[178:179], v[160:161], v[66:67] op_sel_hi:[1,0,1]\n"
"s_waitcnt lgkmcnt(1)\n"
"v_pk_fma_f32 v[96:97], v[184:185], v[160:161], v[96:97] op_sel_hi:[1,0,1]\n"
"v_pk_fma_f32 v[98:99], v[186:187], v[160:161], v[98:99] op_sel_hi:[1,0,1]\n"
"v_pk_mul_f32 v[200:201], v[64:65], v[64:65]\n"
"v_pk_mul_f32 v[202:203], v[66:67], v[66:67]\n"
"v_pk_mul_f32 v[204:205], v[96:97], v[96:97]\n"
"v_pk_mul_f32 v[206:207], v[98:99], v[98:99]\n"
"v_pk_fma_f32 v[200:201], v[200:201], v[162:163], v[160:161] op_sel:[0,0,1] op_sel_hi:[1,0,1]\n"
"v_pk_fma_f32 v[202:203], v[202:203], v[162:163], v[160:161] op_sel:[0,0,1] op_sel_hi:[1,0,1]\n"
"v_pk_fma_f32 v[204:205], v[204:205], v[162:163], v[160:161] op_sel:[0,0,1] op_sel_hi:[1,0,1]\n"
"v_pk_fma_f32 v[206:207], v[206:207], v[162:163], v[160:161] op_sel:[0,0,1] op_sel_hi:[1,0,1]\n"
"v_pk_mul_f32 v[200:201], v[64:65], v[200:201]\n"
"v_pk_mul_f32 v[202:203], v[66:67], v[202:203]\n"
"v_pk_mul_f32 v[204:205], v[96:97], v[204:205]\n"
"v_pk_mul_f32 v[206:207], v[98:99], v[206:207]\n"
"v_exp_f32 v200, v200\n"
"v_exp_f32 v201, v201\n"
"v_pk_fma_f32 v[80:81], v[180:181], v[160:161], v[80:81] op_sel_hi:[1,0,1]\n"
"v_exp_f32 v202, v202\n"
"v_exp_f32 v203, v203\n"
"v_pk_fma_f32 v[82:83], v[182:183], v[160:161], v[82:83] op_sel_hi:[1,0,1]\n"
"v_exp_f32 v204, v204\n"
"v_exp_f32 v205, v205\n"
"s_waitcnt lgkmcnt(0)\n"
"v_pk_fma_f32 v[112:113], v[188:189], v[160:161], v[112:113] op_sel_hi:[1,0,1]\n"
"v_exp_f32 v206, v206\n"
"v_exp_f32 v207, v207\n"
"v_pk_fma_f32 v[114:115], v[190:191], v[160:161], v[114:115] op_sel_hi:[1,0,1]\n"
"v_pk_fma_f32 v[200:201], v[162:163], v[200:201], v[162:163] op_sel:[1,0,1] op_sel_hi:[1,1,1]\n"
"v_pk_fma_f32 v[202:203], v[162:163], v[202:203], v[162:163] op_sel:[1,0,1] op_sel_hi:[1,1,1]\n"
"v_pk_fma_f32 v[204:205], v[162:163], v[204:205], v[162:163] op_sel:[1,0,1] op_sel_hi:[1,1,1]\n"
"v_pk_fma_f32 v[206:207], v[162:163], v[206:207], v[162:163] op_sel:[1,0,1] op_sel_hi:[1,1,1]\n"
"v_rcp_f32 v200, v200\n"
"v_rcp_f32 v201, v201\n"
"v_pk_mul_f32 v[80:81], v[64:65], v[80:81]\n"
"v_rcp_f32 v202, v202\n"
"v_rcp_f32 v203, v203\n"
"v_pk_mul_f32 v[82:83], v[66:67], v[82:83]\n"
"v_rcp_f32 v204, v204\n"
"v_rcp_f32 v205, v205\n"
"v_pk_mul_f32 v[112:113], v[96:97], v[112:113]\n"
"v_rcp_f32 v206, v206\n"
"v_rcp_f32 v207, v207\n"
"v_pk_mul_f32 v[114:115], v[98:99], v[114:115]\n"
"v_pk_mul_f32 v[80:81], v[80:81], v[200:201]\n"
"v_pk_mul_f32 v[82:83], v[82:83], v[202:203]\n"
"v_pk_mul_f32 v[112:113], v[112:113], v[204:205]\n"
"v_pk_mul_f32 v[114:115], v[114:115], v[206:207]\n"
"v_cvt_pk_bf16_f32 v192, v80, v81\n"
"v_cvt_pk_bf16_f32 v193, v82, v83\n"
"v_cvt_pk_bf16_f32 v194, v112, v113\n"
"v_cvt_pk_bf16_f32 v195, v114, v115\n"
"s_add_u32 s60, s78, 16384\n"
"buffer_store_dwordx4 v[192:195], v254, s[32:35], s60 offen offset:0 sc1\n"
"v_pk_fma_f32 v[68:69], v[176:177], v[164:165], v[68:69] op_sel_hi:[1,0,1]\n"
"v_pk_fma_f32 v[70:71], v[178:179], v[164:165], v[70:71] op_sel_hi:[1,0,1]\n"
"v_pk_fma_f32 v[100:101], v[184:185], v[164:165], v[100:101] op_sel_hi:[1,0,1]\n"
"v_pk_fma_f32 v[102:103], v[186:187], v[164:165], v[102:103] op_sel_hi:[1,0,1]\n"
"v_pk_mul_f32 v[200:201], v[68:69], v[68:69]\n"
"v_pk_mul_f32 v[202:203], v[70:71], v[70:71]\n"
"v_pk_mul_f32 v[204:205], v[100:101], v[100:101]\n"
"v_pk_mul_f32 v[206:207], v[102:103], v[102:103]\n"
"v_pk_fma_f32 v[200:201], v[200:201], v[166:167], v[164:165] op_sel:[0,0,1] op_sel_hi:[1,0,1]\n"
"v_pk_fma_f32 v[202:203], v[202:203], v[166:167], v[164:165] op_sel:[0,0,1] op_sel_hi:[1,0,1]\n"
"v_pk_fma_f32 v[204:205], v[204:205], v[166:167], v[164:165] op_sel:[0,0,1] op_sel_hi:[1,0,1]\n"
"v_pk_fma_f32 v[206:207], v[206:207], v[166:167], v[164:165] op_sel:[0,0,1] op_sel_hi:[1,0,1]\n"
"v_pk_mul_f32 v[200:201], v[68:69], v[200:201]\n"
"v_pk_mul_f32 v[202:203], v[70:71], v[202:203]\n"
"v_pk_mul_f32 v[204:205], v[100:101], v[204:205]\n"
"v_pk_mul_f32 v[206:207], v[102:103], v[206:207]\n"
"v_exp_f32 v200, v200\n"
"v_exp_f32 v201, v201\n"
"v_pk_fma_f32 v[84:85], v[180:181], v[164:165], v[84:85] op_sel_hi:[1,0,1]\n"
"v_exp_f32 v202, v202\n"
"v_exp_f32 v203, v203\n"
"v_pk_fma_f32 v[86:87], v[182:183], v[164:165], v[86:87] op_sel_hi:[1,0,1]\n"
"v_exp_f32 v204, v204\n"
"v_exp_f32 v205, v205\n"
"v_pk_fma_f32 v[116:117], v[188:189], v[164:165], v[116:117] op_sel_hi:[1,0,1]\n"
"v_exp_f32 v206, v206\n"
"v_exp_f32 v207, v207\n"
"v_pk_fma_f32 v[118:119], v[190:191], v[164:165], v[118:119] op_sel_hi:[1,0,1]\n"
"v_pk_fma_f32 v[200:201], v[166:167], v[200:201], v[166:167] op_sel:[1,0,1] op_sel_hi:[1,1,1]\n"
"v_pk_fma_f32 v[202:203], v[166:167], v[202:203], v[166:167] op_sel:[1,0,1] op_sel_hi:[1,1,1]\n"
"v_pk_fma_f32 v[204:205], v[166:167], v[204:205], v[166:167] op_sel:[1,0,1] op_sel_hi:[1,1,1]\n"
"v_pk_fma_f32 v[206:207], v[166:167], v[206:207], v[166:167] op_sel:[1,0,1] op_sel_hi:[1,1,1]\n"
"v_rcp_f32 v200, v200\n"
"v_rcp_f32 v201, v201\n"
"v_pk_mul_f32 v[84:85], v[68:69], v[84:85]\n"
"v_rcp_f32 v202, v202\n"
"v_rcp_f32 v203, v203\n"
"v_pk_mul_f32 v[86:87], v[70:71], v[86:87]\n"
"v_rcp_f32 v204, v204\n"
"v_rcp_f32 v205, v205\n"
"v_pk_mul_f32 v[116:117], v[100:101], v[116:117]\n"
"v_rcp_f32 v206, v206\n"
"v_rcp_f32 v207, v207\n"
"v_pk_mul_f32 v[118:119], v[102:103], v[118:119]\n"
"v_pk_mul_f32 v[84:85], v[84:85], v[200:201]\n"
"v_pk_mul_f32 v[86:87], v[86:87], v[202:203]\n"
"v_pk_mul_f32 v[116:117], v[116:117], v[204:205]\n"
"v_pk_mul_f32 v[118:119], v[118:119], v[206:207]\n"
"v_cvt_pk_bf16_f32 v196, v84, v85\n"
"v_cvt_pk_bf16_f32 v197, v86, v87\n"
"v_cvt_pk_bf16_f32 v198, v116, v117\n"
"v_cvt_pk_bf16_f32 v199, v118, v119\n"
"s_waitcnt vmcnt(1)\n"
"s_mov_b64 exec, 1\n"
"buffer_atomic_add v224, v225, s[64:67], s98 offen offset:0\n"
"s_mov_b64 exec, -1\n"
"s_add_u32 s60, s78, 163840\n"
"buffer_store_dwordx4 v[196:199], v254, s[32:35], s60 offen offset:0 sc1\n"
"v_pk_fma_f32 v[72:73], v[176:177], v[168:169], v[72:73] op_sel_hi:[1,0,1]\n"
"v_pk_fma_f32 v[74:75], v[178:179], v[168:169], v[74:75] op_sel_hi:[1,0,1]\n"
"v_pk_fma_f32 v[104:105], v[184:185], v[168:169], v[104:105] op_sel_hi:[1,0,1]\n"
"v_pk_fma_f32 v[106:107], v[186:187], v[168:169], v[106:107] op_sel_hi:[1,0,1]\n"
"v_pk_mul_f32 v[200:201], v[72:73], v[72:73]\n"
"v_pk_mul_f32 v[202:203], v[74:75], v[74:75]\n"
"v_pk_mul_f32 v[204:205], v[104:105], v[104:105]\n"
"v_pk_mul_f32 v[206:207], v[106:107], v[106:107]\n"
"v_pk_fma_f32 v[200:201], v[200:201], v[170:171], v[168:169] op_sel:[0,0,1] op_sel_hi:[1,0,1]\n"
"v_pk_fma_f32 v[202:203], v[202:203], v[170:171], v[168:169] op_sel:[0,0,1] op_sel_hi:[1,0,1]\n"
"v_pk_fma_f32 v[204:205], v[204:205], v[170:171], v[168:169] op_sel:[0,0,1] op_sel_hi:[1,0,1]\n"
"v_pk_fma_f32 v[206:207], v[206:207], v[170:171], v[168:169] op_sel:[0,0,1] op_sel_hi:[1,0,1]\n"
"v_pk_mul_f32 v[200:201], v[72:73], v[200:201]\n"
"v_pk_mul_f32 v[202:203], v[74:75], v[202:203]\n"
"v_pk_mul_f32 v[204:205], v[104:105], v[204:205]\n"
"v_pk_mul_f32 v[206:207], v[106:107], v[206:207]\n"
"v_exp_f32 v200, v200\n"
"v_exp_f32 v201, v201\n"
"v_pk_fma_f32 v[88:89], v[180:181], v[168:169], v[88:89] op_sel_hi:[1,0,1]\n"
"v_exp_f32 v202, v202\n"
"v_exp_f32 v203, v203\n"
"v_pk_fma_f32 v[90:91], v[182:183], v[168:169], v[90:91] op_sel_hi:[1,0,1]\n"
"v_exp_f32 v204, v204\n"
"v_exp_f32 v205, v205\n"
"v_pk_fma_f32 v[120:121], v[188:189], v[168:169], v[120:121] op_sel_hi:[1,0,1]\n"
"v_exp_f32 v206, v206\n"
"v_exp_f32 v207, v207\n"
"v_pk_fma_f32 v[122:123], v[190:191], v[168:169], v[122:123] op_sel_hi:[1,0,1]\n"
"v_pk_fma_f32 v[200:201], v[170:171], v[200:201], v[170:171] op_sel:[1,0,1] op_sel_hi:[1,1,1]\n"
"v_pk_fma_f32 v[202:203], v[170:171], v[202:203], v[170:171] op_sel:[1,0,1] op_sel_hi:[1,1,1]\n"
"v_pk_fma_f32 v[204:205], v[170:171], v[204:205], v[170:171] op_sel:[1,0,1] op_sel_hi:[1,1,1]\n"
"v_pk_fma_f32 v[206:207], v[170:171], v[206:207], v[170:171] op_sel:[1,0,1] op_sel_hi:[1,1,1]\n"
"v_rcp_f32 v200, v200\n"
"v_rcp_f32 v201, v201\n"
"v_pk_mul_f32 v[88:89], v[72:73], v[88:89]\n"
"v_rcp_f32 v202, v202\n"
"v_rcp_f32 v203, v203\n"
"v_pk_mul_f32 v[90:91], v[74:75], v[90:91]\n"
"v_rcp_f32 v204, v204\n"
"v_rcp_f32 v205, v205\n"
"v_pk_mul_f32 v[120:121], v[104:105], v[120:121]\n"
"v_rcp_f32 v206, v206\n"
"v_rcp_f32 v207, v207\n"
"v_pk_mul_f32 v[122:123], v[106:107], v[122:123]\n"
"v_pk_mul_f32 v[88:89], v[88:89], v[200:201]\n"
"v_pk_mul_f32 v[90:91], v[90:91], v[202:203]\n"
"v_pk_mul_f32 v[120:121], v[120:121], v[204:205]\n"
"v_pk_mul_f32 v[122:123], v[122:123], v[206:207]\n"
"v_cvt_pk_bf16_f32 v192, v88, v89\n"
"v_cvt_pk_bf16_f32 v193, v90, v91\n"
"v_cvt_pk_bf16_f32 v194, v120, v121\n"
"v_cvt_pk_bf16_f32 v195, v122, v123\n"
"s_add_u32 s60, s78, 311296\n"
"buffer_store_dwordx4 v[192:195], v254, s[32:35], s60 offen offset:0 sc1\n"
"v_pk_fma_f32 v[76:77], v[176:177], v[172:173], v[76:77] op_sel_hi:[1,0,1]\n"
"v_pk_fma_f32 v[78:79], v[178:179], v[172:173], v[78:79] op_sel_hi:[1,0,1]\n"
"v_pk_fma_f32 v[108:109], v[184:185], v[172:173], v[108:109] op_sel_hi:[1,0,1]\n"
"v_pk_fma_f32 v[110:111], v[186:187], v[172:173], v[110:111] op_sel_hi:[1,0,1]\n"
"v_pk_mul_f32 v[200:201], v[76:77], v[76:77]\n"
"v_pk_mul_f32 v[202:203], v[78:79], v[78:79]\n"
"v_pk_mul_f32 v[204:205], v[108:109], v[108:109]\n"
"v_pk_mul_f32 v[206:207], v[110:111], v[110:111]\n"
"v_pk_fma_f32 v[200:201], v[200:201], v[174:175], v[172:173] op_sel:[0,0,1] op_sel_hi:[1,0,1]\n"
"v_pk_fma_f32 v[202:203], v[202:203], v[174:175], v[172:173] op_sel:[0,0,1] op_sel_hi:[1,0,1]\n"
"v_pk_fma_f32 v[204:205], v[204:205], v[174:175], v[172:173] op_sel:[0,0,1] op_sel_hi:[1,0,1]\n"
"v_pk_fma_f32 v[206:207], v[206:207], v[174:175], v[172:173] op_sel:[0,0,1] op_sel_hi:[1,0,1]\n"
"v_pk_mul_f32 v[200:201], v[76:77], v[200:201]\n"
"v_pk_mul_f32 v[202:203], v[78:79], v[202:203]\n"
"v_pk_mul_f32 v[204:205], v[108:109], v[204:205]\n"
"v_pk_mul_f32 v[206:207], v[110:111], v[206:207]\n"
"v_exp_f32 v200, v200\n"
"v_exp_f32 v201, v201\n"
"v_pk_fma_f32 v[92:93], v[180:181], v[172:173], v[92:93] op_sel_hi:[1,0,1]\n"
"v_exp_f32 v202, v202\n"
"v_exp_f32 v203, v203\n"
"v_pk_fma_f32 v[94:95], v[182:183], v[172:173], v[94:95] op_sel_hi:[1,0,1]\n"
"v_exp_f32 v204, v204\n"
"v_exp_f32 v205, v205\n"
"v_pk_fma_f32 v[124:125], v[188:189], v[172:173], v[124:125] op_sel_hi:[1,0,1]\n"
"v_exp_f32 v206, v206\n"
"v_exp_f32 v207, v207\n"
"v_pk_fma_f32 v[126:127], v[190:191], v[172:173], v[126:127] op_sel_hi:[1,0,1]\n"
"v_pk_fma_f32 v[200:201], v[174:175], v[200:201], v[174:175] op_sel:[1,0,1] op_sel_hi:[1,1,1]\n"
"v_pk_fma_f32 v[202:203], v[174:175], v[202:203], v[174:175] op_sel:[1,0,1] op_sel_hi:[1,1,1]\n"
"v_pk_fma_f32 v[204:205], v[174:175], v[204:205], v[174:175] op_sel:[1,0,1] op_sel_hi:[1,1,1]\n"
"v_pk_fma_f32 v[206:207], v[174:175], v[206:207], v[174:175] op_sel:[1,0,1] op_sel_hi:[1,1,1]\n"
"v_rcp_f32 v200, v200\n"
"v_rcp_f32 v201, v201\n"
"v_pk_mul_f32 v[92:93], v[76:77], v[92:93]\n"
"v_rcp_f32 v202, v202\n"
"v_rcp_f32 v203, v203\n"
"v_pk_mul_f32 v[94:95], v[78:79], v[94:95]\n"
"v_rcp_f32 v204, v204\n"
"v_rcp_f32 v205, v205\n"
"v_pk_mul_f32 v[124:125], v[108:109], v[124:125]\n"
"v_rcp_f32 v206, v206\n"
"v_rcp_f32 v207, v207\n"
"v_pk_mul_f32 v[126:127], v[110:111], v[126:127]\n"
"v_pk_mul_f32 v[92:93], v[92:93], v[200:201]\n"
"v_pk_mul_f32 v[94:95], v[94:95], v[202:203]\n"
"v_pk_mul_f32 v[124:125], v[124:125], v[204:205]\n"
"v_pk_mul_f32 v[126:127], v[126:127], v[206:207]\n"
"v_cvt_pk_bf16_f32 v196, v92, v93\n"
"v_cvt_pk_bf16_f32 v197, v94, v95\n"
"v_cvt_pk_bf16_f32 v198, v124, v125\n"
"v_cvt_pk_bf16_f32 v199, v126, v127\n"
"s_add_u32 s60, s78, 458752\n"
"buffer_store_dwordx4 v[196:199], v254, s[32:35], s60 offen offset:0 sc1\n"
"s_setprio 2\n"
"ds_read_b128 v[176:179], v221 offset:512\n"
"ds_read_b128 v[180:183], v221 offset:1536\n"
"s_waitcnt lgkmcnt(1)\n"
"v_pk_fma_f32 v[128:129], v[176:177], v[160:161], v[128:129] op_sel_hi:[1,0,1]\n"
"v_pk_fma_f32 v[130:131], v[178:179], v[160:161], v[130:131] op_sel_hi:[1,0,1]\n"
"v_pk_fma_f32 v[132:133], v[176:177], v[164:165], v[132:133] op_sel_hi:[1,0,1]\n"
"v_pk_fma_f32 v[134:135], v[178:179], v[164:165], v[134:135] op_sel_hi:[1,0,1]\n"
"v_pk_mul_f32 v[200:201], v[128:129], v[128:129]\n"
"v_pk_mul_f32 v[202:203], v[130:131], v[130:131]\n"
"v_pk_mul_f32 v[204:205], v[132:133], v[132:133]\n"
"v_pk_mul_f32 v[206:207], v[134:135], v[134:135]\n"
"v_pk_fma_f32 v[200:201], v[200:201], v[162:163], v[160:161] op_sel:[0,0,1] op_sel_hi:[1,0,1]\n"
"v_pk_fma_f32 v[202:203], v[202:203], v[162:163], v[160:161] op_sel:[0,0,1] op_sel_hi:[1,0,1]\n"
"v_pk_fma_f32 v[204:205], v[204:205], v[166:167], v[164:165] op_sel:[0,0,1] op_sel_hi:[1,0,1]\n"
"v_pk_fma_f32 v[206:207], v[206:207], v[166:167], v[164:165] op_sel:[0,0,1] op_sel_hi:[1,0,1]\n"
"v_pk_mul_f32 v[200:201], v[128:129], v[200:201]\n"
"v_pk_mul_f32 v[202:203], v[130:131], v[202:203]\n"
"v_pk_mul_f32 v[204:205], v[132:133], v[204:205]\n"
"v_pk_mul_f32 v[206:207], v[134:135], v[206:207]\n"
"v_exp_f32 v200, v200\n"
"v_exp_f32 v201, v201\n"
"s_waitcnt lgkmcnt(0)\n"
"v_pk_fma_f32 v[144:145], v[180:181], v[160:161], v[144:145] op_sel_hi:[1,0,1]\n"
"v_exp_f32 v202, v202\n"
"v_exp_f32 v203, v203\n"
"v_pk_fma_f32 v[146:147], v[182:183], v[160:161], v[146:147] op_sel_hi:[1,0,1]\n"
"v_exp_f32 v204, v204\n"
"v_exp_f32 v205, v205\n"
"v_pk_fma_f32 v[148:149], v[180:181], v[164:165], v[148:149] op_sel_hi:[1,0,1]\n"
"v_exp_f32 v206, v206\n"
"v_exp_f32 v207, v207\n"
"v_pk_fma_f32 v[150:151], v[182:183], v[164:165], v[150:151] op_sel_hi:[1,0,1]\n"
"v_pk_fma_f32 v[200:201], v[162:163], v[200:201], v[162:163] op_sel:[1,0,1] op_sel_hi:[1,1,1]\n"
"v_pk_fma_f32 v[202:203], v[162:163], v[202:203], v[162:163] op_sel:[1,0,1] op_sel_hi:[1,1,1]\n"
"v_pk_fma_f32 v[204:205], v[166:167], v[204:205], v[166:167] op_sel:[1,0,1] op_sel_hi:[1,1,1]\n"
"v_pk_fma_f32 v[206:207], v[166:167], v[206:207], v[166:167] op_sel:[1,0,1] op_sel_hi:[1,1,1]\n"
"v_rcp_f32 v200, v200\n"
"v_rcp_f32 v201, v201\n"
"v_pk_mul_f32 v[144:145], v[128:129], v[144:145]\n"
"v_rcp_f32 v202, v202\n"
"v_rcp_f32 v203, v203\n"
"v_pk_mul_f32 v[146:147], v[130:131], v[146:147]\n"
"v_rcp_f32 v204, v204\n"
"v_rcp_f32 v205, v205\n"
"v_pk_mul_f32 v[148:149], v[132:133], v[148:149]\n"
"v_rcp_f32 v206, v206\n"
"v_rcp_f32 v207, v207\n"
"v_pk_mul_f32 v[150:151], v[134:135], v[150:151]\n"
"v_pk_mul_f32 v[144:145], v[144:145], v[200:201]\n"
"v_pk_mul_f32 v[146:147], v[146:147], v[202:203]\n"
"v_pk_mul_f32 v[148:149], v[148:149], v[204:205]\n"
"v_pk_mul_f32 v[150:151], v[150:151], v[206:207]\n"
"v_cvt_pk_bf16_f32 v192, v144, v145\n"
"v_cvt_pk_bf16_f32 v193, v146, v147\n"
"v_cvt_pk_bf16_f32 v194, v148, v149\n"
"v_cvt_pk_bf16_f32 v195, v150, v151\n"
"v_mov_b32 v227, 0\n"
"s_mov_b64 exec, 1\n"
"buffer_atomic_add v227, v226, s[64:67], s99 offen offset:0 sc0\n"
"s_mov_b64 exec, -1\n"
"s_add_u32 s60, s79, 0\n"
"buffer_store_dwordx2 v[192:193], v223, s[32:35], s60 offen offset:0 sc1\n"
"s_add_u32 s60, s79, 147456\n"
"buffer_store_dwordx2 v[194:195], v223, s[32:35], s60 offen offset:0 sc1\n"
"v_pk_fma_f32 v[136:137], v[176:177], v[168:169], v[136:137] op_sel_hi:[1,0,1]\n"
"v_pk_fma_f32 v[138:139], v[178:179], v[168:169], v[138:139] op_sel_hi:[1,0,1]\n"
"v_pk_fma_f32 v[140:141], v[176:177], v[172:173], v[140:141] op_sel_hi:[1,0,1]\n"
"v_pk_fma_f32 v[142:143], v[178:179], v[172:173], v[142:143] op_sel_hi:[1,0,1]\n"
"v_pk_mul_f32 v[200:201], v[136:137], v[136:137]\n"
"v_pk_mul_f32 v[202:203], v[138:139], v[138:139]\n"
"v_pk_mul_f32 v[204:205], v[140:141], v[140:141]\n"
"v_pk_mul_f32 v[206:207], v[142:143], v[142:143]\n"
"v_pk_fma_f32 v[200:201], v[200:201], v[170:171], v[168:169] op_sel:[0,0,1] op_sel_hi:[1,0,1]\n"
"v_pk_fma_f32 v[202:203], v[202:203], v[170:171], v[168:169] op_sel:[0,0,1] op_sel_hi:[1,0,1]\n"
"v_pk_fma_f32 v[204:205], v[204:205], v[174:175], v[172:173] op_sel:[0,0,1] op_sel_hi:[1,0,1]\n"
"v_pk_fma_f32 v[206:207], v[206:207], v[174:175], v[172:173] op_sel:[0,0,1] op_sel_hi:[1,0,1]\n"
"v_pk_mul_f32 v[200:201], v[136:137], v[200:201]\n"
"v_pk_mul_f32 v[202:203], v[138:139], v[202:203]\n"
"v_pk_mul_f32 v[204:205], v[140:141], v[204:205]\n"
"v_pk_mul_f32 v[206:207], v[142:143], v[206:207]\n"
"v_exp_f32 v200, v200\n"
"v_exp_f32 v201, v201\n"
"v_pk_fma_f32 v[152:153], v[180:181], v[168:169], v[152:153] op_sel_hi:[1,0,1]\n"
"v_exp_f32 v202, v202\n"
"v_exp_f32 v203, v203\n"
"v_pk_fma_f32 v[154:155], v[182:183], v[168:169], v[154:155] op_sel_hi:[1,0,1]\n"
"v_exp_f32 v204, v204\n"
"v_exp_f32 v205, v205\n"
"v_pk_fma_f32 v[156:157], v[180:181], v[172:173], v[156:157] op_sel_hi:[1,0,1]\n"
"v_exp_f32 v206, v206\n"
"v_exp_f32 v207, v207\n"
"v_pk_fma_f32 v[158:159], v[182:183], v[172:173], v[158:159] op_sel_hi:[1,0,1]\n"
"v_pk_fma_f32 v[200:201], v[170:171], v[200:201], v[170:171] op_sel:[1,0,1] op_sel_hi:[1,1,1]\n"
"v_pk_fma_f32 v[202:203], v[170:171], v[202:203], v[170:171] op_sel:[1,0,1] op_sel_hi:[1,1,1]\n"
"v_pk_fma_f32 v[204:205], v[174:175], v[204:205], v[174:175] op_sel:[1,0,1] op_sel_hi:[1,1,1]\n"
"v_pk_fma_f32 v[206:207], v[174:175], v[206:207], v[174:175] op_sel:[1,0,1] op_sel_hi:[1,1,1]\n"
"v_rcp_f32 v200, v200\n"
"v_rcp_f32 v201, v201\n"
"v_pk_mul_f32 v[152:153], v[136:137], v[152:153]\n"
"v_rcp_f32 v202, v202\n"
"v_rcp_f32 v203, v203\n"
"v_pk_mul_f32 v[154:155], v[138:139], v[154:155]\n"
"v_rcp_f32 v204, v204\n"
"v_rcp_f32 v205, v205\n"
"v_pk_mul_f32 v[156:157], v[140:141], v[156:157]\n"
"v_rcp_f32 v206, v206\n"
"v_rcp_f32 v207, v207\n"
"v_pk_mul_f32 v[158:159], v[142:143], v[158:159]\n"
"v_pk_mul_f32 v[152:153], v[152:153], v[200:201]\n"
"v_pk_mul_f32 v[154:155], v[154:155], v[202:203]\n"
"v_pk_mul_f32 v[156:157], v[156:157], v[204:205]\n"
"v_pk_mul_f32 v[158:159], v[158:159], v[206:207]\n"
"v_cvt_pk_bf16_f32 v196, v152, v153\n"
"v_cvt_pk_bf16_f32 v197, v154, v155\n"
"v_cvt_pk_bf16_f32 v198, v156, v157\n"
"v_cvt_pk_bf16_f32 v199, v158, v159\n"
"s_add_u32 s60, s79, 294912\n"
"buffer_store_dwordx2 v[196:197], v223, s[32:35], s60 offen offset:0 sc1\n"
"s_add_u32 s60, s79, 442368\n"
"buffer_store_dwordx2 v[198:199], v223, s[32:35], s60 offen offset:0 sc1\n"
"s_setprio 3\n"
"s_waitcnt vmcnt(4)\n"
"v_readfirstlane_b32 s54, v227\n"
"s_nop 0\n"
"s_cmp_lg_u32 s54, 16\n"
"s_cbranch_scc1 .Lf_gs0a\n"
".Lf_gok0a:\n"
"s_mov_b64 exec, 2\n"
"buffer_atomic_add v224, v225, s[64:67], s98 offen offset:0\n"
"s_mov_b64 exec, -1\n"
"s_add_u32 s60, s47, 0\n"
"s_add_u32 m0, s44, 32768\n"
"s_nop 0\n"
"buffer_load_dwordx4 v216, s[32:35], s60 offen offset:0 sc1 lds\n"
"buffer_load_dwordx4 v216, s[32:35], s60 offen offset:1024 sc1 lds\n"
"buffer_load_dwordx4 v217, s[32:35], s60 offen offset:2048 sc1 lds\n"
"buffer_load_dwordx4 v217, s[32:35], s60 offen offset:3072 sc1 lds\n"
"s_add_u32 s60, s47, 2048\n"
"s_add_u32 m0, s44, 49152\n"
"s_nop 0\n"
"buffer_load_dwordx4 v216, s[32:35], s60 offen offset:0 sc1 lds\n"
"buffer_load_dwordx4 v216, s[32:35], s60 offen offset:1024 sc1 lds\n"
"buffer_load_dwordx4 v217, s[32:35], s60 offen offset:2048 sc1 lds\n"
"buffer_load_dwordx4 v217, s[32:35], s60 offen offset:3072 sc1 lds\n"
"s_add_u32 s60, s47, 4096\n"
"s_add_u32 m0, s44, 0\n"
"s_nop 0\n"
"buffer_load_dwordx4 v216, s[32:35], s60 offen offset:0 sc1 lds\n"
"buffer_load_dwordx4 v216, s[32:35], s60 offen offset:1024 sc1 lds\n"
"buffer_load_dwordx4 v217, s[32:35], s60 offen offset:2048 sc1 lds\n"
"buffer_load_dwordx4 v217, s[32:35], s60 offen offset:3072 sc1 lds\n"
"s_add_u32 s60, s47, 6144\n"
"s_add_u32 m0, s44, 16384\n"
"s_nop 0\n"
"buffer_load_dwordx4 v216, s[32:35], s60 offen offset:0 sc1 lds\n"
"buffer_load_dwordx4 v216, s[32:35], s60 offen offset:1024 sc1 lds\n"
"buffer_load_dwordx4 v217, s[32:35], s60 offen offset:2048 sc1 lds\n"
"buffer_load_dwordx4 v217, s[32:35], s60 offen offset:3072 sc1 lds\n"
"s_setprio 0\n"
"v_lshrrev_b32 v166, 4, v219\n"
"v_bfe_u32 v167, v219, 2, 2\n"
"v_and_b32 v168, 3, v219\n"
"v_lshlrev_b32 v168, 3, v168\n"
"v_lshl_add_u32 v169, v166, 3, v167\n"
"v_lshl_add_u32 v169, v169, 5, v168\n"
"v_add_u32 v169, s77, v169\n"
"s_waitcnt vmcnt(31)\n"
"ds_read_b64_tr_b16 v[200:201], v169 offset:0\n"
"ds_read_b64_tr_b16 v[202:203], v169 offset:128\n"
"s_lshr_b32 s54, s53, 1\n"
"s_lshl_b32 s54, s54, 5\n"
"s_add_u32 s54, s54, s77\n"
"v_lshl_add_u32 v170, v166, 2, v167\n"
"v_lshl_add_u32 v170, v170, 6, v168\n"
"v_add_u32 v170, s54, v170\n"
"v_xor_b32 v171, 32, v170\n"
"ds_read_b64_tr_b16 v[204:205], v170 offset:8192\n"
"ds_read_b64_tr_b16 v[206:207], v171 offset:8192\n"
"s_lshr_b32 s54, s53, 1\n"
"s_lshl_b32 s54, s54, 11\n"
"s_lshl_b32 s55, s50, 12\n"
"s_add_u32 s54, s54, s55\n"
"v_add_u32 v152, s54, v254\n"
"v_xor_b32 v153, 0x800, v152\n"
"s_and_b32 s54, s53, 1\n"
"s_lshl_b32 s54, s54, 11\n"
"s_add_u32 s55, s54, 65536\n"
"v_add_u32 v154, s55, v254\n"
"s_xor_b32 s54, s54, 2048\n"
"s_add_u32 s55, s54, 65536\n"
"v_add_u32 v156, s55, v254\n"
"v_add_u32 v155, 0x9000, v154\n"
"v_add_u32 v157, 0x9000, v156\n"
"v_add_u32 v158, 0x12000, v254\n"
"v_add_u32 v159, 0x9000, v158\n"
"v_lshlrev_b32 v162, 3, v219\n"
"v_sub_u32 v163, v153, v162\n"
"v_sub_u32 v162, v152, v162\n"
"s_waitcnt lgkmcnt(3)\n"
"v_lshlrev_b32 v0, 16, v200\n"
"v_and_b32 v1, 0xffff0000, v200\n"
"v_lshlrev_b32 v2, 16, v201\n"
"v_and_b32 v3, 0xffff0000, v201\n"
"s_waitcnt lgkmcnt(2)\n"
"v_lshlrev_b32 v8, 16, v202\n"
"v_and_b32 v9, 0xffff0000, v202\n"
"v_lshlrev_b32 v10, 16, v203\n"
"v_and_b32 v11, 0xffff0000, v203\n"
"s_cmp_eq_u32 s50, s53\n"
"s_cselect_b32 s54, -1, 0\n"
"s_waitcnt lgkmcnt(1)\n"
"v_lshlrev_b32 v32, 16, v204\n"
"v_and_b32 v33, 0xffff0000, v204\n"
"v_and_b32 v32, s54, v32\n"
"v_and_b32 v33, s54, v33\n"
"v_lshlrev_b32 v34, 16, v205\n"
"v_and_b32 v35, 0xffff0000, v205\n"
"v_and_b32 v34, s54, v34\n"
"v_and_b32 v35, s54, v35\n"
"s_waitcnt lgkmcnt(0)\n"
"v_lshlrev_b32 v36, 16, v206\n"
"v_and_b32 v37, 0xffff0000, v206\n"
"v_and_b32 v36, s54, v36\n"
"v_and_b32 v37, s54, v37\n"
"v_lshlrev_b32 v38, 16, v207\n"
"v_and_b32 v39, 0xffff0000, v207\n"
"v_and_b32 v38, s54, v38\n"
"v_and_b32 v39, s54, v39\n"
"s_waitcnt vmcnt(12)\n"
"s_barrier\n"
"ds_read_b128 v[40:43], v152 offset:32768\n"
"ds_read_b128 v[44:47], v152 offset:33792\n"
"ds_read_b128 v[48:51], v153 offset:32768\n"
"ds_read_b128 v[52:55], v153 offset:33792\n"
"ds_read_b128 v[72:75], v157 offset:0\n"
"ds_read_b128 v[76:79], v157 offset:9216\n"
"ds_read_b128 v[80:83], v157 offset:1024\n"
"ds_read_b128 v[84:87], v157 offset:10240\n"
"ds_read_b128 v[88:91], v159 offset:0\n"
"ds_read_b128 v[92:95], v159 offset:9216\n"
"ds_read_b128 v[56:59], v155 offset:0\n"
"ds_read_b128 v[60:63], v155 offset:9216\n"
"ds_read_b128 v[64:67], v155 offset:1024\n"
"ds_read_b128 v[68:71], v155 offset:10240\n"
"s_waitcnt vmcnt(8)\n"
"s_waitcnt lgkmcnt(0)\n"
"s_barrier\n"
"v_mfma_f32_16x16x32_bf16 v[20:23], v[72:75], v[48:51], 0\n"
"s_add_u32 s60, s47, 8192\n"
"s_add_u32 m0, s44, 32768\n"
"s_nop 0\n"
"buffer_load_dwordx4 v216, s[32:35], s60 offen offset:0 sc1 lds\n"
"v_mfma_f32_16x16x32_bf16 v[20:23], v[76:79], v[52:55], v[20:23]\n"
"ds_read_b128 v[96:99], v152 offset:49152\n"
"v_mfma_f32_16x16x32_bf16 v[16:19], v[72:75], v[40:43], 0\n"
"buffer_load_dwordx4 v216, s[32:35], s60 offen offset:1024 sc1 lds\n"
"v_mfma_f32_16x16x32_bf16 v[16:19], v[76:79], v[44:47], v[16:19]\n"
"ds_read_b128 v[100:103], v152 offset:50176\n"
"v_mfma_f32_16x16x32_bf16 v[28:31], v[80:83], v[48:51], 0\n"
"buffer_load_dwordx4 v217, s[32:35], s60 offen offset:2048 sc1 lds\n"
"v_mfma_f32_16x16x32_bf16 v[28:31], v[84:87], v[52:55], v[28:31]\n"
"ds_read_b128 v[104:107], v153 offset:49152\n"
"v_mfma_f32_16x16x32_bf16 v[24:27], v[80:83], v[40:43], 0\n"
"buffer_load_dwordx4 v217, s[32:35], s60 offen offset:3072 sc1 lds\n"
"v_mfma_f32_16x16x32_bf16 v[24:27], v[84:87], v[44:47], v[24:27]\n"
"ds_read_b128 v[108:111], v153 offset:50176\n"
"v_mfma_f32_16x16x32_bf16 v[36:39], v[88:91], v[48:51], v[36:39]\n"
"ds_read_b128 v[128:131], v157 offset:18432\n"
"v_mfma_f32_16x16x32_bf16 v[36:39], v[92:95], v[52:55], v[36:39]\n"
"ds_read_b128 v[132:135], v157 offset:27648\n"
"v_mfma_f32_16x16x32_bf16 v[32:35], v[88:91], v[40:43], v[32:35]\n"
"ds_read_b128 v[136:139], v157 offset:19456\n"
"v_mfma_f32_16x16x32_bf16 v[32:35], v[92:95], v[44:47], v[32:35]\n"
"ds_read_b128 v[140:143], v157 offset:28672\n"
"v_mfma_f32_16x16x32_bf16 v[4:7], v[56:59], v[48:51], 0\n"
"ds_read_b128 v[144:147], v159 offset:18432\n"
"v_mfma_f32_16x16x32_bf16 v[4:7], v[60:63], v[52:55], v[4:7]\n"
"ds_read_b128 v[148:151], v159 offset:27648\n"
"v_mfma_f32_16x16x32_bf16 v[0:3], v[56:59], v[40:43], v[0:3]\n"
"ds_read_b128 v[112:115], v155 offset:18432\n"
"v_mfma_f32_16x16x32_bf16 v[0:3], v[60:63], v[44:47], v[0:3]\n"
"ds_read_b128 v[116:119], v155 offset:27648\n"
"v_mfma_f32_16x16x32_bf16 v[12:15], v[64:67], v[48:51], 0\n"
"ds_read_b128 v[120:123], v155 offset:19456\n"
"v_mfma_f32_16x16x32_bf16 v[12:15], v[68:71], v[52:55], v[12:15]\n"
"ds_read_b128 v[124:127], v155 offset:28672\n"
"v_mfma_f32_16x16x32_bf16 v[8:11], v[64:67], v[40:43], v[8:11]\n"
"v_mfma_f32_16x16x32_bf16 v[8:11], v[68:71], v[44:47], v[8:11]\n"
"s_waitcnt vmcnt(8)\n"
"s_waitcnt lgkmcnt(0)\n"
"s_barrier\n"
"v_mfma_f32_16x16x32_bf16 v[20:23], v[128:131], v[104:107], v[20:23]\n"
"s_add_u32 s60, s47, 10240\n"
"s_add_u32 m0, s44, 49152\n"
"s_nop 0\n"
"buffer_load_dwordx4 v216, s[32:35], s60 offen offset:0 sc1 lds\n"
"ds_read_b128 v[40:43], v152 offset:0\n"
"v_mfma_f32_16x16x32_bf16 v[20:23], v[132:135], v[108:111], v[20:23]\n"
"buffer_load_dwordx4 v216, s[32:35], s60 offen offset:1024 sc1 lds\n"
"v_mfma_f32_16x16x32_bf16 v[16:19], v[128:131], v[96:99], v[16:19]\n"
"ds_read_b128 v[44:47], v152 offset:1024\n"
"v_mfma_f32_16x16x32_bf16 v[16:19], v[132:135], v[100:103], v[16:19]\n"
"buffer_load_dwordx4 v217, s[32:35], s60 offen offset:2048 sc1 lds\n"
"v_mfma_f32_16x16x32_bf16 v[28:31], v[136:139], v[104:107], v[28:31]\n"
"ds_read_b128 v[48:51], v153 offset:0\n"
"v_mfma_f32_16x16x32_bf16 v[28:31], v[140:143], v[108:111], v[28:31]\n"
"buffer_load_dwordx4 v217, s[32:35], s60 offen offset:3072 sc1 lds\n"
"v_mfma_f32_16x16x32_bf16 v[24:27], v[136:139], v[96:99], v[24:27]\n"
"ds_read_b128 v[52:55], v153 offset:1024\n"
"v_mfma_f32_16x16x32_bf16 v[24:27], v[140:143], v[100:103], v[24:27]\n"
"s_waitcnt vmcnt(25)\n"
"s_mov_b64 exec, 4\n"
"buffer_atomic_add v224, v225, s[64:67], s98 offen offset:0\n"
"s_mov_b64 exec, -1\n"
"v_mfma_f32_16x16x32_bf16 v[36:39], v[144:147], v[104:107], v[36:39]\n"
"ds_read_b128 v[72:75], v156 offset:0\n"
"v_mfma_f32_16x16x32_bf16 v[36:39], v[148:151], v[108:111], v[36:39]\n"
"ds_read_b128 v[76:79], v156 offset:9216\n"
"v_mfma_f32_16x16x32_bf16 v[32:35], v[144:147], v[96:99], v[32:35]\n"
"ds_read_b128 v[80:83], v156 offset:1024\n"
"v_mfma_f32_16x16x32_bf16 v[32:35], v[148:151], v[100:103], v[32:35]\n"
"ds_read_b128 v[84:87], v156 offset:10240\n"
"v_mfma_f32_16x16x32_bf16 v[4:7], v[112:115], v[104:107], v[4:7]\n"
"ds_read_b128 v[88:91], v158 offset:0\n"
"v_mfma_f32_16x16x32_bf16 v[4:7], v[116:119], v[108:111], v[4:7]\n"
"ds_read_b128 v[92:95], v158 offset:9216\n"
"v_mfma_f32_16x16x32_bf16 v[0:3], v[112:115], v[96:99], v[0:3]\n"
"ds_read_b128 v[56:59], v154 offset:0\n"
"v_mfma_f32_16x16x32_bf16 v[0:3], v[116:119], v[100:103], v[0:3]\n"
"ds_read_b128 v[60:63], v154 offset:9216\n"
"v_mfma_f32_16x16x32_bf16 v[12:15], v[120:123], v[104:107], v[12:15]\n"
"ds_read_b128 v[64:67], v154 offset:1024\n"
"v_mfma_f32_16x16x32_bf16 v[12:15], v[124:127], v[108:111], v[12:15]\n"
"ds_read_b128 v[68:71], v154 offset:10240\n"
"v_mfma_f32_16x16x32_bf16 v[8:11], v[120:123], v[96:99], v[8:11]\n"
"v_mfma_f32_16x16x32_bf16 v[8:11], v[124:127], v[100:103], v[8:11]\n"
"s_waitcnt vmcnt(9)\n"
"s_waitcnt lgkmcnt(0)\n"
"s_barrier\n"
"v_mfma_f32_16x16x32_bf16 v[20:23], v[72:75], v[48:51], v[20:23]\n"
"s_add_u32 s60, s47, 12288\n"
"s_add_u32 m0, s44, 0\n"
"s_nop 0\n"
"buffer_load_dwordx4 v216, s[32:35], s60 offen offset:0 sc1 lds\n"
"ds_read_b128 v[96:99], v152 offset:16384\n"
"v_mfma_f32_16x16x32_bf16 v[20:23], v[76:79], v[52:55], v[20:23]\n"
"buffer_load_dwordx4 v216, s[32:35], s60 offen offset:1024 sc1 lds\n"
"v_mfma_f32_16x16x32_bf16 v[16:19], v[72:75], v[40:43], v[16:19]\n"
"ds_read_b128 v[100:103], v152 offset:17408\n"
"v_mfma_f32_16x16x32_bf16 v[16:19], v[76:79], v[44:47], v[16:19]\n"
"buffer_load_dwordx4 v217, s[32:35], s60 offen offset:2048 sc1 lds\n"
"v_mfma_f32_16x16x32_bf16 v[28:31], v[80:83], v[48:51], v[28:31]\n"
"ds_read_b128 v[104:107], v153 offset:16384\n"
"v_mfma_f32_16x16x32_bf16 v[28:31], v[84:87], v[52:55], v[28:31]\n"
"buffer_load_dwordx4 v217, s[32:35], s60 offen offset:3072 sc1 lds\n"
"v_mfma_f32_16x16x32_bf16 v[24:27], v[80:83], v[40:43], v[24:27]\n"
"ds_read_b128 v[108:111], v153 offset:17408\n"
"v_mfma_f32_16x16x32_bf16 v[24:27], v[84:87], v[44:47], v[24:27]\n"
"v_mov_b32 v227, 0\n"
"s_mov_b64 exec, 1\n"
"buffer_atomic_add v227, v226, s[64:67], s99 offen offset:64 sc0\n"
"s_mov_b64 exec, -1\n"
"v_mfma_f32_16x16x32_bf16 v[36:39], v[88:91], v[48:51], v[36:39]\n"
"ds_read_b128 v[128:131], v156 offset:18432\n"
"v_mfma_f32_16x16x32_bf16 v[36:39], v[92:95], v[52:55], v[36:39]\n"
"ds_read_b128 v[132:135], v156 offset:27648\n"
"v_mfma_f32_16x16x32_bf16 v[32:35], v[88:91], v[40:43], v[32:35]\n"
"ds_read_b128 v[136:139], v156 offset:19456\n"
"v_mfma_f32_16x16x32_bf16 v[32:35], v[92:95], v[44:47], v[32:35]\n"
"ds_read_b128 v[140:143], v156 offset:28672\n"
"v_mfma_f32_16x16x32_bf16 v[4:7], v[56:59], v[48:51], v[4:7]\n"
"ds_read_b128 v[144:147], v158 offset:18432\n"
"v_mfma_f32_16x16x32_bf16 v[4:7], v[60:63], v[52:55], v[4:7]\n"
"ds_read_b128 v[148:151], v158 offset:27648\n"
"v_mfma_f32_16x16x32_bf16 v[0:3], v[56:59], v[40:43], v[0:3]\n"
"ds_read_b128 v[112:115], v154 offset:18432\n"
"v_mfma_f32_16x16x32_bf16 v[0:3], v[60:63], v[44:47], v[0:3]\n"
"ds_read_b128 v[116:119], v154 offset:27648\n"
"v_mfma_f32_16x16x32_bf16 v[12:15], v[64:67], v[48:51], v[12:15]\n"
"ds_read_b128 v[120:123], v154 offset:19456\n"
"v_mfma_f32_16x16x32_bf16 v[12:15], v[68:71], v[52:55], v[12:15]\n"
"ds_read_b128 v[124:127], v154 offset:28672\n"
"v_mfma_f32_16x16x32_bf16 v[8:11], v[64:67], v[40:43], v[8:11]\n"
"v_mfma_f32_16x16x32_bf16 v[8:11], v[68:71], v[44:47], v[8:11]\n"
"s_waitcnt vmcnt(10)\n"
"s_waitcnt lgkmcnt(0)\n"
"s_barrier\n"
"v_mfma_f32_16x16x32_bf16 v[20:23], v[128:131], v[104:107], v[20:23]\n"
"s_add_u32 s60, s47, 14336\n"
"s_add_u32 m0, s44, 16384\n"
"s_nop 0\n"
"buffer_load_dwordx4 v216, s[32:35], s60 offen offset:0 sc1 lds\n"
"v_mfma_f32_16x16x32_bf16 v[20:23], v[132:135], v[108:111], v[20:23]\n"
"ds_read_b128 v[40:43], v152 offset:32768\n"
"v_mfma_f32_16x16x32_bf16 v[16:19], v[128:131], v[96:99], v[16:19]\n"
"buffer_load_dwordx4 v216, s[32:35], s60 offen offset:1024 sc1 lds\n"
"v_mfma_f32_16x16x32_bf16 v[16:19], v[132:135], v[100:103], v[16:19]\n"
"ds_read_b128 v[44:47], v152 offset:33792\n"
"v_mfma_f32_16x16x32_bf16 v[28:31], v[136:139], v[104:107], v[28:31]\n"
"buffer_load_dwordx4 v217, s[32:35], s60 offen offset:2048 sc1 lds\n"
"v_mfma_f32_16x16x32_bf16 v[28:31], v[140:143], v[108:111], v[28:31]\n"
"ds_read_b128 v[48:51], v153 offset:32768\n"
"v_mfma_f32_16x16x32_bf16 v[24:27], v[136:139], v[96:99], v[24:27]\n"
"buffer_load_dwordx4 v217, s[32:35], s60 offen offset:3072 sc1 lds\n"
"v_mfma_f32_16x16x32_bf16 v[24:27], v[140:143], v[100:103], v[24:27]\n"
"ds_read_b128 v[52:55], v153 offset:33792\n"
"v_mfma_f32_16x16x32_bf16 v[36:39], v[144:147], v[104:107], v[36:39]\n"
"ds_read_b128 v[72:75], v157 offset:0\n"
"v_mfma_f32_16x16x32_bf16 v[36:39], v[148:151], v[108:111], v[36:39]\n"
"ds_read_b128 v[76:79], v157 offset:9216\n"
"v_mfma_f32_16x16x32_bf16 v[32:35], v[144:147], v[96:99], v[32:35]\n"
"ds_read_b128 v[80:83], v157 offset:1024\n"
"v_mfma_f32_16x16x32_bf16 v[32:35], v[148:151], v[100:103], v[32:35]\n"
"ds_read_b128 v[84:87], v157 offset:10240\n"
"v_mfma_f32_16x16x32_bf16 v[4:7], v[112:115], v[104:107], v[4:7]\n"
"ds_read_b128 v[88:91], v159 offset:0\n"
"v_mfma_f32_16x16x32_bf16 v[4:7], v[116:119], v[108:111], v[4:7]\n"
"ds_read_b128 v[92:95], v159 offset:9216\n"
"v_mfma_f32_16x16x32_bf16 v[0:3], v[112:115], v[96:99], v[0:3]\n"
"ds_read_b128 v[56:59], v155 offset:0\n"
"v_mfma_f32_16x16x32_bf16 v[0:3], v[116:119], v[100:103], v[0:3]\n"
"ds_read_b128 v[60:63], v155 offset:9216\n"
"v_mfma_f32_16x16x32_bf16 v[12:15], v[120:123], v[104:107], v[12:15]\n"
"ds_read_b128 v[64:67], v155 offset:1024\n"
"v_mfma_f32_16x16x32_bf16 v[12:15], v[124:127], v[108:111], v[12:15]\n"
"ds_read_b128 v[68:71], v155 offset:10240\n"
"v_mfma_f32_16x16x32_bf16 v[8:11], v[120:123], v[96:99], v[8:11]\n"
"v_mfma_f32_16x16x32_bf16 v[8:11], v[124:127], v[100:103], v[8:11]\n"
"s_waitcnt vmcnt(10)\n"
"s_waitcnt lgkmcnt(0)\n"
"s_barrier\n"
"v_mfma_f32_16x16x32_bf16 v[20:23], v[72:75], v[48:51], v[20:23]\n"
"s_waitcnt vmcnt(4)\n"
"v_readfirstlane_b32 s54, v227\n"
"s_nop 0\n"
"s_cmp_lg_u32 s54, 16\n"
"s_cbranch_scc1 .Lf_gs1a\n"
".Lf_gok1a:\n"
"ds_read_b128 v[96:99], v152 offset:49152\n"
"v_mfma_f32_16x16x32_bf16 v[20:23], v[76:79], v[52:55], v[20:23]\n"
"s_add_u32 s60, s47, 16384\n"
"s_add_u32 m0, s44, 32768\n"
"s_nop 0\n"
"buffer_load_dwordx4 v216, s[32:35], s60 offen offset:0 sc1 lds\n"
"v_mfma_f32_16x16x32_bf16 v[16:19], v[72:75], v[40:43], v[16:19]\n"
"ds_read_b128 v[100:103], v152 offset:50176\n"
"v_mfma_f32_16x16x32_bf16 v[16:19], v[76:79], v[44:47], v[16:19]\n"
"buffer_load_dwordx4 v216, s[32:35], s60 offen offset:1024 sc1 lds\n"
"v_mfma_f32_16x16x32_bf16 v[28:31], v[80:83], v[48:51], v[28:31]\n"
"ds_read_b128 v[104:107], v153 offset:49152\n"
"v_mfma_f32_16x16x32_bf16 v[28:31], v[84:87], v[52:55], v[28:31]\n"
"buffer_load_dwordx4 v217, s[32:35], s60 offen offset:2048 sc1 lds\n"
"v_mfma_f32_16x16x32_bf16 v[24:27], v[80:83], v[40:43], v[24:27]\n"
"ds_read_b128 v[108:111], v153 offset:50176\n"
"v_mfma_f32_16x16x32_bf16 v[24:27], v[84:87], v[44:47], v[24:27]\n"
"buffer_load_dwordx4 v217, s[32:35], s60 offen offset:3072 sc1 lds\n"
"v_mfma_f32_16x16x32_bf16 v[36:39], v[88:91], v[48:51], v[36:39]\n"
"ds_read_b128 v[128:131], v157 offset:18432\n"
"v_mfma_f32_16x16x32_bf16 v[36:39], v[92:95], v[52:55], v[36:39]\n"
"ds_read_b128 v[132:135], v157 offset:27648\n"
"v_mfma_f32_16x16x32_bf16 v[32:35], v[88:91], v[40:43], v[32:35]\n"
"ds_read_b128 v[136:139], v157 offset:19456\n"
"v_mfma_f32_16x16x32_bf16 v[32:35], v[92:95], v[44:47], v[32:35]\n"
"ds_read_b128 v[140:143], v157 offset:28672\n"
"v_mfma_f32_16x16x32_bf16 v[4:7], v[56:59], v[48:51], v[4:7]\n"
"ds_read_b128 v[144:147], v159 offset:18432\n"
"v_mfma_f32_16x16x32_bf16 v[4:7], v[60:63], v[52:55], v[4:7]\n"
"ds_read_b128 v[148:151], v159 offset:27648\n"
"v_mfma_f32_16x16x32_bf16 v[0:3], v[56:59], v[40:43], v[0:3]\n"
"ds_read_b128 v[112:115], v155 offset:18432\n"
"v_mfma_f32_16x16x32_bf16 v[0:3], v[60:63], v[44:47], v[0:3]\n"
"ds_read_b128 v[116:119], v155 offset:27648\n"
"v_mfma_f32_16x16x32_bf16 v[12:15], v[64:67], v[48:51], v[12:15]\n"
"ds_read_b128 v[120:123], v155 offset:19456\n"
"v_mfma_f32_16x16x32_bf16 v[12:15], v[68:71], v[52:55], v[12:15]\n"
"ds_read_b128 v[124:127], v155 offset:28672\n"
"v_mfma_f32_16x16x32_bf16 v[8:11], v[64:67], v[40:43], v[8:11]\n"
"v_mfma_f32_16x16x32_bf16 v[8:11], v[68:71], v[44:47], v[8:11]\n"
"s_waitcnt vmcnt(9)\n"
"s_waitcnt lgkmcnt(0)\n"
"s_barrier\n"
"v_mfma_f32_16x16x32_bf16 v[20:23], v[128:131], v[104:107], v[20:23]\n"
"s_add_u32 s60, s47, 18432\n"
"s_add_u32 m0, s44, 49152\n"
"s_nop 0\n"
"buffer_load_dwordx4 v216, s[32:35], s60 offen offset:0 sc1 lds\n"
"v_mfma_f32_16x16x32_bf16 v[20:23], v[132:135], v[108:111], v[20:23]\n"
"ds_read_b128 v[40:43], v152 offset:0\n"
"v_mfma_f32_16x16x32_bf16 v[16:19], v[128:131], v[96:99], v[16:19]\n"
"buffer_load_dwordx4 v216, s[32:35], s60 offen offset:1024 sc1 lds\n"
"v_mfma_f32_16x16x32_bf16 v[16:19], v[132:135], v[100:103], v[16:19]\n"
"ds_read_b128 v[44:47], v152 offset:1024\n"
"v_mfma_f32_16x16x32_bf16 v[28:31], v[136:139], v[104:107], v[28:31]\n"
"buffer_load_dwordx4 v217, s[32:35], s60 offen offset:2048 sc1 lds\n"
"v_mfma_f32_16x16x32_bf16 v[28:31], v[140:143], v[108:111], v[28:31]\n"
"ds_read_b128 v[48:51], v153 offset:0\n"
"v_mfma_f32_16x16x32_bf16 v[24:27], v[136:139], v[96:99], v[24:27]\n"
"buffer_load_dwordx4 v217, s[32:35], s60 offen offset:3072 sc1 lds\n"
"v_mfma_f32_16x16x32_bf16 v[24:27], v[140:143], v[100:103], v[24:27]\n"
"ds_read_b128 v[52:55], v153 offset:1024\n"
"v_mfma_f32_16x16x32_bf16 v[36:39], v[144:147], v[104:107], v[36:39]\n"
"ds_read_b128 v[72:75], v156 offset:0\n"
"v_mfma_f32_16x16x32_bf16 v[36:39], v[148:151], v[108:111], v[36:39]\n"
"ds_read_b128 v[76:79], v156 offset:9216\n"
"v_mfma_f32_16x16x32_bf16 v[32:35], v[144:147], v[96:99], v[32:35]\n"
"ds_read_b128 v[80:83], v156 offset:1024\n"
"v_mfma_f32_16x16x32_bf16 v[32:35], v[148:151], v[100:103], v[32:35]\n"
"ds_read_b128 v[84:87], v156 offset:10240\n"
"v_mfma_f32_16x16x32_bf16 v[4:7], v[112:115], v[104:107], v[4:7]\n"
"ds_read_b128 v[88:91], v158 offset:0\n"
"v_mfma_f32_16x16x32_bf16 v[4:7], v[116:119], v[108:111], v[4:7]\n"
"ds_read_b128 v[92:95], v158 offset:9216\n"
"v_mfma_f32_16x16x32_bf16 v[0:3], v[112:115], v[96:99], v[0:3]\n"
"ds_read_b128 v[56:59], v154 offset:0\n"
"v_mfma_f32_16x16x32_bf16 v[0:3], v[116:119], v[100:103], v[0:3]\n"
"ds_read_b128 v[60:63], v154 offset:9216\n"
"v_mfma_f32_16x16x32_bf16 v[12:15], v[120:123], v[104:107], v[12:15]\n"
"ds_read_b128 v[64:67], v154 offset:1024\n"
"v_mfma_f32_16x16x32_bf16 v[12:15], v[124:127], v[108:111], v[12:15]\n"
"ds_read_b128 v[68:71], v154 offset:10240\n"
"v_mfma_f32_16x16x32_bf16 v[8:11], v[120:123], v[96:99], v[8:11]\n"
"v_mfma_f32_16x16x32_bf16 v[8:11], v[124:127], v[100:103], v[8:11]\n"
"s_waitcnt vmcnt(8)\n"
"s_waitcnt lgkmcnt(0)\n"
"s_barrier\n"
"v_mfma_f32_16x16x32_bf16 v[20:23], v[72:75], v[48:51], v[20:23]\n"
"s_add_u32 s60, s47, 20480\n"
"s_add_u32 m0, s44, 0\n"
"s_nop 0\n"
"buffer_load_dwordx4 v216, s[32:35], s60 offen offset:0 sc1 lds\n"
"v_mfma_f32_16x16x32_bf16 v[20:23], v[76:79], v[52:55], v[20:23]\n"
"ds_read_b128 v[96:99], v152 offset:16384\n"
"v_mfma_f32_16x16x32_bf16 v[16:19], v[72:75], v[40:43], v[16:19]\n"
"buffer_load_dwordx4 v216, s[32:35], s60 offen offset:1024 sc1 lds\n"
"v_mfma_f32_16x16x32_bf16 v[16:19], v[76:79], v[44:47], v[16:19]\n"
"ds_read_b128 v[100:103], v152 offset:17408\n"
"v_mfma_f32_16x16x32_bf16 v[28:31], v[80:83], v[48:51], v[28:31]\n"
"buffer_load_dwordx4 v217, s[32:35], s60 offen offset:2048 sc1 lds\n"
"v_mfma_f32_16x16x32_bf16 v[28:31], v[84:87], v[52:55], v[28:31]\n"
"ds_read_b128 v[104:107], v153 offset:16384\n"
"v_mfma_f32_16x16x32_bf16 v[24:27], v[80:83], v[40:43], v[24:27]\n"
"buffer_load_dwordx4 v217, s[32:35], s60 offen offset:3072 sc1 lds\n"
"v_mfma_f32_16x16x32_bf16 v[24:27], v[84:87], v[44:47], v[24:27]\n"
"ds_read_b128 v[108:111], v153 offset:17408\n"
"v_mfma_f32_16x16x32_bf16 v[36:39], v[88:91], v[48:51], v[36:39]\n"
"ds_read_b128 v[128:131], v156 offset:18432\n"
"v_mfma_f32_16x16x32_bf16 v[36:39], v[92:95], v[52:55], v[36:39]\n"
"ds_read_b128 v[132:135], v156 offset:27648\n"
"v_mfma_f32_16x16x32_bf16 v[32:35], v[88:91], v[40:43], v[32:35]\n"
"ds_read_b128 v[136:139], v156 offset:19456\n"
"v_mfma_f32_16x16x32_bf16 v[32:35], v[92:95], v[44:47], v[32:35]\n"
"ds_read_b128 v[140:143], v156 offset:28672\n"
"v_mfma_f32_16x16x32_bf16 v[4:7], v[56:59], v[48:51], v[4:7]\n"
"ds_read_b128 v[144:147], v158 offset:18432\n"
"v_mfma_f32_16x16x32_bf16 v[4:7], v[60:63], v[52:55], v[4:7]\n"
"ds_read_b128 v[148:151], v158 offset:27648\n"
"v_mfma_f32_16x16x32_bf16 v[0:3], v[56:59], v[40:43], v[0:3]\n"
"ds_read_b128 v[112:115], v154 offset:18432\n"
"v_mfma_f32_16x16x32_bf16 v[0:3], v[60:63], v[44:47], v[0:3]\n"
"ds_read_b128 v[116:119], v154 offset:27648\n"
"v_mfma_f32_16x16x32_bf16 v[12:15], v[64:67], v[48:51], v[12:15]\n"
"ds_read_b128 v[120:123], v154 offset:19456\n"
"v_mfma_f32_16x16x32_bf16 v[12:15], v[68:71], v[52:55], v[12:15]\n"
"ds_read_b128 v[124:127], v154 offset:28672\n"
"v_mfma_f32_16x16x32_bf16 v[8:11], v[64:67], v[40:43], v[8:11]\n"
"v_mfma_f32_16x16x32_bf16 v[8:11], v[68:71], v[44:47], v[8:11]\n"
"s_waitcnt vmcnt(8)\n"
"s_waitcnt lgkmcnt(0)\n"
"s_barrier\n"
"v_mfma_f32_16x16x32_bf16 v[20:23], v[128:131], v[104:107], v[20:23]\n"
"s_add_u32 s60, s47, 22528\n"
"s_add_u32 m0, s44, 16384\n"
"s_nop 0\n"
"buffer_load_dwordx4 v216, s[32:35], s60 offen offset:0 sc1 lds\n"
"v_mfma_f32_16x16x32_bf16 v[20:23], v[132:135], v[108:111], v[20:23]\n"
"ds_read_b128 v[40:43], v152 offset:32768\n"
"v_mfma_f32_16x16x32_bf16 v[16:19], v[128:131], v[96:99], v[16:19]\n"
"buffer_load_dwordx4 v216, s[32:35], s60 offen offset:1024 sc1 lds\n"
"v_mfma_f32_16x16x32_bf16 v[16:19], v[132:135], v[100:103], v[16:19]\n"
"ds_read_b128 v[44:47], v152 offset:33792\n"
"v_mfma_f32_16x16x32_bf16 v[28:31], v[136:139], v[104:107], v[28:31]\n"
"buffer_load_dwordx4 v217, s[32:35], s60 offen offset:2048 sc1 lds\n"
"v_mfma_f32_16x16x32_bf16 v[28:31], v[140:143], v[108:111], v[28:31]\n"
"ds_read_b128 v[48:51], v153 offset:32768\n"
"v_mfma_f32_16x16x32_bf16 v[24:27], v[136:139], v[96:99], v[24:27]\n"
"buffer_load_dwordx4 v217, s[32:35], s60 offen offset:3072 sc1 lds\n"
"v_mfma_f32_16x16x32_bf16 v[24:27], v[140:143], v[100:103], v[24:27]\n"
"ds_read_b128 v[52:55], v153 offset:33792\n"
"v_mfma_f32_16x16x32_bf16 v[36:39], v[144:147], v[104:107], v[36:39]\n"
"ds_read_b128 v[72:75], v157 offset:0\n"
"v_mfma_f32_16x16x32_bf16 v[36:39], v[148:151], v[108:111], v[36:39]\n"
"ds_read_b128 v[76:79], v157 offset:9216\n"
"v_mfma_f32_16x16x32_bf16 v[32:35], v[144:147], v[96:99], v[32:35]\n"
"ds_read_b128 v[80:83], v157 offset:1024\n"
"v_mfma_f32_16x16x32_bf16 v[32:35], v[148:151], v[100:103], v[32:35]\n"
"ds_read_b128 v[84:87], v157 offset:10240\n"
"v_mfma_f32_16x16x32_bf16 v[4:7], v[112:115], v[104:107], v[4:7]\n"
"ds_read_b128 v[88:91], v159 offset:0\n"
"v_mfma_f32_16x16x32_bf16 v[4:7], v[116:119], v[108:111], v[4:7]\n"
"ds_read_b128 v[92:95], v159 offset:9216\n"
"v_mfma_f32_16x16x32_bf16 v[0:3], v[112:115], v[96:99], v[0:3]\n"
"ds_read_b128 v[56:59], v155 offset:0\n"
"v_mfma_f32_16x16x32_bf16 v[0:3], v[116:119], v[100:103], v[0:3]\n"
"ds_read_b128 v[60:63], v155 offset:9216\n"
"v_mfma_f32_16x16x32_bf16 v[12:15], v[120:123], v[104:107], v[12:15]\n"
"ds_read_b128 v[64:67], v155 offset:1024\n"
"v_mfma_f32_16x16x32_bf16 v[12:15], v[124:127], v[108:111], v[12:15]\n"
"ds_read_b128 v[68:71], v155 offset:10240\n"
"v_mfma_f32_16x16x32_bf16 v[8:11], v[120:123], v[96:99], v[8:11]\n"
"v_mfma_f32_16x16x32_bf16 v[8:11], v[124:127], v[100:103], v[8:11]\n"
"s_waitcnt vmcnt(8)\n"
"s_waitcnt lgkmcnt(0)\n"
"s_barrier\n"
"v_mfma_f32_16x16x32_bf16 v[20:23], v[72:75], v[48:51], v[20:23]\n"
"s_add_u32 s60, s47, 24576\n"
"s_add_u32 m0, s44, 32768\n"
"s_nop 0\n"
"buffer_load_dwordx4 v216, s[32:35], s60 offen offset:0 sc1 lds\n"
"v_mfma_f32_16x16x32_bf16 v[20:23], v[76:79], v[52:55], v[20:23]\n"
"ds_read_b128 v[96:99], v152 offset:49152\n"
"v_mfma_f32_16x16x32_bf16 v[16:19], v[72:75], v[40:43], v[16:19]\n"
"buffer_load_dwordx4 v216, s[32:35], s60 offen offset:1024 sc1 lds\n"
"v_mfma_f32_16x16x32_bf16 v[16:19], v[76:79], v[44:47], v[16:19]\n"
"ds_read_b128 v[100:103], v152 offset:50176\n"
"v_mfma_f32_16x16x32_bf16 v[28:31], v[80:83], v[48:51], v[28:31]\n"
"buffer_load_dwordx4 v217, s[32:35], s60 offen offset:2048 sc1 lds\n"
"v_mfma_f32_16x16x32_bf16 v[28:31], v[84:87], v[52:55], v[28:31]\n"
"ds_read_b128 v[104:107], v153 offset:49152\n"
"v_mfma_f32_16x16x32_bf16 v[24:27], v[80:83], v[40:43], v[24:27]\n"
"buffer_load_dwordx4 v217, s[32:35], s60 offen offset:3072 sc1 lds\n"
"v_mfma_f32_16x16x32_bf16 v[24:27], v[84:87], v[44:47], v[24:27]\n"
"ds_read_b128 v[108:111], v153 offset:50176\n"
"v_mfma_f32_16x16x32_bf16 v[36:39], v[88:91], v[48:51], v[36:39]\n"
"ds_read_b128 v[128:131], v157 offset:18432\n"
"v_mfma_f32_16x16x32_bf16 v[36:39], v[92:95], v[52:55], v[36:39]\n"
"ds_read_b128 v[132:135], v157 offset:27648\n"
"v_mfma_f32_16x16x32_bf16 v[32:35], v[88:91], v[40:43], v[32:35]\n"
"ds_read_b128 v[136:139], v157 offset:19456\n"
"v_mfma_f32_16x16x32_bf16 v[32:35], v[92:95], v[44:47], v[32:35]\n"
"ds_read_b128 v[140:143], v157 offset:28672\n"
"v_mfma_f32_16x16x32_bf16 v[4:7], v[56:59], v[48:51], v[4:7]\n"
"ds_read_b128 v[144:147], v159 offset:18432\n"
"v_mfma_f32_16x16x32_bf16 v[4:7], v[60:63], v[52:55], v[4:7]\n"
"ds_read_b128 v[148:151], v159 offset:27648\n"
"v_mfma_f32_16x16x32_bf16 v[0:3], v[56:59], v[40:43], v[0:3]\n"
"ds_read_b128 v[112:115], v155 offset:18432\n"
"v_mfma_f32_16x16x32_bf16 v[0:3], v[60:63], v[44:47], v[0:3]\n"
"ds_read_b128 v[116:119], v155 offset:27648\n"
"v_mfma_f32_16x16x32_bf16 v[12:15], v[64:67], v[48:51], v[12:15]\n"
"ds_read_b128 v[120:123], v155 offset:19456\n"
"v_mfma_f32_16x16x32_bf16 v[12:15], v[68:71], v[52:55], v[12:15]\n"
"ds_read_b128 v[124:127], v155 offset:28672\n"
"v_mfma_f32_16x16x32_bf16 v[8:11], v[64:67], v[40:43], v[8:11]\n"
"v_mfma_f32_16x16x32_bf16 v[8:11], v[68:71], v[44:47], v[8:11]\n"
"s_waitcnt vmcnt(8)\n"
"s_waitcnt lgkmcnt(0)\n"
"s_barrier\n"
"v_mfma_f32_16x16x32_bf16 v[20:23], v[128:131], v[104:107], v[20:23]\n"
"s_add_u32 s60, s47, 26624\n"
"s_add_u32 m0, s44, 49152\n"
"s_nop 0\n"
"buffer_load_dwordx4 v216, s[32:35], s60 offen offset:0 sc1 lds\n"
"ds_read_b128 v[40:43], v152 offset:0\n"
"v_mfma_f32_16x16x32_bf16 v[20:23], v[132:135], v[108:111], v[20:23]\n"
"buffer_load_dwordx4 v216, s[32:35], s60 offen offset:1024 sc1 lds\n"
"v_mfma_f32_16x16x32_bf16 v[16:19], v[128:131], v[96:99], v[16:19]\n"
"ds_read_b128 v[44:47], v152 offset:1024\n"
"v_mfma_f32_16x16x32_bf16 v[16:19], v[132:135], v[100:103], v[16:19]\n"
"buffer_load_dwordx4 v217, s[32:35], s60 offen offset:2048 sc1 lds\n"
"v_mfma_f32_16x16x32_bf16 v[28:31], v[136:139], v[104:107], v[28:31]\n"
"ds_read_b128 v[48:51], v153 offset:0\n"
"v_mfma_f32_16x16x32_bf16 v[28:31], v[140:143], v[108:111], v[28:31]\n"
"buffer_load_dwordx4 v217, s[32:35], s60 offen offset:3072 sc1 lds\n"
"v_mfma_f32_16x16x32_bf16 v[24:27], v[136:139], v[96:99], v[24:27]\n"
"ds_read_b128 v[52:55], v153 offset:1024\n"
"v_mfma_f32_16x16x32_bf16 v[24:27], v[140:143], v[100:103], v[24:27]\n"
"v_mov_b32 v227, 0\n"
"s_mov_b64 exec, 1\n"
"buffer_atomic_add v227, v226, s[64:67], s99 offen offset:128 sc0\n"
"s_mov_b64 exec, -1\n"
"v_mfma_f32_16x16x32_bf16 v[36:39], v[144:147], v[104:107], v[36:39]\n"
"ds_read_b128 v[72:75], v156 offset:0\n"
"v_mfma_f32_16x16x32_bf16 v[36:39], v[148:151], v[108:111], v[36:39]\n"
"ds_read_b128 v[76:79], v156 offset:9216\n"
"v_mfma_f32_16x16x32_bf16 v[32:35], v[144:147], v[96:99], v[32:35]\n"
"ds_read_b128 v[80:83], v156 offset:1024\n"
"v_mfma_f32_16x16x32_bf16 v[32:35], v[148:151], v[100:103], v[32:35]\n"
"ds_read_b128 v[84:87], v156 offset:10240\n"
"v_mfma_f32_16x16x32_bf16 v[4:7], v[112:115], v[104:107], v[4:7]\n"
"ds_read_b128 v[88:91], v158 offset:0\n"
"v_mfma_f32_16x16x32_bf16 v[4:7], v[116:119], v[108:111], v[4:7]\n"
"ds_read_b128 v[92:95], v158 offset:9216\n"
"v_mfma_f32_16x16x32_bf16 v[0:3], v[112:115], v[96:99], v[0:3]\n"
"ds_read_b128 v[56:59], v154 offset:0\n"
"v_mfma_f32_16x16x32_bf16 v[0:3], v[116:119], v[100:103], v[0:3]\n"
"ds_read_b128 v[60:63], v154 offset:9216\n"
"v_mfma_f32_16x16x32_bf16 v[12:15], v[120:123], v[104:107], v[12:15]\n"
"ds_read_b128 v[64:67], v154 offset:1024\n"
"v_mfma_f32_16x16x32_bf16 v[12:15], v[124:127], v[108:111], v[12:15]\n"
"ds_read_b128 v[68:71], v154 offset:10240\n"
"v_mfma_f32_16x16x32_bf16 v[8:11], v[120:123], v[96:99], v[8:11]\n"
"v_mfma_f32_16x16x32_bf16 v[8:11], v[124:127], v[100:103], v[8:11]\n"
"s_waitcnt vmcnt(9)\n"
"s_waitcnt lgkmcnt(0)\n"
"s_barrier\n"
"v_mfma_f32_16x16x32_bf16 v[20:23], v[72:75], v[48:51], v[20:23]\n"
"s_add_u32 s60, s47, 28672\n"
"s_add_u32 m0, s44, 0\n"
"s_nop 0\n"
"buffer_load_dwordx4 v216, s[32:35], s60 offen offset:0 sc1 lds\n"
"v_mfma_f32_16x16x32_bf16 v[20:23], v[76:79], v[52:55], v[20:23]\n"
"ds_read_b128 v[96:99], v152 offset:16384\n"
"v_mfma_f32_16x16x32_bf16 v[16:19], v[72:75], v[40:43], v[16:19]\n"
"buffer_load_dwordx4 v216, s[32:35], s60 offen offset:1024 sc1 lds\n"
"v_mfma_f32_16x16x32_bf16 v[16:19], v[76:79], v[44:47], v[16:19]\n"
"ds_read_b128 v[100:103], v152 offset:17408\n"
"v_mfma_f32_16x16x32_bf16 v[28:31], v[80:83], v[48:51], v[28:31]\n"
"buffer_load_dwordx4 v217, s[32:35], s60 offen offset:2048 sc1 lds\n"
"v_mfma_f32_16x16x32_bf16 v[28:31], v[84:87], v[52:55], v[28:31]\n"
"ds_read_b128 v[104:107], v153 offset:16384\n"
"v_mfma_f32_16x16x32_bf16 v[24:27], v[80:83], v[40:43], v[24:27]\n"
"buffer_load_dwordx4 v217, s[32:35], s60 offen offset:3072 sc1 lds\n"
"v_mfma_f32_16x16x32_bf16 v[24:27], v[84:87], v[44:47], v[24:27]\n"
"ds_read_b128 v[108:111], v153 offset:17408\n"
"v_mfma_f32_16x16x32_bf16 v[36:39], v[88:91], v[48:51], v[36:39]\n"
"ds_read_b128 v[128:131], v156 offset:18432\n"
"v_mfma_f32_16x16x32_bf16 v[36:39], v[92:95], v[52:55], v[36:39]\n"
"ds_read_b128 v[132:135], v156 offset:27648\n"
"v_mfma_f32_16x16x32_bf16 v[32:35], v[88:91], v[40:43], v[32:35]\n"
"ds_read_b128 v[136:139], v156 offset:19456\n"
"v_mfma_f32_16x16x32_bf16 v[32:35], v[92:95], v[44:47], v[32:35]\n"
"ds_read_b128 v[140:143], v156 offset:28672\n"
"v_mfma_f32_16x16x32_bf16 v[4:7], v[56:59], v[48:51], v[4:7]\n"
"ds_read_b128 v[144:147], v158 offset:18432\n"
"v_mfma_f32_16x16x32_bf16 v[4:7], v[60:63], v[52:55], v[4:7]\n"
"ds_read_b128 v[148:151], v158 offset:27648\n"
"v_mfma_f32_16x16x32_bf16 v[0:3], v[56:59], v[40:43], v[0:3]\n"
"ds_read_b128 v[112:115], v154 offset:18432\n"
"v_mfma_f32_16x16x32_bf16 v[0:3], v[60:63], v[44:47], v[0:3]\n"
"ds_read_b128 v[116:119], v154 offset:27648\n"
"v_mfma_f32_16x16x32_bf16 v[12:15], v[64:67], v[48:51], v[12:15]\n"
"ds_read_b128 v[120:123], v154 offset:19456\n"
"v_mfma_f32_16x16x32_bf16 v[12:15], v[68:71], v[52:55], v[12:15]\n"
"ds_read_b128 v[124:127], v154 offset:28672\n"
"v_mfma_f32_16x16x32_bf16 v[8:11], v[64:67], v[40:43], v[8:11]\n"
"v_mfma_f32_16x16x32_bf16 v[8:11], v[68:71], v[44:47], v[8:11]\n"
"s_waitcnt vmcnt(9)\n"
"s_waitcnt lgkmcnt(0)\n"
"s_barrier\n"
"v_mfma_f32_16x16x32_bf16 v[20:23], v[128:131], v[104:107], v[20:23]\n"
"s_add_u32 s60, s47, 30720\n"
"s_add_u32 m0, s44, 16384\n"
"s_nop 0\n"
"buffer_load_dwordx4 v216, s[32:35], s60 offen offset:0 sc1 lds\n"
"v_mfma_f32_16x16x32_bf16 v[20:23], v[132:135], v[108:111], v[20:23]\n"
"ds_read_b128 v[40:43], v152 offset:32768\n"
"v_mfma_f32_16x16x32_bf16 v[16:19], v[128:131], v[96:99], v[16:19]\n"
"buffer_load_dwordx4 v216, s[32:35], s60 offen offset:1024 sc1 lds\n"
"v_mfma_f32_16x16x32_bf16 v[16:19], v[132:135], v[100:103], v[16:19]\n"
"ds_read_b128 v[44:47], v152 offset:33792\n"
"v_mfma_f32_16x16x32_bf16 v[28:31], v[136:139], v[104:107], v[28:31]\n"
"buffer_load_dwordx4 v217, s[32:35], s60 offen offset:2048 sc1 lds\n"
"v_mfma_f32_16x16x32_bf16 v[28:31], v[140:143], v[108:111], v[28:31]\n"
"ds_read_b128 v[48:51], v153 offset:32768\n"
"v_mfma_f32_16x16x32_bf16 v[24:27], v[136:139], v[96:99], v[24:27]\n"
"buffer_load_dwordx4 v217, s[32:35], s60 offen offset:3072 sc1 lds\n"
"v_mfma_f32_16x16x32_bf16 v[24:27], v[140:143], v[100:103], v[24:27]\n"
"ds_read_b128 v[52:55], v153 offset:33792\n"
"v_mfma_f32_16x16x32_bf16 v[36:39], v[144:147], v[104:107], v[36:39]\n"
"ds_read_b128 v[72:75], v157 offset:0\n"
"v_mfma_f32_16x16x32_bf16 v[36:39], v[148:151], v[108:111], v[36:39]\n"
"ds_read_b128 v[76:79], v157 offset:9216\n"
"v_mfma_f32_16x16x32_bf16 v[32:35], v[144:147], v[96:99], v[32:35]\n"
"ds_read_b128 v[80:83], v157 offset:1024\n"
"v_mfma_f32_16x16x32_bf16 v[32:35], v[148:151], v[100:103], v[32:35]\n"
"ds_read_b128 v[84:87], v157 offset:10240\n"
"v_mfma_f32_16x16x32_bf16 v[4:7], v[112:115], v[104:107], v[4:7]\n"
"ds_read_b128 v[88:91], v159 offset:0\n"
"v_mfma_f32_16x16x32_bf16 v[4:7], v[116:119], v[108:111], v[4:7]\n"
"ds_read_b128 v[92:95], v159 offset:9216\n"
"v_mfma_f32_16x16x32_bf16 v[0:3], v[112:115], v[96:99], v[0:3]\n"
"ds_read_b128 v[56:59], v155 offset:0\n"
"v_mfma_f32_16x16x32_bf16 v[0:3], v[116:119], v[100:103], v[0:3]\n"
"ds_read_b128 v[60:63], v155 offset:9216\n"
"v_mfma_f32_16x16x32_bf16 v[12:15], v[120:123], v[104:107], v[12:15]\n"
"ds_read_b128 v[64:67], v155 offset:1024\n"
"v_mfma_f32_16x16x32_bf16 v[12:15], v[124:127], v[108:111], v[12:15]\n"
"ds_read_b128 v[68:71], v155 offset:10240\n"
"v_mfma_f32_16x16x32_bf16 v[8:11], v[120:123], v[96:99], v[8:11]\n"
"v_mfma_f32_16x16x32_bf16 v[8:11], v[124:127], v[100:103], v[8:11]\n"
"s_waitcnt vmcnt(9)\n"
"s_waitcnt lgkmcnt(0)\n"
"s_barrier\n"
"v_mfma_f32_16x16x32_bf16 v[20:23], v[72:75], v[48:51], v[20:23]\n"
"s_waitcnt vmcnt(8)\n"
"v_readfirstlane_b32 s54, v227\n"
"s_nop 0\n"
"s_cmp_lg_u32 s54, 16\n"
"s_cbranch_scc1 .Lf_gs2a\n"
".Lf_gok2a:\n"
"ds_read_b128 v[96:99], v152 offset:49152\n"
"v_mfma_f32_16x16x32_bf16 v[20:23], v[76:79], v[52:55], v[20:23]\n"
"s_add_u32 s60, s47, 32768\n"
"s_add_u32 m0, s44, 32768\n"
"s_nop 0\n"
"buffer_load_dwordx4 v216, s[32:35], s60 offen offset:0 sc1 lds\n"
"v_mfma_f32_16x16x32_bf16 v[16:19], v[72:75], v[40:43], v[16:19]\n"
"ds_read_b128 v[100:103], v152 offset:50176\n"
"v_mfma_f32_16x16x32_bf16 v[16:19], v[76:79], v[44:47], v[16:19]\n"
"buffer_load_dwordx4 v216, s[32:35], s60 offen offset:1024 sc1 lds\n"
"v_mfma_f32_16x16x32_bf16 v[28:31], v[80:83], v[48:51], v[28:31]\n"
"ds_read_b128 v[104:107], v153 offset:49152\n"
"v_mfma_f32_16x16x32_bf16 v[28:31], v[84:87], v[52:55], v[28:31]\n"
"buffer_load_dwordx4 v217, s[32:35], s60 offen offset:2048 sc1 lds\n"
"v_mfma_f32_16x16x32_bf16 v[24:27], v[80:83], v[40:43], v[24:27]\n"
"ds_read_b128 v[108:111], v153 offset:50176\n"
"v_mfma_f32_16x16x32_bf16 v[24:27], v[84:87], v[44:47], v[24:27]\n"
"buffer_load_dwordx4 v217, s[32:35], s60 offen offset:3072 sc1 lds\n"
"v_mfma_f32_16x16x32_bf16 v[36:39], v[88:91], v[48:51], v[36:39]\n"
"ds_read_b128 v[128:131], v157 offset:18432\n"
"v_mfma_f32_16x16x32_bf16 v[36:39], v[92:95], v[52:55], v[36:39]\n"
"ds_read_b128 v[132:135], v157 offset:27648\n"
"v_mfma_f32_16x16x32_bf16 v[32:35], v[88:91], v[40:43], v[32:35]\n"
"ds_read_b128 v[136:139], v157 offset:19456\n"
"v_mfma_f32_16x16x32_bf16 v[32:35], v[92:95], v[44:47], v[32:35]\n"
"ds_read_b128 v[140:143], v157 offset:28672\n"
"v_mfma_f32_16x16x32_bf16 v[4:7], v[56:59], v[48:51], v[4:7]\n"
"ds_read_b128 v[144:147], v159 offset:18432\n"
"v_mfma_f32_16x16x32_bf16 v[4:7], v[60:63], v[52:55], v[4:7]\n"
"ds_read_b128 v[148:151], v159 offset:27648\n"
"v_mfma_f32_16x16x32_bf16 v[0:3], v[56:59], v[40:43], v[0:3]\n"
"ds_read_b128 v[112:115], v155 offset:18432\n"
"v_mfma_f32_16x16x32_bf16 v[0:3], v[60:63], v[44:47], v[0:3]\n"
"ds_read_b128 v[116:119], v155 offset:27648\n"
"v_mfma_f32_16x16x32_bf16 v[12:15], v[64:67], v[48:51], v[12:15]\n"
"ds_read_b128 v[120:123], v155 offset:19456\n"
"v_mfma_f32_16x16x32_bf16 v[12:15], v[68:71], v[52:55], v[12:15]\n"
"ds_read_b128 v[124:127], v155 offset:28672\n"
"v_mfma_f32_16x16x32_bf16 v[8:11], v[64:67], v[40:43], v[8:11]\n"
"v_mfma_f32_16x16x32_bf16 v[8:11], v[68:71], v[44:47], v[8:11]\n"
"s_waitcnt vmcnt(8)\n"
"s_waitcnt lgkmcnt(0)\n"
"s_barrier\n"
"v_mfma_f32_16x16x32_bf16 v[20:23], v[128:131], v[104:107], v[20:23]\n"
"s_add_u32 s60, s47, 34816\n"
"s_add_u32 m0, s44, 49152\n"
"s_nop 0\n"
"buffer_load_dwordx4 v216, s[32:35], s60 offen offset:0 sc1 lds\n"
"v_mfma_f32_16x16x32_bf16 v[20:23], v[132:135], v[108:111], v[20:23]\n"
"ds_read_b128 v[40:43], v152 offset:0\n"
"v_mfma_f32_16x16x32_bf16 v[16:19], v[128:131], v[96:99], v[16:19]\n"
"buffer_load_dwordx4 v216, s[32:35], s60 offen offset:1024 sc1 lds\n"
"v_mfma_f32_16x16x32_bf16 v[16:19], v[132:135], v[100:103], v[16:19]\n"
"ds_read_b128 v[44:47], v152 offset:1024\n"
"v_mfma_f32_16x16x32_bf16 v[28:31], v[136:139], v[104:107], v[28:31]\n"
"buffer_load_dwordx4 v217, s[32:35], s60 offen offset:2048 sc1 lds\n"
"v_mfma_f32_16x16x32_bf16 v[28:31], v[140:143], v[108:111], v[28:31]\n"
"ds_read_b128 v[48:51], v153 offset:0\n"
"v_mfma_f32_16x16x32_bf16 v[24:27], v[136:139], v[96:99], v[24:27]\n"
"buffer_load_dwordx4 v217, s[32:35], s60 offen offset:3072 sc1 lds\n"
"v_mfma_f32_16x16x32_bf16 v[24:27], v[140:143], v[100:103], v[24:27]\n"
"ds_read_b128 v[52:55], v153 offset:1024\n"
"v_mfma_f32_16x16x32_bf16 v[36:39], v[144:147], v[104:107], v[36:39]\n"
"ds_read_b128 v[72:75], v156 offset:0\n"
"v_mfma_f32_16x16x32_bf16 v[36:39], v[148:151], v[108:111], v[36:39]\n"
"ds_read_b128 v[76:79], v156 offset:9216\n"
"v_mfma_f32_16x16x32_bf16 v[32:35], v[144:147], v[96:99], v[32:35]\n"
"ds_read_b128 v[80:83], v156 offset:1024\n"
"v_mfma_f32_16x16x32_bf16 v[32:35], v[148:151], v[100:103], v[32:35]\n"
"ds_read_b128 v[84:87], v156 offset:10240\n"
"v_mfma_f32_16x16x32_bf16 v[4:7], v[112:115], v[104:107], v[4:7]\n"
"ds_read_b128 v[88:91], v158 offset:0\n"
"v_mfma_f32_16x16x32_bf16 v[4:7], v[116:119], v[108:111], v[4:7]\n"
"ds_read_b128 v[92:95], v158 offset:9216\n"
"v_mfma_f32_16x16x32_bf16 v[0:3], v[112:115], v[96:99], v[0:3]\n"
"ds_read_b128 v[56:59], v154 offset:0\n"
"v_mfma_f32_16x16x32_bf16 v[0:3], v[116:119], v[100:103], v[0:3]\n"
"ds_read_b128 v[60:63], v154 offset:9216\n"
"v_mfma_f32_16x16x32_bf16 v[12:15], v[120:123], v[104:107], v[12:15]\n"
"ds_read_b128 v[64:67], v154 offset:1024\n"
"v_mfma_f32_16x16x32_bf16 v[12:15], v[124:127], v[108:111], v[12:15]\n"
"ds_read_b128 v[68:71], v154 offset:10240\n"
"v_mfma_f32_16x16x32_bf16 v[8:11], v[120:123], v[96:99], v[8:11]\n"
"v_mfma_f32_16x16x32_bf16 v[8:11], v[124:127], v[100:103], v[8:11]\n"
"s_waitcnt vmcnt(8)\n"
"s_waitcnt lgkmcnt(0)\n"
"s_barrier\n"
"v_mfma_f32_16x16x32_bf16 v[20:23], v[72:75], v[48:51], v[20:23]\n"
"s_lshl_b32 s54, s72, 4\n"
"s_add_u32 s54, s54, s53\n"
"s_mul_i32 s78, s54, 9216\n"
"s_lshl_b32 s54, s49, 10\n"
"s_add_u32 s79, s78, s54\n"
"ds_read_b128 v[96:99], v152 offset:16384\n"
"v_mfma_f32_16x16x32_bf16 v[20:23], v[76:79], v[52:55], v[20:23]\n"
"s_lshl_b32 s55, s72, 2\n"
"s_add_u32 s55, s55, s53\n"
"s_mul_i32 s58, s55, 36864\n"
"s_lshl_b32 s54, s49, 10\n"
"s_add_u32 s59, s58, s54\n"
"ds_read_b128 v[100:103], v152 offset:17408\n"
"v_mfma_f32_16x16x32_bf16 v[16:19], v[72:75], v[40:43], v[16:19]\n"
"s_lshr_b32 s54, s53, 1\n"
"s_lshl_b32 s52, s54, 3\n"
"ds_read_b128 v[104:107], v153 offset:16384\n"
"v_mfma_f32_16x16x32_bf16 v[16:19], v[76:79], v[44:47], v[16:19]\n"
"v_add_u32 v164, s52, v254\n"
"v_mfma_f32_16x16x32_bf16 v[28:31], v[80:83], v[48:51], v[28:31]\n"
"ds_read_b128 v[108:111], v153 offset:17408\n"
"v_xor_b32 v165, 8, v164\n"
"v_mfma_f32_16x16x32_bf16 v[28:31], v[84:87], v[52:55], v[28:31]\n"
"ds_read_b128 v[128:131], v156 offset:18432\n"
"s_lshl_b32 s54, s72, 11\n"
"s_lshl_b32 s55, s49, 6\n"
"s_add_u32 s98, s54, s55\n"
"s_lshl_b32 s54, s53, 9\n"
"s_add_u32 s99, s98, s54\n"
"v_mfma_f32_16x16x32_bf16 v[24:27], v[80:83], v[40:43], v[24:27]\n"
"ds_read_b128 v[132:135], v156 offset:27648\n"
"v_lshlrev_b32 v166, 9, v219\n"
"v_mfma_f32_16x16x32_bf16 v[24:27], v[84:87], v[44:47], v[24:27]\n"
"ds_read_b128 v[136:139], v156 offset:19456\n"
"v_mfma_f32_16x16x32_bf16 v[36:39], v[88:91], v[48:51], v[36:39]\n"
"v_mov_b32 v167, 1\n"
"ds_read_b128 v[140:143], v156 offset:28672\n"
"v_mfma_f32_16x16x32_bf16 v[36:39], v[92:95], v[52:55], v[36:39]\n"
"v_mov_b32 v168, 0\n"
"ds_read_b128 v[144:147], v158 offset:18432\n"
"v_mfma_f32_16x16x32_bf16 v[32:35], v[88:91], v[40:43], v[32:35]\n"
"v_and_b32 v169, 15, v219\n"
"v_mfma_f32_16x16x32_bf16 v[32:35], v[92:95], v[44:47], v[32:35]\n"
"ds_read_b128 v[148:151], v158 offset:27648\n"
"v_mul_u32_u24 v169, 0x480, v169\n"
"v_mfma_f32_16x16x32_bf16 v[4:7], v[56:59], v[48:51], v[4:7]\n"
"ds_read_b128 v[112:115], v154 offset:18432\n"
"v_and_b32 v170, 48, v219\n"
"v_mfma_f32_16x16x32_bf16 v[4:7], v[60:63], v[52:55], v[4:7]\n"
"ds_read_b128 v[116:119], v154 offset:27648\n"
"v_add_u32 v171, v169, v170\n"
"v_mfma_f32_16x16x32_bf16 v[0:3], v[56:59], v[40:43], v[0:3]\n"
"ds_read_b128 v[120:123], v154 offset:19456\n"
"v_mfma_f32_16x16x32_bf16 v[0:3], v[60:63], v[44:47], v[0:3]\n"
"v_lshrrev_b32 v170, 1, v170\n"
"ds_read_b128 v[124:127], v154 offset:28672\n"
"v_mfma_f32_16x16x32_bf16 v[12:15], v[64:67], v[48:51], v[12:15]\n"
"v_add_u32 v172, v169, v170\n"
"s_lshl_b32 s54, 1, s53\n"
"s_xor_b32 s47, s54, 15\n"
"s_xor_b32 s54, s53, 1\n"
"s_mul_i32 s44, s54, 9216\n"
"s_xor_b32 s54, s53, 2\n"
"s_mul_i32 s45, s54, 9216\n"
"s_xor_b32 s54, s53, 3\n"
"s_mul_i32 s46, s54, 9216\n"
"v_mfma_f32_16x16x32_bf16 v[12:15], v[68:71], v[52:55], v[12:15]\n"
"s_mul_i32 s54, s76, 1152\n"
"s_lshl_b32 s55, s74, 1\n"
"s_add_u32 s74, s54, s55\n"
"s_lshl_b32 s55, s75, 1\n"
"s_add_u32 s75, s54, s55\n"
"s_xor_b32 s54, s76, 16\n"
"s_mul_i32 s54, s54, 1152\n"
"s_add_u32 s76, s54, s55\n"
"v_mfma_f32_16x16x32_bf16 v[8:11], v[64:67], v[40:43], v[8:11]\n"
"v_mfma_f32_16x16x32_bf16 v[8:11], v[68:71], v[44:47], v[8:11]\n"
"s_waitcnt vmcnt(4)\n"
"s_waitcnt lgkmcnt(0)\n"
"s_barrier\n"
"v_mfma_f32_16x16x32_bf16 v[20:23], v[128:131], v[104:107], v[20:23]\n"
"ds_read_b64 v[40:41], v162 offset:32768\n"
"v_mfma_f32_16x16x32_bf16 v[20:23], v[132:135], v[108:111], v[20:23]\n"
"ds_read_b64 v[42:43], v162 offset:33280\n"
"v_mfma_f32_16x16x32_bf16 v[16:19], v[128:131], v[96:99], v[16:19]\n"
"ds_read_b64 v[44:45], v162 offset:33792\n"
"v_mfma_f32_16x16x32_bf16 v[16:19], v[132:135], v[100:103], v[16:19]\n"
"ds_read_b64 v[46:47], v162 offset:34304\n"
"v_mfma_f32_16x16x32_bf16 v[28:31], v[136:139], v[104:107], v[28:31]\n"
"ds_read_b64 v[48:49], v163 offset:32768\n"
"v_mfma_f32_16x16x32_bf16 v[28:31], v[140:143], v[108:111], v[28:31]\n"
"ds_read_b64 v[50:51], v163 offset:33280\n"
"v_mfma_f32_16x16x32_bf16 v[24:27], v[136:139], v[96:99], v[24:27]\n"
"ds_read_b64 v[52:53], v163 offset:33792\n"
"v_mfma_f32_16x16x32_bf16 v[24:27], v[140:143], v[100:103], v[24:27]\n"
"ds_read_b64 v[54:55], v163 offset:34304\n"
"v_mfma_f32_16x16x32_bf16 v[36:39], v[144:147], v[104:107], v[36:39]\n"
"ds_read_b128 v[72:75], v157 offset:0\n"
"v_mfma_f32_16x16x32_bf16 v[36:39], v[148:151], v[108:111], v[36:39]\n"
"ds_read_b128 v[76:79], v157 offset:9216\n"
"v_mfma_f32_16x16x32_bf16 v[32:35], v[144:147], v[96:99], v[32:35]\n"
"ds_read_b128 v[80:83], v157 offset:1024\n"
"v_mfma_f32_16x16x32_bf16 v[32:35], v[148:151], v[100:103], v[32:35]\n"
"ds_read_b128 v[84:87], v157 offset:10240\n"
"v_mfma_f32_16x16x32_bf16 v[4:7], v[112:115], v[104:107], v[4:7]\n"
"ds_read_b128 v[88:91], v159 offset:0\n"
"v_mfma_f32_16x16x32_bf16 v[4:7], v[116:119], v[108:111], v[4:7]\n"
"ds_read_b128 v[92:95], v159 offset:9216\n"
"v_mfma_f32_16x16x32_bf16 v[0:3], v[112:115], v[96:99], v[0:3]\n"
"ds_read_b128 v[56:59], v155 offset:0\n"
"v_mfma_f32_16x16x32_bf16 v[0:3], v[116:119], v[100:103], v[0:3]\n"
"ds_read_b128 v[60:63], v155 offset:9216\n"
"v_mfma_f32_16x16x32_bf16 v[12:15], v[120:123], v[104:107], v[12:15]\n"
"ds_read_b128 v[64:67], v155 offset:1024\n"
"v_mfma_f32_16x16x32_bf16 v[12:15], v[124:127], v[108:111], v[12:15]\n"
"ds_read_b128 v[68:71], v155 offset:10240\n"
"v_mfma_f32_16x16x32_bf16 v[8:11], v[120:123], v[96:99], v[8:11]\n"
"v_mfma_f32_16x16x32_bf16 v[8:11], v[124:127], v[100:103], v[8:11]\n"
"s_waitcnt vmcnt(0)\n"
"s_waitcnt lgkmcnt(0)\n"
"s_barrier\n"
"v_mfma_f32_16x16x32_bf16 v[20:23], v[72:75], v[48:51], v[20:23]\n"
"ds_read_b64 v[96:97], v162 offset:49152\n"
"v_mfma_f32_16x16x32_bf16 v[20:23], v[76:79], v[52:55], v[20:23]\n"
"ds_read_b64 v[98:99], v162 offset:49664\n"
"v_mfma_f32_16x16x32_bf16 v[16:19], v[72:75], v[40:43], v[16:19]\n"
"ds_read_b64 v[100:101], v162 offset:50176\n"
"v_mfma_f32_16x16x32_bf16 v[16:19], v[76:79], v[44:47], v[16:19]\n"
"ds_read_b64 v[102:103], v162 offset:50688\n"
"v_mfma_f32_16x16x32_bf16 v[28:31], v[80:83], v[48:51], v[28:31]\n"
"ds_read_b64 v[104:105], v163 offset:49152\n"
"v_mfma_f32_16x16x32_bf16 v[28:31], v[84:87], v[52:55], v[28:31]\n"
"ds_read_b64 v[106:107], v163 offset:49664\n"
"v_mfma_f32_16x16x32_bf16 v[24:27], v[80:83], v[40:43], v[24:27]\n"
"ds_read_b64 v[108:109], v163 offset:50176\n"
"v_mfma_f32_16x16x32_bf16 v[24:27], v[84:87], v[44:47], v[24:27]\n"
"ds_read_b64 v[110:111], v163 offset:50688\n"
"v_mfma_f32_16x16x32_bf16 v[36:39], v[88:91], v[48:51], v[36:39]\n"
"ds_read_b128 v[128:131], v157 offset:18432\n"
"v_mfma_f32_16x16x32_bf16 v[36:39], v[92:95], v[52:55], v[36:39]\n"
"ds_read_b128 v[132:135], v157 offset:27648\n"
"v_mfma_f32_16x16x32_bf16 v[32:35], v[88:91], v[40:43], v[32:35]\n"
"ds_read_b128 v[136:139], v157 offset:19456\n"
"v_mfma_f32_16x16x32_bf16 v[32:35], v[92:95], v[44:47], v[32:35]\n"
"ds_read_b128 v[140:143], v157 offset:28672\n"
"v_mfma_f32_16x16x32_bf16 v[4:7], v[56:59], v[48:51], v[4:7]\n"
"ds_read_b128 v[144:147], v159 offset:18432\n"
"v_mfma_f32_16x16x32_bf16 v[4:7], v[60:63], v[52:55], v[4:7]\n"
"ds_read_b128 v[148:151], v159 offset:27648\n"
"v_mfma_f32_16x16x32_bf16 v[0:3], v[56:59], v[40:43], v[0:3]\n"
"ds_read_b128 v[112:115], v155 offset:18432\n"
"v_mfma_f32_16x16x32_bf16 v[0:3], v[60:63], v[44:47], v[0:3]\n"
"ds_read_b128 v[116:119], v155 offset:27648\n"
"v_mfma_f32_16x16x32_bf16 v[12:15], v[64:67], v[48:51], v[12:15]\n"
"ds_read_b128 v[120:123], v155 offset:19456\n"
"v_mfma_f32_16x16x32_bf16 v[12:15], v[68:71], v[52:55], v[12:15]\n"
"ds_read_b128 v[124:127], v155 offset:28672\n"
"v_mfma_f32_16x16x32_bf16 v[8:11], v[64:67], v[40:43], v[8:11]\n"
"v_mfma_f32_16x16x32_bf16 v[8:11], v[68:71], v[44:47], v[8:11]\n"
"s_waitcnt lgkmcnt(9)\n"
"v_mfma_f32_16x16x32_bf16 v[20:23], v[128:131], v[104:107], v[20:23]\n"
"s_waitcnt lgkmcnt(8)\n"
"v_mfma_f32_16x16x32_bf16 v[20:23], v[132:135], v[108:111], v[20:23]\n"
"s_waitcnt lgkmcnt(7)\n"
"v_mfma_f32_16x16x32_bf16 v[28:31], v[136:139], v[104:107], v[28:31]\n"
"s_waitcnt lgkmcnt(6)\n"
"v_mfma_f32_16x16x32_bf16 v[28:31], v[140:143], v[108:111], v[28:31]\n"
"v_mfma_f32_16x16x32_bf16 v[16:19], v[128:131], v[96:99], v[16:19]\n"
"v_mfma_f32_16x16x32_bf16 v[16:19], v[132:135], v[100:103], v[16:19]\n"
"v_mfma_f32_16x16x32_bf16 v[24:27], v[136:139], v[96:99], v[24:27]\n"
"v_mfma_f32_16x16x32_bf16 v[24:27], v[140:143], v[100:103], v[24:27]\n"
"s_waitcnt lgkmcnt(5)\n"
"v_mfma_f32_16x16x32_bf16 v[36:39], v[144:147], v[104:107], v[36:39]\n"
"s_waitcnt lgkmcnt(4)\n"
"v_mfma_f32_16x16x32_bf16 v[36:39], v[148:151], v[108:111], v[36:39]\n"
"v_mfma_f32_16x16x32_bf16 v[32:35], v[144:147], v[96:99], v[32:35]\n"
"v_mfma_f32_16x16x32_bf16 v[32:35], v[148:151], v[100:103], v[32:35]\n"
"s_xor_b32 s54, s53, 3\n"
"s_mul_i32 s54, s54, 36864\n"
"s_add_u32 s60, s79, s54\n"
"v_cvt_pk_bf16_f32 v240, v20, v21\n"
"v_cvt_pk_bf16_f32 v241, v22, v23\n"
"v_cvt_pk_bf16_f32 v242, v28, v29\n"
"v_cvt_pk_bf16_f32 v243, v30, v31\n"
"buffer_store_dwordx4 v[240:243], v254, s[28:31], s60 offen offset:0 sc1\n"
"s_waitcnt lgkmcnt(3)\n"
"v_mfma_f32_16x16x32_bf16 v[4:7], v[112:115], v[104:107], v[4:7]\n"
"s_waitcnt lgkmcnt(2)\n"
"v_mfma_f32_16x16x32_bf16 v[4:7], v[116:119], v[108:111], v[4:7]\n"
"s_waitcnt lgkmcnt(1)\n"
"v_mfma_f32_16x16x32_bf16 v[12:15], v[120:123], v[104:107], v[12:15]\n"
"s_waitcnt lgkmcnt(0)\n"
"v_mfma_f32_16x16x32_bf16 v[12:15], v[124:127], v[108:111], v[12:15]\n"
"s_xor_b32 s54, s53, 1\n"
"s_mul_i32 s54, s54, 36864\n"
"s_add_u32 s60, s79, s54\n"
"v_cvt_pk_bf16_f32 v244, v16, v17\n"
"v_cvt_pk_bf16_f32 v245, v18, v19\n"
"v_cvt_pk_bf16_f32 v246, v24, v25\n"
"v_cvt_pk_bf16_f32 v247, v26, v27\n"
"buffer_store_dwordx4 v[244:247], v254, s[28:31], s60 offen offset:0 sc1\n"
"v_mfma_f32_16x16x32_bf16 v[0:3], v[112:115], v[96:99], v[0:3]\n"
"v_mfma_f32_16x16x32_bf16 v[0:3], v[116:119], v[100:103], v[0:3]\n"
"s_cmp_eq_u32 s50, s53\n"
"s_cbranch_scc1 .Lf_nosingle\n"
"s_mul_i32 s54, s50, 36864\n"
"s_add_u32 s60, s78, s54\n"
"s_add_u32 s60, s60, 8192\n"
"v_cvt_pk_bf16_f32 v248, v32, v33\n"
"v_cvt_pk_bf16_f32 v249, v34, v35\n"
"v_cvt_pk_bf16_f32 v250, v36, v37\n"
"v_cvt_pk_bf16_f32 v251, v38, v39\n"
"buffer_store_dwordx2 v[248:249], v164, s[28:31], s60 offen offset:0 sc1\n"
"buffer_store_dwordx2 v[250:251], v165, s[28:31], s60 offen offset:0 sc1\n"
".Lf_nosingle:\n"
"v_mfma_f32_16x16x32_bf16 v[8:11], v[120:123], v[96:99], v[8:11]\n"
"v_mfma_f32_16x16x32_bf16 v[8:11], v[124:127], v[100:103], v[8:11]\n"
"s_xor_b32 s54, s53, 2\n"
"s_mul_i32 s54, s54, 36864\n"
"s_add_u32 s60, s79, s54\n"
"v_cvt_pk_bf16_f32 v240, v4, v5\n"
"v_cvt_pk_bf16_f32 v241, v6, v7\n"
"v_cvt_pk_bf16_f32 v242, v12, v13\n"
"v_cvt_pk_bf16_f32 v243, v14, v15\n"
"buffer_store_dwordx4 v[240:243], v254, s[28:31], s60 offen offset:0 sc1\n"
"s_waitcnt vmcnt(0)\n"
"s_mov_b32 exec_hi, 0\n"
"s_mov_b32 exec_lo, s47\n"
"buffer_atomic_add v167, v166, s[64:67], s98 offen offset:0\n"
"s_mov_b32 exec_lo, 1\n"
"s_movk_i32 s55, 0\n"
".Lf_polla:\n"
"v_mov_b32 v173, 0\n"
"buffer_atomic_add v173, v168, s[64:67], s99 offen offset:0 sc0\n"
"s_waitcnt vmcnt(0)\n"
"v_readfirstlane_b32 s54, v173\n"
"s_add_u32 s55, s55, 1\n"
"s_nop 0\n"
"s_cmp_eq_u32 s54, 3\n"
"s_cbranch_scc1 .Lf_goa\n"
"s_cmp_lt_u32 s55, 0x20000\n"
"s_cbranch_scc1 .Lf_polla\n"
".Lf_goa:\n"
"s_mov_b64 exec, -1\n"
"s_add_u32 s60, s59, s44\n"
"buffer_load_dwordx4 v[180:183], v254, s[28:31], s60 offen offset:0 sc1\n"
"s_add_u32 s60, s59, s45\n"
"buffer_load_dwordx4 v[184:187], v254, s[28:31], s60 offen offset:0 sc1\n"
"s_add_u32 s60, s59, s46\n"
"buffer_load_dwordx4 v[188:191], v254, s[28:31], s60 offen offset:0 sc1\n"
"s_cmp_lg_u32 s50, s53\n"
"s_cbranch_scc1 .Lf_fin0\n"
"s_add_u32 s60, s58, s44\n"
"s_add_u32 s60, s60, 8192\n"
"buffer_load_dwordx2 v[192:193], v164, s[28:31], s60 offen offset:0 sc1\n"
"buffer_load_dwordx2 v[194:195], v165, s[28:31], s60 offen offset:0 sc1\n"
"s_add_u32 s60, s58, s45\n"
"s_add_u32 s60, s60, 8192\n"
"buffer_load_dwordx2 v[196:197], v164, s[28:31], s60 offen offset:0 sc1\n"
"buffer_load_dwordx2 v[198:199], v165, s[28:31], s60 offen offset:0 sc1\n"
"s_add_u32 s60, s58, s46\n"
"s_add_u32 s60, s60, 8192\n"
"buffer_load_dwordx2 v[200:201], v164, s[28:31], s60 offen offset:0 sc1\n"
"buffer_load_dwordx2 v[202:203], v165, s[28:31], s60 offen offset:0 sc1\n"
".Lf_fin0:\n"
"s_waitcnt vmcnt(0)\n"
"v_lshlrev_b32 v174, 16, v180\n"
"v_and_b32 v175, 0xffff0000, v180\n"
"v_pk_add_f32 v[0:1], v[0:1], v[174:175]\n"
"v_lshlrev_b32 v176, 16, v181\n"
"v_and_b32 v177, 0xffff0000, v181\n"
"v_pk_add_f32 v[2:3], v[2:3], v[176:177]\n"
"v_lshlrev_b32 v174, 16, v182\n"
"v_and_b32 v175, 0xffff0000, v182\n"
"v_pk_add_f32 v[8:9], v[8:9], v[174:175]\n"
"v_lshlrev_b32 v176, 16, v183\n"
"v_and_b32 v177, 0xffff0000, v183\n"
"v_pk_add_f32 v[10:11], v[10:11], v[176:177]\n"
"v_lshlrev_b32 v174, 16, v184\n"
"v_and_b32 v175, 0xffff0000, v184\n"
"v_pk_add_f32 v[0:1], v[0:1], v[174:175]\n"
"v_lshlrev_b32 v176, 16, v185\n"
"v_and_b32 v177, 0xffff0000, v185\n"
"v_pk_add_f32 v[2:3], v[2:3], v[176:177]\n"
"v_lshlrev_b32 v174, 16, v186\n"
"v_and_b32 v175, 0xffff0000, v186\n"
"v_pk_add_f32 v[8:9], v[8:9], v[174:175]\n"
"v_lshlrev_b32 v176, 16, v187\n"
"v_and_b32 v177, 0xffff0000, v187\n"
"v_pk_add_f32 v[10:11], v[10:11], v[176:177]\n"
"v_lshlrev_b32 v174, 16, v188\n"
"v_and_b32 v175, 0xffff0000, v188\n"
"v_pk_add_f32 v[0:1], v[0:1], v[174:175]\n"
"v_lshlrev_b32 v176, 16, v189\n"
"v_and_b32 v177, 0xffff0000, v189\n"
"v_pk_add_f32 v[2:3], v[2:3], v[176:177]\n"
"v_lshlrev_b32 v174, 16, v190\n"
"v_and_b32 v175, 0xffff0000, v190\n"
"v_pk_add_f32 v[8:9], v[8:9], v[174:175]\n"
"v_lshlrev_b32 v176, 16, v191\n"
"v_and_b32 v177, 0xffff0000, v191\n"
"v_pk_add_f32 v[10:11], v[10:11], v[176:177]\n"
"v_cvt_pk_bf16_f32 v240, v0, v1\n"
"v_cvt_pk_bf16_f32 v241, v2, v3\n"
"v_cvt_pk_bf16_f32 v242, v8, v9\n"
"v_cvt_pk_bf16_f32 v243, v10, v11\n"
"buffer_store_dwordx4 v[240:243], v171, s[40:43], s74 offen offset:0 \n"
"s_cmp_lg_u32 s50, s53\n"
"s_cbranch_scc1 .Lf_fin1\n"
"v_lshlrev_b32 v174, 16, v192\n"
"v_and_b32 v175, 0xffff0000, v192\n"
"v_pk_add_f32 v[32:33], v[32:33], v[174:175]\n"
"v_lshlrev_b32 v176, 16, v193\n"
"v_and_b32 v177, 0xffff0000, v193\n"
"v_pk_add_f32 v[34:35], v[34:35], v[176:177]\n"
"v_lshlrev_b32 v174, 16, v194\n"
"v_and_b32 v175, 0xffff0000, v194\n"
"v_pk_add_f32 v[36:37], v[36:37], v[174:175]\n"
"v_lshlrev_b32 v176, 16, v195\n"
"v_and_b32 v177, 0xffff0000, v195\n"
"v_pk_add_f32 v[38:39], v[38:39], v[176:177]\n"
"v_lshlrev_b32 v174, 16, v196\n"
"v_and_b32 v175, 0xffff0000, v196\n"
"v_pk_add_f32 v[32:33], v[32:33], v[174:175]\n"
"v_lshlrev_b32 v176, 16, v197\n"
"v_and_b32 v177, 0xffff0000, v197\n"
"v_pk_add_f32 v[34:35], v[34:35], v[176:177]\n"
"v_lshlrev_b32 v174, 16, v198\n"
"v_and_b32 v175, 0xffff0000, v198\n"
"v_pk_add_f32 v[36:37], v[36:37], v[174:175]\n"
"v_lshlrev_b32 v176, 16, v199\n"
"v_and_b32 v177, 0xffff0000, v199\n"
"v_pk_add_f32 v[38:39], v[38:39], v[176:177]\n"
"v_lshlrev_b32 v174, 16, v200\n"
"v_and_b32 v175, 0xffff0000, v200\n"
"v_pk_add_f32 v[32:33], v[32:33], v[174:175]\n"
"v_lshlrev_b32 v176, 16, v201\n"
"v_and_b32 v177, 0xffff0000, v201\n"
"v_pk_add_f32 v[34:35], v[34:35], v[176:177]\n"
"v_lshlrev_b32 v174, 16, v202\n"
"v_and_b32 v175, 0xffff0000, v202\n"
"v_pk_add_f32 v[36:37], v[36:37], v[174:175]\n"
"v_lshlrev_b32 v176, 16, v203\n"
"v_and_b32 v177, 0xffff0000, v203\n"
"v_pk_add_f32 v[38:39], v[38:39], v[176:177]\n"
"v_cvt_pk_bf16_f32 v248, v32, v33\n"
"v_cvt_pk_bf16_f32 v249, v34, v35\n"
"v_cvt_pk_bf16_f32 v250, v36, v37\n"
"v_cvt_pk_bf16_f32 v251, v38, v39\n"
"buffer_store_dwordx2 v[248:249], v172, s[40:43], s75 offen offset:0 \n"
"buffer_store_dwordx2 v[250:251], v172, s[40:43], s76 offen offset:0 \n"
".Lf_fin1:\n"
"s_endpgm\n"
"s_nop 1\n"
".Lf_gs0a:\n"
"s_movk_i32 s55, 0\n"
".Lf_gl0a:\n"
"v_mov_b32 v227, 0\n"
"s_mov_b64 exec, 1\n"
"buffer_atomic_add v227, v226, s[64:67], s99 offen offset:0 sc0\n"
"s_mov_b64 exec, -1\n"
"s_waitcnt vmcnt(0)\n"
"v_readfirstlane_b32 s54, v227\n"
"s_nop 0\n"
"s_cmp_eq_u32 s54, 16\n"
"s_cbranch_scc1 .Lf_gok0a\n"
"s_add_u32 s55, s55, 1\n"
"s_cmp_lt_u32 s55, 0x20000\n"
"s_cbranch_scc1 .Lf_gl0a\n"
"s_branch .Lf_gok0a\n"
"s_nop 1\n"
".Lf_gs1a:\n"
"s_movk_i32 s55, 0\n"
".Lf_gl1a:\n"
"v_mov_b32 v227, 0\n"
"s_mov_b64 exec, 1\n"
"buffer_atomic_add v227, v226, s[64:67], s99 offen offset:64 sc0\n"
"s_mov_b64 exec, -1\n"
"s_waitcnt vmcnt(0)\n"
"v_readfirstlane_b32 s54, v227\n"
"s_nop 0\n"
"s_cmp_eq_u32 s54, 16\n"
"s_cbranch_scc1 .Lf_gok1a\n"
"s_add_u32 s55, s55, 1\n"
"s_cmp_lt_u32 s55, 0x20000\n"
"s_cbranch_scc1 .Lf_gl1a\n"
"s_branch .Lf_gok1a\n"
"s_nop 1\n"
".Lf_gs2a:\n"
"s_movk_i32 s55, 0\n"
".Lf_gl2a:\n"
"v_mov_b32 v227, 0\n"
"s_mov_b64 exec, 1\n"
"buffer_atomic_add v227, v226, s[64:67], s99 offen offset:128 sc0\n"
"s_mov_b64 exec, -1\n"
"s_waitcnt vmcnt(0)\n"
"v_readfirstlane_b32 s54, v227\n"
"s_nop 0\n"
"s_cmp_eq_u32 s54, 16\n"
"s_cbranch_scc1 .Lf_gok2a\n"
"s_add_u32 s55, s55, 1\n"
"s_cmp_lt_u32 s55, 0x20000\n"
"s_cbranch_scc1 .Lf_gl2a\n"
"s_branch .Lf_gok2a\n"
".Lf_roleB:\n"
"s_mul_i32 s55, s52, 288\n"
"s_add_u32 s55, s55, 4096\n"
"s_lshl_b32 s54, s50, 2\n"
"s_add_u32 s54, s54, 0\n"
"s_sub_u32 s60, s54, 9\n"
"s_cmp_ge_u32 s54, 9\n"
"s_cselect_b32 s54, s60, s54\n"
"s_lshl_b32 s54, s54, 6\n"
"v_add_u32 v220, s54, v219\n"
"v_mul_u32_u24 v221, 0xe38f, v220\n"
"v_lshrrev_b32 v221, 20, v221\n"
"v_mul_u32_u24 v222, 0x22e0, v221\n"
"v_lshl_add_u32 v222, v220, 4, v222\n"
"s_sub_u32 s60, s55, 0\n"
"v_add_u32 v214, s60, v222\n"
"s_lshl_b32 s54, s50, 2\n"
"s_add_u32 s54, s54, 1\n"
"s_sub_u32 s60, s54, 9\n"
"s_cmp_ge_u32 s54, 9\n"
"s_cselect_b32 s54, s60, s54\n"
"s_lshl_b32 s54, s54, 6\n"
"v_add_u32 v220, s54, v219\n"
"v_mul_u32_u24 v221, 0xe38f, v220\n"
"v_lshrrev_b32 v221, 20, v221\n"
"v_mul_u32_u24 v222, 0x22e0, v221\n"
"v_lshl_add_u32 v222, v220, 4, v222\n"
"s_sub_u32 s60, s55, 1024\n"
"v_add_u32 v215, s60, v222\n"
"s_lshl_b32 s54, s50, 2\n"
"s_add_u32 s54, s54, 2\n"
"s_sub_u32 s60, s54, 9\n"
"s_cmp_ge_u32 s54, 9\n"
"s_cselect_b32 s54, s60, s54\n"
"s_lshl_b32 s54, s54, 6\n"
"v_add_u32 v220, s54, v219\n"
"v_mul_u32_u24 v221, 0xe38f, v220\n"
"v_lshrrev_b32 v221, 20, v221\n"
"v_mul_u32_u24 v222, 0x22e0, v221\n"
"v_lshl_add_u32 v222, v220, 4, v222\n"
"s_sub_u32 s60, s55, 2048\n"
"v_add_u32 v216, s60, v222\n"
"s_lshl_b32 s54, s50, 2\n"
"s_add_u32 s54, s54, 3\n"
"s_sub_u32 s60, s54, 9\n"
"s_cmp_ge_u32 s54, 9\n"
"s_cselect_b32 s54, s60, s54\n"
"s_lshl_b32 s54, s54, 6\n"
"v_add_u32 v220, s54, v219\n"
"v_mul_u32_u24 v221, 0xe38f, v220\n"
"v_lshrrev_b32 v221, 20, v221\n"
"v_mul_u32_u24 v222, 0x22e0, v221\n"
"v_lshl_add_u32 v222, v220, 4, v222\n"
"s_sub_u32 s60, s55, 3072\n"
"v_add_u32 v217, s60, v222\n"
"s_add_u32 s54, s50, 16\n"
"s_sub_u32 s60, s54, 9\n"
"s_cmp_ge_u32 s54, 9\n"
"s_cselect_b32 s54, s60, s54\n"
"s_lshl_b32 s54, s54, 6\n"
"v_add_u32 v220, s54, v219\n"
"v_mul_u32_u24 v221, 0xe38f, v220\n"
"v_lshrrev_b32 v221, 20, v221\n"
"v_mul_u32_u24 v222, 0x22e0, v221\n"
"v_lshl_add_u32 v222, v220, 4, v222\n"
"s_sub_u32 s60, s55, 0\n"
"v_add_u32 v218, s60, v222\n"
"s_cmp_lt_u32 s50, 3\n"
"s_cselect_b64 s[40:41], s[24:25], s[28:29]\n"
"s_mov_b64 s[42:43], s[26:27]\n"
"s_cmp_lt_u32 s50, 2\n"
"s_cselect_b64 s[44:45], s[24:25], s[28:29]\n"
"s_mov_b64 s[46:47], s[26:27]\n"
"s_lshl_b32 s61, s50, 12\n"
"s_add_u32 s61, s61, 65536\n"
"s_lshl_b32 s62, s50, 10\n"
"s_add_u32 s62, s62, 81920\n"
"s_add_u32 s64, s56, 0xb40000\n"
"s_addc_u32 s65, s57, 0\n"
"s_and_b32 s65, s65, 0xffff\n"
"s_mov_b32 s66, 0x4e000\n"
"s_mov_b32 s67, 0x20000\n"
"s_mov_b32 s60, 0\n"
"s_add_u32 m0, s61, 0\n"
"s_nop 0\n"
"buffer_load_dwordx4 v214, s[40:43], s60 offen offset:0  lds\n"
"buffer_load_dwordx4 v215, s[44:47], s60 offen offset:1024  lds\n"
"buffer_load_dwordx4 v216, s[44:47], s60 offen offset:2048  lds\n"
"buffer_load_dwordx4 v217, s[44:47], s60 offen offset:3072  lds\n"
"s_add_u32 m0, s62, 0\n"
"s_cmp_ge_u32 s50, 2\n"
"s_cbranch_scc1 .Lf_s1\n"
"buffer_load_dwordx4 v218, s[28:31], s60 offen offset:0  lds\n"
".Lf_s1:\n"
"s_mov_b32 s60, 294912\n"
"s_add_u32 m0, s61, 18432\n"
"s_nop 0\n"
"buffer_load_dwordx4 v214, s[40:43], s60 offen offset:0  lds\n"
"buffer_load_dwordx4 v215, s[44:47], s60 offen offset:1024  lds\n"
"buffer_load_dwordx4 v216, s[44:47], s60 offen offset:2048  lds\n"
"buffer_load_dwordx4 v217, s[44:47], s60 offen offset:3072  lds\n"
"s_add_u32 m0, s62, 18432\n"
"s_cmp_ge_u32 s50, 2\n"
"s_cbranch_scc1 .Lf_s2\n"
"buffer_load_dwordx4 v218, s[28:31], s60 offen offset:0  lds\n"
".Lf_s2:\n"
"s_mov_b32 s60, 589824\n"
"s_add_u32 m0, s61, 36864\n"
"s_nop 0\n"
"buffer_load_dwordx4 v214, s[40:43], s60 offen offset:0  lds\n"
"buffer_load_dwordx4 v215, s[44:47], s60 offen offset:1024  lds\n"
"buffer_load_dwordx4 v216, s[44:47], s60 offen offset:2048  lds\n"
"buffer_load_dwordx4 v217, s[44:47], s60 offen offset:3072  lds\n"
"s_add_u32 m0, s62, 36864\n"
"s_cmp_ge_u32 s50, 2\n"
"s_cbranch_scc1 .Lf_s3\n"
"buffer_load_dwordx4 v218, s[28:31], s60 offen offset:0  lds\n"
".Lf_s3:\n"
"s_barrier\n"
"s_add_u32 s36, s56, 0xa80000\n"
"s_addc_u32 s37, s57, 0\n"
"s_and_b32 s37, s37, 0xffff\n"
"s_mov_b32 s38, 0x24000\n"
"s_mov_b32 s39, 0x20000\n"
"s_add_u32 s32, s56, 0x1200000\n"
"s_addc_u32 s33, s57, 0\n"
"s_and_b32 s33, s33, 0xffff\n"
"s_mov_b32 s34, 0x1200000\n"
"s_mov_b32 s35, 0x20000\n"
"s_mul_i32 s55, s50, 18432\n"
"s_mul_i32 s60, s52, 576\n"
"s_add_u32 s55, s55, s60\n"
"s_cmp_lt_u32 s50, 2\n"
"s_cselect_b32 s55, s55, 0x40000000\n"
"v_cmp_gt_u32 vcc, 36, v219\n"
"v_add_u32 v229, s55, v254\n"
"v_mov_b32 v228, 0x40000000\n"
"v_cndmask_b32 v229, v228, v229, vcc\n"
"s_lshl_b32 s54, s50, 10\n"
"s_add_u32 s54, s54, 139264\n"
"v_add_u32 v228, s54, v254\n"
"s_lshr_b32 s54, s52, 3\n"
"s_lshl_b32 s54, s54, 2\n"
"s_add_u32 s54, s54, s50\n"
"s_mul_i32 s54, s54, 147456\n"
"s_lshl_b32 s55, s51, 9\n"
"s_add_u32 s54, s54, s55\n"
"s_and_b32 s55, s52, 7\n"
"s_lshl_b32 s55, s55, 6\n"
"s_add_u32 s63, s54, s55\n"
"v_lshrrev_b32 v156, 2, v219\n"
"v_lshlrev_b32 v156, 12, v156\n"
"v_and_b32 v157, 3, v219\n"
"v_lshl_add_u32 v156, v157, 4, v156\n"
"v_cmp_gt_u32 vcc, 16, v219\n"
"v_add_u32 v157, 0x10000, v156\n"
"v_add_u32 v158, 0x20000, v156\n"
"v_mov_b32 v159, 0x7ffffff0\n"
"v_cndmask_b32 v158, v159, v158, vcc\n"
"v_lshrrev_b32 v220, 4, v219\n"
"v_bfe_u32 v221, v219, 2, 2\n"
"v_and_b32 v222, 3, v219\n"
"v_lshlrev_b32 v222, 3, v222\n"
"v_and_b32 v224, 1, v220\n"
"v_lshl_add_u32 v224, v224, 2, v221\n"
"v_lshrrev_b32 v223, 1, v220\n"
"v_lshl_add_u32 v223, v223, 4, v224\n"
"v_mul_u32_u24 v212, 0x120, v223\n"
"v_add_u32 v212, v212, v222\n"
"v_add_u32 v212, 0x10000, v212\n"
"v_add_u32 v213, 0x9000, v212\n"
"v_lshl_add_u32 v225, v223, 9, v222\n"
"s_lshl_b32 s54, s50, 2\n"
"v_xor_b32 v226, s54, v224\n"
"v_xor_b32 v226, 0, v226\n"
"v_lshl_add_u32 v208, v226, 5, v225\n"
"v_xor_b32 v226, s54, v224\n"
"v_xor_b32 v226, 1, v226\n"
"v_lshl_add_u32 v209, v226, 5, v225\n"
"v_xor_b32 v226, s54, v224\n"
"v_xor_b32 v226, 2, v226\n"
"v_lshl_add_u32 v210, v226, 5, v225\n"
"v_xor_b32 v226, s54, v224\n"
"v_xor_b32 v226, 3, v226\n"
"v_lshl_add_u32 v211, v226, 5, v225\n"
"s_waitcnt vmcnt(8)\n"
"s_barrier\n"
"ds_read_b64_tr_b16 v[176:177], v208 offset:0\n"
"ds_read_b64_tr_b16 v[178:179], v208 offset:4096\n"
"ds_read_b64_tr_b16 v[180:181], v209 offset:0\n"
"ds_read_b64_tr_b16 v[182:183], v209 offset:4096\n"
"ds_read_b64_tr_b16 v[184:185], v210 offset:0\n"
"ds_read_b64_tr_b16 v[186:187], v210 offset:4096\n"
"ds_read_b64_tr_b16 v[188:189], v211 offset:0\n"
"ds_read_b64_tr_b16 v[190:191], v211 offset:4096\n"
"ds_read_b64_tr_b16 v[160:161], v212 offset:128\n"
"ds_read_b64_tr_b16 v[162:163], v212 offset:2432\n"
"ds_read_b64_tr_b16 v[164:165], v212 offset:9344\n"
"ds_read_b64_tr_b16 v[166:167], v212 offset:11648\n"
"ds_read_b64_tr_b16 v[168:169], v212 offset:160\n"
"ds_read_b64_tr_b16 v[170:171], v212 offset:2464\n"
"s_waitcnt lgkmcnt(4)\n"
"v_mfma_f32_16x16x32_bf16 v[0:3], v[160:163], v[176:179], 0\n"
"s_mov_b32 s60, 884736\n"
"s_add_u32 m0, s61, 55296\n"
"s_nop 0\n"
"buffer_load_dwordx4 v214, s[40:43], s60 offen offset:0  lds\n"
"v_mfma_f32_16x16x32_bf16 v[4:7], v[160:163], v[180:183], 0\n"
"ds_read_b64_tr_b16 v[172:173], v212 offset:9376\n"
"v_mfma_f32_16x16x32_bf16 v[8:11], v[160:163], v[184:187], 0\n"
"ds_read_b64_tr_b16 v[174:175], v212 offset:11680\n"
"v_mfma_f32_16x16x32_bf16 v[12:15], v[160:163], v[188:191], 0\n"
"s_waitcnt lgkmcnt(4)\n"
"v_mfma_f32_16x16x32_bf16 v[28:31], v[164:167], v[188:191], 0\n"
"buffer_load_dwordx4 v215, s[44:47], s60 offen offset:1024  lds\n"
"v_mfma_f32_16x16x32_bf16 v[24:27], v[164:167], v[184:187], 0\n"
"ds_read_b64_tr_b16 v[160:161], v212 offset:192\n"
"v_mfma_f32_16x16x32_bf16 v[20:23], v[164:167], v[180:183], 0\n"
"ds_read_b64_tr_b16 v[162:163], v212 offset:2496\n"
"v_mfma_f32_16x16x32_bf16 v[16:19], v[164:167], v[176:179], 0\n"
"s_waitcnt lgkmcnt(4)\n"
"v_mfma_f32_16x16x32_bf16 v[32:35], v[168:171], v[176:179], 0\n"
"buffer_load_dwordx4 v216, s[44:47], s60 offen offset:2048  lds\n"
"v_mfma_f32_16x16x32_bf16 v[36:39], v[168:171], v[180:183], 0\n"
"ds_read_b64_tr_b16 v[164:165], v212 offset:9408\n"
"v_mfma_f32_16x16x32_bf16 v[40:43], v[168:171], v[184:187], 0\n"
"ds_read_b64_tr_b16 v[166:167], v212 offset:11712\n"
"v_mfma_f32_16x16x32_bf16 v[44:47], v[168:171], v[188:191], 0\n"
"s_waitcnt lgkmcnt(4)\n"
"v_mfma_f32_16x16x32_bf16 v[60:63], v[172:175], v[188:191], 0\n"
"buffer_load_dwordx4 v217, s[44:47], s60 offen offset:3072  lds\n"
"v_mfma_f32_16x16x32_bf16 v[56:59], v[172:175], v[184:187], 0\n"
"ds_read_b64_tr_b16 v[168:169], v212 offset:224\n"
"v_mfma_f32_16x16x32_bf16 v[52:55], v[172:175], v[180:183], 0\n"
"ds_read_b64_tr_b16 v[170:171], v212 offset:2528\n"
"v_mfma_f32_16x16x32_bf16 v[48:51], v[172:175], v[176:179], 0\n"
"s_waitcnt vmcnt(8)\n"
"s_barrier\n"
"s_waitcnt lgkmcnt(4)\n"
"v_mfma_f32_16x16x32_bf16 v[64:67], v[160:163], v[176:179], 0\n"
"s_add_u32 m0, s62, 55296\n"
"s_cmp_ge_u32 s50, 2\n"
"s_cbranch_scc1 .Lf_s4\n"
"buffer_load_dwordx4 v218, s[28:31], s60 offen offset:0  lds\n"
".Lf_s4:\n"
"ds_read_b64_tr_b16 v[192:193], v208 offset:16384\n"
"v_mfma_f32_16x16x32_bf16 v[68:71], v[160:163], v[180:183], 0\n"
"ds_read_b64_tr_b16 v[194:195], v208 offset:20480\n"
"v_mfma_f32_16x16x32_bf16 v[72:75], v[160:163], v[184:187], 0\n"
"ds_read_b64_tr_b16 v[172:173], v212 offset:9440\n"
"v_mfma_f32_16x16x32_bf16 v[76:79], v[160:163], v[188:191], 0\n"
"ds_read_b64_tr_b16 v[174:175], v212 offset:11744\n"
"s_waitcnt lgkmcnt(6)\n"
"v_mfma_f32_16x16x32_bf16 v[92:95], v[164:167], v[188:191], 0\n"
"ds_read_b64_tr_b16 v[196:197], v209 offset:16384\n"
"v_mfma_f32_16x16x32_bf16 v[88:91], v[164:167], v[184:187], 0\n"
"ds_read_b64_tr_b16 v[198:199], v209 offset:20480\n"
"v_mfma_f32_16x16x32_bf16 v[84:87], v[164:167], v[180:183], 0\n"
"ds_read_b64_tr_b16 v[160:161], v212 offset:18560\n"
"v_mfma_f32_16x16x32_bf16 v[80:83], v[164:167], v[176:179], 0\n"
"ds_read_b64_tr_b16 v[162:163], v212 offset:20864\n"
"s_waitcnt lgkmcnt(8)\n"
"v_mfma_f32_16x16x32_bf16 v[96:99], v[168:171], v[176:179], 0\n"
"ds_read_b64_tr_b16 v[200:201], v210 offset:16384\n"
"v_mfma_f32_16x16x32_bf16 v[100:103], v[168:171], v[180:183], 0\n"
"ds_read_b64_tr_b16 v[202:203], v210 offset:20480\n"
"v_mfma_f32_16x16x32_bf16 v[104:107], v[168:171], v[184:187], 0\n"
"ds_read_b64_tr_b16 v[164:165], v212 offset:27776\n"
"v_mfma_f32_16x16x32_bf16 v[108:111], v[168:171], v[188:191], 0\n"
"ds_read_b64_tr_b16 v[166:167], v212 offset:30080\n"
"s_waitcnt lgkmcnt(8)\n"
"v_mfma_f32_16x16x32_bf16 v[124:127], v[172:175], v[188:191], 0\n"
"ds_read_b64_tr_b16 v[204:205], v211 offset:16384\n"
"ds_read_b64_tr_b16 v[206:207], v211 offset:20480\n"
"v_mfma_f32_16x16x32_bf16 v[120:123], v[172:175], v[184:187], 0\n"
"ds_read_b64_tr_b16 v[168:169], v212 offset:18592\n"
"v_mfma_f32_16x16x32_bf16 v[116:119], v[172:175], v[180:183], 0\n"
"ds_read_b64_tr_b16 v[170:171], v212 offset:20896\n"
"v_mfma_f32_16x16x32_bf16 v[112:115], v[172:175], v[176:179], 0\n"
"buffer_load_dwordx4 v[128:131], v156, s[20:23], s63 offen offset:0 sc1\n"
"s_waitcnt vmcnt(5)\n"
"s_barrier\n"
"s_waitcnt lgkmcnt(8)\n"
"v_mfma_f32_16x16x32_bf16 v[0:3], v[160:163], v[192:195], v[0:3]\n"
"s_mov_b32 s60, 1179648\n"
"s_add_u32 m0, s61, 0\n"
"s_nop 0\n"
"buffer_load_dwordx4 v214, s[40:43], s60 offen offset:0  lds\n"
"ds_read_b64_tr_b16 v[172:173], v212 offset:27808\n"
"v_mfma_f32_16x16x32_bf16 v[4:7], v[160:163], v[196:199], v[4:7]\n"
"ds_read_b64_tr_b16 v[174:175], v212 offset:30112\n"
"s_waitcnt lgkmcnt(8)\n"
"v_mfma_f32_16x16x32_bf16 v[8:11], v[160:163], v[200:203], v[8:11]\n"
"ds_read_b64_tr_b16 v[176:177], v208 offset:32768\n"
"s_waitcnt lgkmcnt(5)\n"
"v_mfma_f32_16x16x32_bf16 v[12:15], v[160:163], v[204:207], v[12:15]\n"
"ds_read_b64_tr_b16 v[178:179], v208 offset:36864\n"
"v_mfma_f32_16x16x32_bf16 v[28:31], v[164:167], v[204:207], v[28:31]\n"
"buffer_load_dwordx4 v215, s[44:47], s60 offen offset:1024  lds\n"
"ds_read_b64_tr_b16 v[160:161], v212 offset:18624\n"
"v_mfma_f32_16x16x32_bf16 v[24:27], v[164:167], v[200:203], v[24:27]\n"
"ds_read_b64_tr_b16 v[162:163], v212 offset:20928\n"
"v_mfma_f32_16x16x32_bf16 v[20:23], v[164:167], v[196:199], v[20:23]\n"
"ds_read_b64_tr_b16 v[180:181], v209 offset:32768\n"
"v_mfma_f32_16x16x32_bf16 v[16:19], v[164:167], v[192:195], v[16:19]\n"
"ds_read_b64_tr_b16 v[182:183], v209 offset:36864\n"
"s_waitcnt lgkmcnt(8)\n"
"v_mfma_f32_16x16x32_bf16 v[32:35], v[168:171], v[192:195], v[32:35]\n"
"buffer_load_dwordx4 v216, s[44:47], s60 offen offset:2048  lds\n"
"ds_read_b64_tr_b16 v[164:165], v212 offset:27840\n"
"v_mfma_f32_16x16x32_bf16 v[36:39], v[168:171], v[196:199], v[36:39]\n"
"ds_read_b64_tr_b16 v[166:167], v212 offset:30144\n"
"v_mfma_f32_16x16x32_bf16 v[40:43], v[168:171], v[200:203], v[40:43]\n"
"ds_read_b64_tr_b16 v[184:185], v210 offset:32768\n"
"v_mfma_f32_16x16x32_bf16 v[44:47], v[168:171], v[204:207], v[44:47]\n"
"ds_read_b64_tr_b16 v[186:187], v210 offset:36864\n"
"s_waitcnt lgkmcnt(10)\n"
"v_mfma_f32_16x16x32_bf16 v[60:63], v[172:175], v[204:207], v[60:63]\n"
"buffer_load_dwordx4 v217, s[44:47], s60 offen offset:3072  lds\n"
"ds_read_b64_tr_b16 v[168:169], v212 offset:18656\n"
"v_mfma_f32_16x16x32_bf16 v[56:59], v[172:175], v[200:203], v[56:59]\n"
"ds_read_b64_tr_b16 v[170:171], v212 offset:20960\n"
"v_mfma_f32_16x16x32_bf16 v[52:55], v[172:175], v[196:199], v[52:55]\n"
"ds_read_b64_tr_b16 v[188:189], v211 offset:32768\n"
"v_mfma_f32_16x16x32_bf16 v[48:51], v[172:175], v[192:195], v[48:51]\n"
"ds_read_b64_tr_b16 v[190:191], v211 offset:36864\n"
"s_waitcnt lgkmcnt(10)\n"
"v_mfma_f32_16x16x32_bf16 v[64:67], v[160:163], v[192:195], v[64:67]\n"
"s_add_u32 m0, s62, 0\n"
"s_cmp_ge_u32 s50, 2\n"
"s_cbranch_scc1 .Lf_s5\n"
"buffer_load_dwordx4 v218, s[28:31], s60 offen offset:0  lds\n"
".Lf_s5:\n"
"v_mfma_f32_16x16x32_bf16 v[68:71], v[160:163], v[196:199], v[68:71]\n"
"ds_read_b64_tr_b16 v[172:173], v212 offset:27872\n"
"v_mfma_f32_16x16x32_bf16 v[72:75], v[160:163], v[200:203], v[72:75]\n"
"ds_read_b64_tr_b16 v[174:175], v212 offset:30176\n"
"v_mfma_f32_16x16x32_bf16 v[76:79], v[160:163], v[204:207], v[76:79]\n"
"s_waitcnt lgkmcnt(8)\n"
"v_mfma_f32_16x16x32_bf16 v[92:95], v[164:167], v[204:207], v[92:95]\n"
"ds_read_b64_tr_b16 v[160:161], v213 offset:128\n"
"v_mfma_f32_16x16x32_bf16 v[88:91], v[164:167], v[200:203], v[88:91]\n"
"v_mfma_f32_16x16x32_bf16 v[84:87], v[164:167], v[196:199], v[84:87]\n"
"ds_read_b64_tr_b16 v[162:163], v213 offset:2432\n"
"v_mfma_f32_16x16x32_bf16 v[80:83], v[164:167], v[192:195], v[80:83]\n"
"s_waitcnt lgkmcnt(6)\n"
"v_mfma_f32_16x16x32_bf16 v[96:99], v[168:171], v[192:195], v[96:99]\n"
"ds_read_b64_tr_b16 v[164:165], v213 offset:9344\n"
"v_mfma_f32_16x16x32_bf16 v[100:103], v[168:171], v[196:199], v[100:103]\n"
"v_mfma_f32_16x16x32_bf16 v[104:107], v[168:171], v[200:203], v[104:107]\n"
"ds_read_b64_tr_b16 v[166:167], v213 offset:11648\n"
"v_mfma_f32_16x16x32_bf16 v[108:111], v[168:171], v[204:207], v[108:111]\n"
"s_waitcnt lgkmcnt(4)\n"
"v_mfma_f32_16x16x32_bf16 v[124:127], v[172:175], v[204:207], v[124:127]\n"
"ds_read_b64_tr_b16 v[168:169], v213 offset:160\n"
"v_mfma_f32_16x16x32_bf16 v[120:123], v[172:175], v[200:203], v[120:123]\n"
"ds_read_b64_tr_b16 v[170:171], v213 offset:2464\n"
"v_mfma_f32_16x16x32_bf16 v[116:119], v[172:175], v[196:199], v[116:119]\n"
"buffer_load_dwordx4 v[132:135], v157, s[20:23], s63 offen offset:0 sc1\n"
"v_mfma_f32_16x16x32_bf16 v[112:115], v[172:175], v[192:195], v[112:115]\n"
"s_waitcnt vmcnt(6)\n"
"s_barrier\n"
"s_waitcnt lgkmcnt(4)\n"
"v_mfma_f32_16x16x32_bf16 v[0:3], v[160:163], v[176:179], v[0:3]\n"
"s_mov_b32 s60, 1474560\n"
"s_add_u32 m0, s61, 18432\n"
"s_nop 0\n"
"buffer_load_dwordx4 v214, s[40:43], s60 offen offset:0  lds\n"
"ds_read_b64_tr_b16 v[172:173], v213 offset:9376\n"
"v_mfma_f32_16x16x32_bf16 v[4:7], v[160:163], v[180:183], v[4:7]\n"
"ds_read_b64_tr_b16 v[174:175], v213 offset:11680\n"
"v_mfma_f32_16x16x32_bf16 v[8:11], v[160:163], v[184:187], v[8:11]\n"
"ds_read_b64_tr_b16 v[192:193], v208 offset:49152\n"
"v_mfma_f32_16x16x32_bf16 v[12:15], v[160:163], v[188:191], v[12:15]\n"
"ds_read_b64_tr_b16 v[194:195], v208 offset:53248\n"
"s_waitcnt lgkmcnt(6)\n"
"v_mfma_f32_16x16x32_bf16 v[28:31], v[164:167], v[188:191], v[28:31]\n"
"buffer_load_dwordx4 v215, s[44:47], s60 offen offset:1024  lds\n"
"ds_read_b64_tr_b16 v[160:161], v213 offset:192\n"
"v_mfma_f32_16x16x32_bf16 v[24:27], v[164:167], v[184:187], v[24:27]\n"
"ds_read_b64_tr_b16 v[162:163], v213 offset:2496\n"
"v_mfma_f32_16x16x32_bf16 v[20:23], v[164:167], v[180:183], v[20:23]\n"
"ds_read_b64_tr_b16 v[196:197], v209 offset:49152\n"
"v_mfma_f32_16x16x32_bf16 v[16:19], v[164:167], v[176:179], v[16:19]\n"
"ds_read_b64_tr_b16 v[198:199], v209 offset:53248\n"
"s_waitcnt lgkmcnt(8)\n"
"v_mfma_f32_16x16x32_bf16 v[32:35], v[168:171], v[176:179], v[32:35]\n"
"buffer_load_dwordx4 v216, s[44:47], s60 offen offset:2048  lds\n"
"ds_read_b64_tr_b16 v[164:165], v213 offset:9408\n"
"v_mfma_f32_16x16x32_bf16 v[36:39], v[168:171], v[180:183], v[36:39]\n"
"ds_read_b64_tr_b16 v[166:167], v213 offset:11712\n"
"v_mfma_f32_16x16x32_bf16 v[40:43], v[168:171], v[184:187], v[40:43]\n"
"ds_read_b64_tr_b16 v[200:201], v210 offset:49152\n"
"v_mfma_f32_16x16x32_bf16 v[44:47], v[168:171], v[188:191], v[44:47]\n"
"ds_read_b64_tr_b16 v[202:203], v210 offset:53248\n"
"s_waitcnt lgkmcnt(10)\n"
"v_mfma_f32_16x16x32_bf16 v[60:63], v[172:175], v[188:191], v[60:63]\n"
"buffer_load_dwordx4 v217, s[44:47], s60 offen offset:3072  lds\n"
"ds_read_b64_tr_b16 v[168:169], v213 offset:224\n"
"v_mfma_f32_16x16x32_bf16 v[56:59], v[172:175], v[184:187], v[56:59]\n"
"ds_read_b64_tr_b16 v[170:171], v213 offset:2528\n"
"v_mfma_f32_16x16x32_bf16 v[52:55], v[172:175], v[180:183], v[52:55]\n"
"ds_read_b64_tr_b16 v[204:205], v211 offset:49152\n"
"v_mfma_f32_16x16x32_bf16 v[48:51], v[172:175], v[176:179], v[48:51]\n"
"ds_read_b64_tr_b16 v[206:207], v211 offset:53248\n"
"s_waitcnt lgkmcnt(10)\n"
"v_mfma_f32_16x16x32_bf16 v[64:67], v[160:163], v[176:179], v[64:67]\n"
"s_add_u32 m0, s62, 18432\n"
"s_cmp_ge_u32 s50, 2\n"
"s_cbranch_scc1 .Lf_s6\n"
"buffer_load_dwordx4 v218, s[28:31], s60 offen offset:0  lds\n"
".Lf_s6:\n"
"v_mfma_f32_16x16x32_bf16 v[68:71], v[160:163], v[180:183], v[68:71]\n"
"ds_read_b64_tr_b16 v[172:173], v213 offset:9440\n"
"v_mfma_f32_16x16x32_bf16 v[72:75], v[160:163], v[184:187], v[72:75]\n"
"ds_read_b64_tr_b16 v[174:175], v213 offset:11744\n"
"v_mfma_f32_16x16x32_bf16 v[76:79], v[160:163], v[188:191], v[76:79]\n"
"s_waitcnt lgkmcnt(8)\n"
"v_mfma_f32_16x16x32_bf16 v[92:95], v[164:167], v[188:191], v[92:95]\n"
"ds_read_b64_tr_b16 v[160:161], v213 offset:18560\n"
"v_mfma_f32_16x16x32_bf16 v[88:91], v[164:167], v[184:187], v[88:91]\n"
"v_mfma_f32_16x16x32_bf16 v[84:87], v[164:167], v[180:183], v[84:87]\n"
"ds_read_b64_tr_b16 v[162:163], v213 offset:20864\n"
"v_mfma_f32_16x16x32_bf16 v[80:83], v[164:167], v[176:179], v[80:83]\n"
"s_waitcnt lgkmcnt(6)\n"
"v_mfma_f32_16x16x32_bf16 v[96:99], v[168:171], v[176:179], v[96:99]\n"
"ds_read_b64_tr_b16 v[164:165], v213 offset:27776\n"
"v_mfma_f32_16x16x32_bf16 v[100:103], v[168:171], v[180:183], v[100:103]\n"
"v_mfma_f32_16x16x32_bf16 v[104:107], v[168:171], v[184:187], v[104:107]\n"
"ds_read_b64_tr_b16 v[166:167], v213 offset:30080\n"
"v_mfma_f32_16x16x32_bf16 v[108:111], v[168:171], v[188:191], v[108:111]\n"
"s_waitcnt lgkmcnt(4)\n"
"v_mfma_f32_16x16x32_bf16 v[124:127], v[172:175], v[188:191], v[124:127]\n"
"ds_read_b64_tr_b16 v[168:169], v213 offset:18592\n"
"v_mfma_f32_16x16x32_bf16 v[120:123], v[172:175], v[184:187], v[120:123]\n"
"ds_read_b64_tr_b16 v[170:171], v213 offset:20896\n"
"v_mfma_f32_16x16x32_bf16 v[116:119], v[172:175], v[180:183], v[116:119]\n"
"buffer_load_dwordx4 v[136:139], v158, s[20:23], s63 offen offset:0 sc1\n"
"v_mfma_f32_16x16x32_bf16 v[112:115], v[172:175], v[176:179], v[112:115]\n"
"s_waitcnt vmcnt(6)\n"
"s_barrier\n"
"s_waitcnt lgkmcnt(4)\n"
"v_mfma_f32_16x16x32_bf16 v[0:3], v[160:163], v[192:195], v[0:3]\n"
"s_mov_b32 s60, 1769472\n"
"s_add_u32 m0, s61, 36864\n"
"s_nop 0\n"
"buffer_load_dwordx4 v214, s[40:43], s60 offen offset:0  lds\n"
"ds_read_b64_tr_b16 v[172:173], v213 offset:27808\n"
"v_mfma_f32_16x16x32_bf16 v[4:7], v[160:163], v[196:199], v[4:7]\n"
"ds_read_b64_tr_b16 v[174:175], v213 offset:30112\n"
"v_mfma_f32_16x16x32_bf16 v[8:11], v[160:163], v[200:203], v[8:11]\n"
"ds_read_b64_tr_b16 v[176:177], v208 offset:0\n"
"v_mfma_f32_16x16x32_bf16 v[12:15], v[160:163], v[204:207], v[12:15]\n"
"ds_read_b64_tr_b16 v[178:179], v208 offset:4096\n"
"s_waitcnt lgkmcnt(6)\n"
"v_mfma_f32_16x16x32_bf16 v[28:31], v[164:167], v[204:207], v[28:31]\n"
"buffer_load_dwordx4 v215, s[44:47], s60 offen offset:1024  lds\n"
"ds_read_b64_tr_b16 v[160:161], v213 offset:18624\n"
"v_mfma_f32_16x16x32_bf16 v[24:27], v[164:167], v[200:203], v[24:27]\n"
"ds_read_b64_tr_b16 v[162:163], v213 offset:20928\n"
"v_mfma_f32_16x16x32_bf16 v[20:23], v[164:167], v[196:199], v[20:23]\n"
"ds_read_b64_tr_b16 v[180:181], v209 offset:0\n"
"v_mfma_f32_16x16x32_bf16 v[16:19], v[164:167], v[192:195], v[16:19]\n"
"ds_read_b64_tr_b16 v[182:183], v209 offset:4096\n"
"s_waitcnt lgkmcnt(8)\n"
"v_mfma_f32_16x16x32_bf16 v[32:35], v[168:171], v[192:195], v[32:35]\n"
"buffer_load_dwordx4 v216, s[44:47], s60 offen offset:2048  lds\n"
"ds_read_b64_tr_b16 v[164:165], v213 offset:27840\n"
"v_mfma_f32_16x16x32_bf16 v[36:39], v[168:171], v[196:199], v[36:39]\n"
"ds_read_b64_tr_b16 v[166:167], v213 offset:30144\n"
"v_mfma_f32_16x16x32_bf16 v[40:43], v[168:171], v[200:203], v[40:43]\n"
"ds_read_b64_tr_b16 v[184:185], v210 offset:0\n"
"v_mfma_f32_16x16x32_bf16 v[44:47], v[168:171], v[204:207], v[44:47]\n"
"ds_read_b64_tr_b16 v[186:187], v210 offset:4096\n"
"s_waitcnt lgkmcnt(10)\n"
"v_mfma_f32_16x16x32_bf16 v[60:63], v[172:175], v[204:207], v[60:63]\n"
"buffer_load_dwordx4 v217, s[44:47], s60 offen offset:3072  lds\n"
"ds_read_b64_tr_b16 v[168:169], v213 offset:18656\n"
"v_mfma_f32_16x16x32_bf16 v[56:59], v[172:175], v[200:203], v[56:59]\n"
"ds_read_b64_tr_b16 v[170:171], v213 offset:20960\n"
"v_mfma_f32_16x16x32_bf16 v[52:55], v[172:175], v[196:199], v[52:55]\n"
"ds_read_b64_tr_b16 v[188:189], v211 offset:0\n"
"v_mfma_f32_16x16x32_bf16 v[48:51], v[172:175], v[192:195], v[48:51]\n"
"ds_read_b64_tr_b16 v[190:191], v211 offset:4096\n"
"s_waitcnt lgkmcnt(10)\n"
"v_mfma_f32_16x16x32_bf16 v[64:67], v[160:163], v[192:195], v[64:67]\n"
"s_add_u32 m0, s62, 36864\n"
"s_cmp_ge_u32 s50, 2\n"
"s_cbranch_scc1 .Lf_s7\n"
"buffer_load_dwordx4 v218, s[28:31], s60 offen offset:0  lds\n"
".Lf_s7:\n"
"v_mfma_f32_16x16x32_bf16 v[68:71], v[160:163], v[196:199], v[68:71]\n"
"ds_read_b64_tr_b16 v[172:173], v213 offset:27872\n"
"v_mfma_f32_16x16x32_bf16 v[72:75], v[160:163], v[200:203], v[72:75]\n"
"ds_read_b64_tr_b16 v[174:175], v213 offset:30176\n"
"v_mfma_f32_16x16x32_bf16 v[76:79], v[160:163], v[204:207], v[76:79]\n"
"s_waitcnt lgkmcnt(8)\n"
"v_mfma_f32_16x16x32_bf16 v[92:95], v[164:167], v[204:207], v[92:95]\n"
"ds_read_b64_tr_b16 v[160:161], v212 offset:128\n"
"v_mfma_f32_16x16x32_bf16 v[88:91], v[164:167], v[200:203], v[88:91]\n"
"ds_read_b64_tr_b16 v[162:163], v212 offset:2432\n"
"v_mfma_f32_16x16x32_bf16 v[84:87], v[164:167], v[196:199], v[84:87]\n"
"s_mov_b32 s99, 0\n"
"buffer_load_dwordx4 v[232:235], v229, s[36:39], s99 offen offset:0 sc1\n"
"v_mfma_f32_16x16x32_bf16 v[80:83], v[164:167], v[192:195], v[80:83]\n"
"s_waitcnt lgkmcnt(6)\n"
"v_mfma_f32_16x16x32_bf16 v[96:99], v[168:171], v[192:195], v[96:99]\n"
"ds_read_b64_tr_b16 v[164:165], v212 offset:9344\n"
"v_mfma_f32_16x16x32_bf16 v[100:103], v[168:171], v[196:199], v[100:103]\n"
"ds_read_b64_tr_b16 v[166:167], v212 offset:11648\n"
"v_mfma_f32_16x16x32_bf16 v[104:107], v[168:171], v[200:203], v[104:107]\n"
"s_mov_b32 s99, 36864\n"
"buffer_load_dwordx4 v[236:239], v229, s[36:39], s99 offen offset:0 sc1\n"
"v_mfma_f32_16x16x32_bf16 v[108:111], v[168:171], v[204:207], v[108:111]\n"
"s_waitcnt lgkmcnt(4)\n"
"v_mfma_f32_16x16x32_bf16 v[124:127], v[172:175], v[204:207], v[124:127]\n"
"ds_read_b64_tr_b16 v[168:169], v212 offset:160\n"
"v_mfma_f32_16x16x32_bf16 v[120:123], v[172:175], v[200:203], v[120:123]\n"
"ds_read_b64_tr_b16 v[170:171], v212 offset:2464\n"
"v_mfma_f32_16x16x32_bf16 v[116:119], v[172:175], v[196:199], v[116:119]\n"
"s_mov_b32 s99, 73728\n"
"buffer_load_dwordx4 v[240:243], v229, s[36:39], s99 offen offset:0 sc1\n"
"v_mfma_f32_16x16x32_bf16 v[112:115], v[172:175], v[192:195], v[112:115]\n"
"s_mov_b32 s99, 110592\n"
"buffer_load_dwordx4 v[244:247], v229, s[36:39], s99 offen offset:0 sc1\n"
"s_waitcnt vmcnt(9)\n"
"s_barrier\n"
"s_waitcnt lgkmcnt(4)\n"
"v_mfma_f32_16x16x32_bf16 v[0:3], v[160:163], v[176:179], v[0:3]\n"
"s_mov_b32 s60, 2064384\n"
"s_add_u32 m0, s61, 55296\n"
"s_nop 0\n"
"buffer_load_dwordx4 v214, s[40:43], s60 offen offset:0  lds\n"
"ds_read_b64_tr_b16 v[172:173], v212 offset:9376\n"
"v_mfma_f32_16x16x32_bf16 v[4:7], v[160:163], v[180:183], v[4:7]\n"
"ds_read_b64_tr_b16 v[174:175], v212 offset:11680\n"
"v_mfma_f32_16x16x32_bf16 v[8:11], v[160:163], v[184:187], v[8:11]\n"
"ds_read_b64_tr_b16 v[192:193], v208 offset:16384\n"
"v_mfma_f32_16x16x32_bf16 v[12:15], v[160:163], v[188:191], v[12:15]\n"
"ds_read_b64_tr_b16 v[194:195], v208 offset:20480\n"
"s_waitcnt lgkmcnt(6)\n"
"v_mfma_f32_16x16x32_bf16 v[28:31], v[164:167], v[188:191], v[28:31]\n"
"buffer_load_dwordx4 v215, s[44:47], s60 offen offset:1024  lds\n"
"ds_read_b64_tr_b16 v[160:161], v212 offset:192\n"
"ds_read_b64_tr_b16 v[162:163], v212 offset:2496\n"
"v_mfma_f32_16x16x32_bf16 v[24:27], v[164:167], v[184:187], v[24:27]\n"
"ds_read_b64_tr_b16 v[196:197], v209 offset:16384\n"
"ds_read_b64_tr_b16 v[198:199], v209 offset:20480\n"
"v_mfma_f32_16x16x32_bf16 v[20:23], v[164:167], v[180:183], v[20:23]\n"
"v_lshlrev_b32 v140, 16, v128\n"
"v_and_b32 v141, 0xffff0000, v128\n"
"v_mfma_f32_16x16x32_bf16 v[16:19], v[164:167], v[176:179], v[16:19]\n"
"v_mul_f32 v148, v140, v140\n"
"v_mul_f32 v149, v141, v141\n"
"s_waitcnt lgkmcnt(8)\n"
"v_mfma_f32_16x16x32_bf16 v[32:35], v[168:171], v[176:179], v[32:35]\n"
"buffer_load_dwordx4 v216, s[44:47], s60 offen offset:2048  lds\n"
"ds_read_b64_tr_b16 v[164:165], v212 offset:9408\n"
"ds_read_b64_tr_b16 v[166:167], v212 offset:11712\n"
"v_mfma_f32_16x16x32_bf16 v[36:39], v[168:171], v[180:183], v[36:39]\n"
"ds_read_b64_tr_b16 v[200:201], v210 offset:16384\n"
"ds_read_b64_tr_b16 v[202:203], v210 offset:20480\n"
"v_mfma_f32_16x16x32_bf16 v[40:43], v[168:171], v[184:187], v[40:43]\n"
"v_lshlrev_b32 v142, 16, v129\n"
"v_and_b32 v143, 0xffff0000, v129\n"
"v_mul_f32 v150, v142, v142\n"
"v_mfma_f32_16x16x32_bf16 v[44:47], v[168:171], v[188:191], v[44:47]\n"
"v_mul_f32 v151, v143, v143\n"
"v_lshlrev_b32 v144, 16, v130\n"
"s_waitcnt lgkmcnt(10)\n"
"v_mfma_f32_16x16x32_bf16 v[60:63], v[172:175], v[188:191], v[60:63]\n"
"buffer_load_dwordx4 v217, s[44:47], s60 offen offset:3072  lds\n"
"ds_read_b64_tr_b16 v[168:169], v212 offset:224\n"
"ds_read_b64_tr_b16 v[170:171], v212 offset:2528\n"
"v_mfma_f32_16x16x32_bf16 v[56:59], v[172:175], v[184:187], v[56:59]\n"
"ds_read_b64_tr_b16 v[204:205], v211 offset:16384\n"
"ds_read_b64_tr_b16 v[206:207], v211 offset:20480\n"
"v_mfma_f32_16x16x32_bf16 v[52:55], v[172:175], v[180:183], v[52:55]\n"
"v_and_b32 v145, 0xffff0000, v130\n"
"v_mul_f32 v152, v144, v144\n"
"v_mul_f32 v153, v145, v145\n"
"v_mfma_f32_16x16x32_bf16 v[48:51], v[172:175], v[176:179], v[48:51]\n"
"v_lshlrev_b32 v146, 16, v131\n"
"v_and_b32 v147, 0xffff0000, v131\n"
"s_waitcnt lgkmcnt(10)\n"
"v_mfma_f32_16x16x32_bf16 v[64:67], v[160:163], v[176:179], v[64:67]\n"
"s_add_u32 m0, s62, 55296\n"
"s_cmp_ge_u32 s50, 2\n"
"s_cbranch_scc1 .Lf_s8\n"
"buffer_load_dwordx4 v218, s[28:31], s60 offen offset:0  lds\n"
".Lf_s8:\n"
"ds_read_b64_tr_b16 v[172:173], v212 offset:9440\n"
"v_mfma_f32_16x16x32_bf16 v[68:71], v[160:163], v[180:183], v[68:71]\n"
"ds_read_b64_tr_b16 v[174:175], v212 offset:11744\n"
"v_mul_f32 v154, v146, v146\n"
"v_mfma_f32_16x16x32_bf16 v[72:75], v[160:163], v[184:187], v[72:75]\n"
"v_mul_f32 v155, v147, v147\n"
"v_lshlrev_b32 v156, 16, v132\n"
"v_mfma_f32_16x16x32_bf16 v[76:79], v[160:163], v[188:191], v[76:79]\n"
"v_and_b32 v157, 0xffff0000, v132\n"
"s_waitcnt lgkmcnt(8)\n"
"v_mfma_f32_16x16x32_bf16 v[92:95], v[164:167], v[188:191], v[92:95]\n"
"ds_read_b64_tr_b16 v[160:161], v212 offset:18560\n"
"ds_read_b64_tr_b16 v[162:163], v212 offset:20864\n"
"v_mfma_f32_16x16x32_bf16 v[88:91], v[164:167], v[184:187], v[88:91]\n"
"v_add_f32 v140, v140, v156\n"
"v_fmac_f32 v148, v156, v156\n"
"v_mfma_f32_16x16x32_bf16 v[84:87], v[164:167], v[180:183], v[84:87]\n"
"v_add_f32 v141, v141, v157\n"
"v_fmac_f32 v149, v157, v157\n"
"v_mfma_f32_16x16x32_bf16 v[80:83], v[164:167], v[176:179], v[80:83]\n"
"v_lshlrev_b32 v158, 16, v133\n"
"s_waitcnt lgkmcnt(6)\n"
"v_mfma_f32_16x16x32_bf16 v[96:99], v[168:171], v[176:179], v[96:99]\n"
"ds_read_b64_tr_b16 v[164:165], v212 offset:27776\n"
"ds_read_b64_tr_b16 v[166:167], v212 offset:30080\n"
"v_mfma_f32_16x16x32_bf16 v[100:103], v[168:171], v[180:183], v[100:103]\n"
"v_and_b32 v159, 0xffff0000, v133\n"
"v_add_f32 v142, v142, v158\n"
"v_mfma_f32_16x16x32_bf16 v[104:107], v[168:171], v[184:187], v[104:107]\n"
"v_fmac_f32 v150, v158, v158\n"
"v_add_f32 v143, v143, v159\n"
"v_mfma_f32_16x16x32_bf16 v[108:111], v[168:171], v[188:191], v[108:111]\n"
"v_fmac_f32 v151, v159, v159\n"
"s_waitcnt lgkmcnt(4)\n"
"v_mfma_f32_16x16x32_bf16 v[124:127], v[172:175], v[188:191], v[124:127]\n"
"ds_read_b64_tr_b16 v[168:169], v212 offset:18592\n"
"ds_read_b64_tr_b16 v[170:171], v212 offset:20896\n"
"v_mfma_f32_16x16x32_bf16 v[120:123], v[172:175], v[184:187], v[120:123]\n"
"v_lshlrev_b32 v156, 16, v134\n"
"v_and_b32 v157, 0xffff0000, v134\n"
"v_mfma_f32_16x16x32_bf16 v[116:119], v[172:175], v[180:183], v[116:119]\n"
"v_add_f32 v144, v144, v156\n"
"v_fmac_f32 v152, v156, v156\n"
"v_mfma_f32_16x16x32_bf16 v[112:115], v[172:175], v[176:179], v[112:115]\n"
"v_add_f32 v145, v145, v157\n"
"s_waitcnt vmcnt(8)\n"
"s_barrier\n"
"s_waitcnt lgkmcnt(4)\n"
"v_mfma_f32_16x16x32_bf16 v[0:3], v[160:163], v[192:195], v[0:3]\n"
"s_mov_b32 s60, 2359296\n"
"s_add_u32 m0, s61, 0\n"
"s_nop 0\n"
"buffer_load_dwordx4 v214, s[40:43], s60 offen offset:0  lds\n"
"ds_read_b64_tr_b16 v[172:173], v212 offset:27808\n"
"v_mfma_f32_16x16x32_bf16 v[4:7], v[160:163], v[196:199], v[4:7]\n"
"ds_read_b64_tr_b16 v[174:175], v212 offset:30112\n"
"v_mfma_f32_16x16x32_bf16 v[8:11], v[160:163], v[200:203], v[8:11]\n"
"ds_read_b64_tr_b16 v[176:177], v208 offset:32768\n"
"v_mfma_f32_16x16x32_bf16 v[12:15], v[160:163], v[204:207], v[12:15]\n"
"ds_read_b64_tr_b16 v[178:179], v208 offset:36864\n"
"s_waitcnt lgkmcnt(6)\n"
"v_mfma_f32_16x16x32_bf16 v[28:31], v[164:167], v[204:207], v[28:31]\n"
"buffer_load_dwordx4 v215, s[44:47], s60 offen offset:1024  lds\n"
"ds_read_b64_tr_b16 v[160:161], v212 offset:18624\n"
"ds_read_b64_tr_b16 v[162:163], v212 offset:20928\n"
"v_mfma_f32_16x16x32_bf16 v[24:27], v[164:167], v[200:203], v[24:27]\n"
"ds_read_b64_tr_b16 v[180:181], v209 offset:32768\n"
"ds_read_b64_tr_b16 v[182:183], v209 offset:36864\n"
"v_mfma_f32_16x16x32_bf16 v[20:23], v[164:167], v[196:199], v[20:23]\n"
"v_fmac_f32 v153, v157, v157\n"
"v_lshlrev_b32 v158, 16, v135\n"
"v_mfma_f32_16x16x32_bf16 v[16:19], v[164:167], v[192:195], v[16:19]\n"
"v_and_b32 v159, 0xffff0000, v135\n"
"v_add_f32 v146, v146, v158\n"
"s_waitcnt lgkmcnt(8)\n"
"v_mfma_f32_16x16x32_bf16 v[32:35], v[168:171], v[192:195], v[32:35]\n"
"buffer_load_dwordx4 v216, s[44:47], s60 offen offset:2048  lds\n"
"ds_read_b64_tr_b16 v[164:165], v212 offset:27840\n"
"ds_read_b64_tr_b16 v[166:167], v212 offset:30144\n"
"v_mfma_f32_16x16x32_bf16 v[36:39], v[168:171], v[196:199], v[36:39]\n"
"ds_read_b64_tr_b16 v[184:185], v210 offset:32768\n"
"ds_read_b64_tr_b16 v[186:187], v210 offset:36864\n"
"v_mfma_f32_16x16x32_bf16 v[40:43], v[168:171], v[200:203], v[40:43]\n"
"v_fmac_f32 v154, v158, v158\n"
"v_add_f32 v147, v147, v159\n"
"v_fmac_f32 v155, v159, v159\n"
"v_mfma_f32_16x16x32_bf16 v[44:47], v[168:171], v[204:207], v[44:47]\n"
"v_lshlrev_b32 v156, 16, v136\n"
"v_and_b32 v157, 0xffff0000, v136\n"
"s_waitcnt lgkmcnt(10)\n"
"v_mfma_f32_16x16x32_bf16 v[60:63], v[172:175], v[204:207], v[60:63]\n"
"buffer_load_dwordx4 v217, s[44:47], s60 offen offset:3072  lds\n"
"ds_read_b64_tr_b16 v[168:169], v212 offset:18656\n"
"ds_read_b64_tr_b16 v[170:171], v212 offset:20960\n"
"v_mfma_f32_16x16x32_bf16 v[56:59], v[172:175], v[200:203], v[56:59]\n"
"ds_read_b64_tr_b16 v[188:189], v211 offset:32768\n"
"ds_read_b64_tr_b16 v[190:191], v211 offset:36864\n"
"v_mfma_f32_16x16x32_bf16 v[52:55], v[172:175], v[196:199], v[52:55]\n"
"v_add_f32 v140, v140, v156\n"
"v_fmac_f32 v148, v156, v156\n"
"v_add_f32 v141, v141, v157\n"
"v_mfma_f32_16x16x32_bf16 v[48:51], v[172:175], v[192:195], v[48:51]\n"
"v_fmac_f32 v149, v157, v157\n"
"v_lshlrev_b32 v158, 16, v137\n"
"s_waitcnt lgkmcnt(10)\n"
"v_mfma_f32_16x16x32_bf16 v[64:67], v[160:163], v[192:195], v[64:67]\n"
"s_add_u32 m0, s62, 0\n"
"s_cmp_ge_u32 s50, 2\n"
"s_cbranch_scc1 .Lf_s9\n"
"buffer_load_dwordx4 v218, s[28:31], s60 offen offset:0  lds\n"
".Lf_s9:\n"
"ds_read_b64_tr_b16 v[172:173], v212 offset:27872\n"
"v_mfma_f32_16x16x32_bf16 v[68:71], v[160:163], v[196:199], v[68:71]\n"
"ds_read_b64_tr_b16 v[174:175], v212 offset:30176\n"
"v_and_b32 v159, 0xffff0000, v137\n"
"v_mfma_f32_16x16x32_bf16 v[72:75], v[160:163], v[200:203], v[72:75]\n"
"v_add_f32 v142, v142, v158\n"
"v_fmac_f32 v150, v158, v158\n"
"v_mfma_f32_16x16x32_bf16 v[76:79], v[160:163], v[204:207], v[76:79]\n"
"v_add_f32 v143, v143, v159\n"
"s_waitcnt lgkmcnt(8)\n"
"v_mfma_f32_16x16x32_bf16 v[92:95], v[164:167], v[204:207], v[92:95]\n"
"ds_read_b64_tr_b16 v[160:161], v213 offset:128\n"
"ds_read_b64_tr_b16 v[162:163], v213 offset:2432\n"
"v_mfma_f32_16x16x32_bf16 v[88:91], v[164:167], v[200:203], v[88:91]\n"
"v_fmac_f32 v151, v159, v159\n"
"v_lshlrev_b32 v156, 16, v138\n"
"v_mfma_f32_16x16x32_bf16 v[84:87], v[164:167], v[196:199], v[84:87]\n"
"v_and_b32 v157, 0xffff0000, v138\n"
"v_add_f32 v144, v144, v156\n"
"v_mfma_f32_16x16x32_bf16 v[80:83], v[164:167], v[192:195], v[80:83]\n"
"v_fmac_f32 v152, v156, v156\n"
"s_waitcnt lgkmcnt(6)\n"
"v_mfma_f32_16x16x32_bf16 v[96:99], v[168:171], v[192:195], v[96:99]\n"
"ds_read_b64_tr_b16 v[164:165], v213 offset:9344\n"
"ds_read_b64_tr_b16 v[166:167], v213 offset:11648\n"
"v_mfma_f32_16x16x32_bf16 v[100:103], v[168:171], v[196:199], v[100:103]\n"
"v_add_f32 v145, v145, v157\n"
"v_fmac_f32 v153, v157, v157\n"
"v_mfma_f32_16x16x32_bf16 v[104:107], v[168:171], v[200:203], v[104:107]\n"
"v_lshlrev_b32 v158, 16, v139\n"
"v_and_b32 v159, 0xffff0000, v139\n"
"v_mfma_f32_16x16x32_bf16 v[108:111], v[168:171], v[204:207], v[108:111]\n"
"v_add_f32 v146, v146, v158\n"
"s_waitcnt lgkmcnt(4)\n"
"v_mfma_f32_16x16x32_bf16 v[124:127], v[172:175], v[204:207], v[124:127]\n"
"ds_read_b64_tr_b16 v[168:169], v213 offset:160\n"
"ds_read_b64_tr_b16 v[170:171], v213 offset:2464\n"
"v_mfma_f32_16x16x32_bf16 v[120:123], v[172:175], v[200:203], v[120:123]\n"
"v_fmac_f32 v154, v158, v158\n"
"v_add_f32 v147, v147, v159\n"
"v_mfma_f32_16x16x32_bf16 v[116:119], v[172:175], v[196:199], v[116:119]\n"
"v_fmac_f32 v155, v159, v159\n"
"v_permlane32_swap_b32 v140, v144\n"
"v_mfma_f32_16x16x32_bf16 v[112:115], v[172:175], v[192:195], v[112:115]\n"
"v_permlane32_swap_b32 v141, v145\n"
"s_waitcnt vmcnt(4)\n"
"s_barrier\n"
"s_waitcnt lgkmcnt(4)\n"
"v_mfma_f32_16x16x32_bf16 v[0:3], v[160:163], v[176:179], v[0:3]\n"
"s_mov_b32 s60, 2654208\n"
"s_add_u32 m0, s61, 18432\n"
"s_nop 0\n"
"buffer_load_dwordx4 v214, s[40:43], s60 offen offset:0  lds\n"
"ds_read_b64_tr_b16 v[172:173], v213 offset:9376\n"
"v_mfma_f32_16x16x32_bf16 v[4:7], v[160:163], v[180:183], v[4:7]\n"
"ds_read_b64_tr_b16 v[174:175], v213 offset:11680\n"
"v_mfma_f32_16x16x32_bf16 v[8:11], v[160:163], v[184:187], v[8:11]\n"
"ds_read_b64_tr_b16 v[192:193], v208 offset:49152\n"
"v_mfma_f32_16x16x32_bf16 v[12:15], v[160:163], v[188:191], v[12:15]\n"
"ds_read_b64_tr_b16 v[194:195], v208 offset:53248\n"
"s_waitcnt lgkmcnt(6)\n"
"v_mfma_f32_16x16x32_bf16 v[28:31], v[164:167], v[188:191], v[28:31]\n"
"buffer_load_dwordx4 v215, s[44:47], s60 offen offset:1024  lds\n"
"ds_read_b64_tr_b16 v[160:161], v213 offset:192\n"
"ds_read_b64_tr_b16 v[162:163], v213 offset:2496\n"
"v_mfma_f32_16x16x32_bf16 v[24:27], v[164:167], v[184:187], v[24:27]\n"
"ds_read_b64_tr_b16 v[196:197], v209 offset:49152\n"
"ds_read_b64_tr_b16 v[198:199], v209 offset:53248\n"
"v_mfma_f32_16x16x32_bf16 v[20:23], v[164:167], v[180:183], v[20:23]\n"
"v_permlane32_swap_b32 v142, v146\n"
"v_permlane32_swap_b32 v143, v147\n"
"v_mfma_f32_16x16x32_bf16 v[16:19], v[164:167], v[176:179], v[16:19]\n"
"v_permlane32_swap_b32 v148, v152\n"
"v_permlane32_swap_b32 v149, v153\n"
"s_waitcnt lgkmcnt(8)\n"
"v_mfma_f32_16x16x32_bf16 v[32:35], v[168:171], v[176:179], v[32:35]\n"
"buffer_load_dwordx4 v216, s[44:47], s60 offen offset:2048  lds\n"
"ds_read_b64_tr_b16 v[164:165], v213 offset:9408\n"
"ds_read_b64_tr_b16 v[166:167], v213 offset:11712\n"
"v_mfma_f32_16x16x32_bf16 v[36:39], v[168:171], v[180:183], v[36:39]\n"
"ds_read_b64_tr_b16 v[200:201], v210 offset:49152\n"
"ds_read_b64_tr_b16 v[202:203], v210 offset:53248\n"
"v_mfma_f32_16x16x32_bf16 v[40:43], v[168:171], v[184:187], v[40:43]\n"
"v_permlane32_swap_b32 v150, v154\n"
"v_permlane32_swap_b32 v151, v155\n"
"v_add_f32 v140, v140, v144\n"
"v_mfma_f32_16x16x32_bf16 v[44:47], v[168:171], v[188:191], v[44:47]\n"
"v_add_f32 v141, v141, v145\n"
"v_add_f32 v142, v142, v146\n"
"s_waitcnt lgkmcnt(10)\n"
"v_mfma_f32_16x16x32_bf16 v[60:63], v[172:175], v[188:191], v[60:63]\n"
"buffer_load_dwordx4 v217, s[44:47], s60 offen offset:3072  lds\n"
"ds_read_b64_tr_b16 v[168:169], v213 offset:224\n"
"ds_read_b64_tr_b16 v[170:171], v213 offset:2528\n"
"v_mfma_f32_16x16x32_bf16 v[56:59], v[172:175], v[184:187], v[56:59]\n"
"ds_read_b64_tr_b16 v[204:205], v211 offset:49152\n"
"ds_read_b64_tr_b16 v[206:207], v211 offset:53248\n"
"v_mfma_f32_16x16x32_bf16 v[52:55], v[172:175], v[180:183], v[52:55]\n"
"v_add_f32 v143, v143, v147\n"
"v_add_f32 v148, v148, v152\n"
"v_add_f32 v149, v149, v153\n"
"v_mfma_f32_16x16x32_bf16 v[48:51], v[172:175], v[176:179], v[48:51]\n"
"v_add_f32 v150, v150, v154\n"
"v_add_f32 v151, v151, v155\n"
"s_waitcnt lgkmcnt(10)\n"
"v_mfma_f32_16x16x32_bf16 v[64:67], v[160:163], v[176:179], v[64:67]\n"
"s_add_u32 m0, s62, 18432\n"
"s_cmp_ge_u32 s50, 2\n"
"s_cbranch_scc1 .Lf_s10\n"
"buffer_load_dwordx4 v218, s[28:31], s60 offen offset:0  lds\n"
".Lf_s10:\n"
"ds_read_b64_tr_b16 v[172:173], v213 offset:9440\n"
"v_mfma_f32_16x16x32_bf16 v[68:71], v[160:163], v[180:183], v[68:71]\n"
"ds_read_b64_tr_b16 v[174:175], v213 offset:11744\n"
"v_permlane16_swap_b32 v140, v142\n"
"v_mfma_f32_16x16x32_bf16 v[72:75], v[160:163], v[184:187], v[72:75]\n"
"v_permlane16_swap_b32 v141, v143\n"
"v_permlane16_swap_b32 v148, v150\n"
"v_mfma_f32_16x16x32_bf16 v[76:79], v[160:163], v[188:191], v[76:79]\n"
"v_permlane16_swap_b32 v149, v151\n"
"s_waitcnt lgkmcnt(8)\n"
"v_mfma_f32_16x16x32_bf16 v[92:95], v[164:167], v[188:191], v[92:95]\n"
"ds_read_b64_tr_b16 v[160:161], v213 offset:18560\n"
"ds_read_b64_tr_b16 v[162:163], v213 offset:20864\n"
"v_mfma_f32_16x16x32_bf16 v[88:91], v[164:167], v[184:187], v[88:91]\n"
"v_add_f32 v140, v140, v142\n"
"v_add_f32 v141, v141, v143\n"
"v_mfma_f32_16x16x32_bf16 v[84:87], v[164:167], v[180:183], v[84:87]\n"
"v_add_f32 v148, v148, v150\n"
"v_add_f32 v149, v149, v151\n"
"v_mfma_f32_16x16x32_bf16 v[80:83], v[164:167], v[176:179], v[80:83]\n"
"v_add_f32_dpp v140, v140, v140 row_ror:8 row_mask:0xf bank_mask:0xf\n"
"s_waitcnt lgkmcnt(6)\n"
"v_mfma_f32_16x16x32_bf16 v[96:99], v[168:171], v[176:179], v[96:99]\n"
"ds_read_b64_tr_b16 v[164:165], v213 offset:27776\n"
"ds_read_b64_tr_b16 v[166:167], v213 offset:30080\n"
"v_mfma_f32_16x16x32_bf16 v[100:103], v[168:171], v[180:183], v[100:103]\n"
"v_add_f32_dpp v141, v141, v141 row_ror:8 row_mask:0xf bank_mask:0xf\n"
"v_add_f32_dpp v148, v148, v148 row_ror:8 row_mask:0xf bank_mask:0xf\n"
"v_mfma_f32_16x16x32_bf16 v[104:107], v[168:171], v[184:187], v[104:107]\n"
"v_add_f32_dpp v149, v149, v149 row_ror:8 row_mask:0xf bank_mask:0xf\n"
"v_add_f32_dpp v128, v140, v140 row_ror:4 row_mask:0xf bank_mask:0xf\n"
"v_mfma_f32_16x16x32_bf16 v[108:111], v[168:171], v[188:191], v[108:111]\n"
"v_add_f32_dpp v129, v141, v141 row_ror:4 row_mask:0xf bank_mask:0xf\n"
"s_waitcnt lgkmcnt(4)\n"
"v_mfma_f32_16x16x32_bf16 v[124:127], v[172:175], v[188:191], v[124:127]\n"
"ds_read_b64_tr_b16 v[168:169], v213 offset:18592\n"
"ds_read_b64_tr_b16 v[170:171], v213 offset:20896\n"
"v_mfma_f32_16x16x32_bf16 v[120:123], v[172:175], v[184:187], v[120:123]\n"
"v_add_f32_dpp v130, v148, v148 row_ror:4 row_mask:0xf bank_mask:0xf\n"
"v_add_f32_dpp v131, v149, v149 row_ror:4 row_mask:0xf bank_mask:0xf\n"
"v_mfma_f32_16x16x32_bf16 v[116:119], v[172:175], v[180:183], v[116:119]\n"
"s_lshl_b32 s54, s50, 10\n"
"s_add_u32 s54, s54, 149504\n"
"v_add_u32 v156, s54, v254\n"
"ds_write_b128 v156, v[128:131] offset:0\n"
"v_mfma_f32_16x16x32_bf16 v[112:115], v[172:175], v[176:179], v[112:115]\n"
"v_add_u32 v156, 0x24800, v254\n"
"s_waitcnt vmcnt(4)\n"
"s_waitcnt lgkmcnt(0)\n"
"s_barrier\n"
"v_mfma_f32_16x16x32_bf16 v[0:3], v[160:163], v[192:195], v[0:3]\n"
"s_mov_b32 s60, 2949120\n"
"s_add_u32 m0, s61, 36864\n"
"s_nop 0\n"
"buffer_load_dwordx4 v214, s[40:43], s60 offen offset:0  lds\n"
"ds_read_b64_tr_b16 v[172:173], v213 offset:27808\n"
"v_mfma_f32_16x16x32_bf16 v[4:7], v[160:163], v[196:199], v[4:7]\n"
"ds_read_b64_tr_b16 v[174:175], v213 offset:30112\n"
"v_mfma_f32_16x16x32_bf16 v[8:11], v[160:163], v[200:203], v[8:11]\n"
"ds_read_b64_tr_b16 v[176:177], v208 offset:0\n"
"v_mfma_f32_16x16x32_bf16 v[12:15], v[160:163], v[204:207], v[12:15]\n"
"ds_read_b64_tr_b16 v[178:179], v208 offset:4096\n"
"v_mfma_f32_16x16x32_bf16 v[28:31], v[164:167], v[204:207], v[28:31]\n"
"buffer_load_dwordx4 v215, s[44:47], s60 offen offset:1024  lds\n"
"ds_read_b64_tr_b16 v[160:161], v213 offset:18624\n"
"v_mfma_f32_16x16x32_bf16 v[24:27], v[164:167], v[200:203], v[24:27]\n"
"ds_read_b64_tr_b16 v[162:163], v213 offset:20928\n"
"ds_read_b64_tr_b16 v[180:181], v209 offset:0\n"
"v_mfma_f32_16x16x32_bf16 v[20:23], v[164:167], v[196:199], v[20:23]\n"
"ds_read_b64_tr_b16 v[182:183], v209 offset:4096\n"
"ds_read_b128 v[140:143], v156 offset:0\n"
"v_mfma_f32_16x16x32_bf16 v[16:19], v[164:167], v[192:195], v[16:19]\n"
"ds_read_b128 v[144:147], v156 offset:1024\n"
"ds_read_b128 v[148:151], v156 offset:2048\n"
"v_mfma_f32_16x16x32_bf16 v[32:35], v[168:171], v[192:195], v[32:35]\n"
"buffer_load_dwordx4 v216, s[44:47], s60 offen offset:2048  lds\n"
"ds_read_b64_tr_b16 v[164:165], v213 offset:27840\n"
"ds_read_b64_tr_b16 v[166:167], v213 offset:30144\n"
"v_mfma_f32_16x16x32_bf16 v[36:39], v[168:171], v[196:199], v[36:39]\n"
"ds_read_b64_tr_b16 v[184:185], v210 offset:0\n"
"ds_read_b64_tr_b16 v[186:187], v210 offset:4096\n"
"v_mfma_f32_16x16x32_bf16 v[40:43], v[168:171], v[200:203], v[40:43]\n"
"ds_read_b128 v[152:155], v156 offset:3072\n"
"v_lshrrev_b32 v157, 4, v219\n"
"v_mfma_f32_16x16x32_bf16 v[44:47], v[168:171], v[204:207], v[44:47]\n"
"v_lshlrev_b32 v157, 5, v157\n"
"v_and_b32 v158, 3, v219\n"
"s_waitcnt lgkmcnt(14)\n"
"v_mfma_f32_16x16x32_bf16 v[60:63], v[172:175], v[204:207], v[60:63]\n"
"buffer_load_dwordx4 v217, s[44:47], s60 offen offset:3072  lds\n"
"ds_read_b64_tr_b16 v[168:169], v213 offset:18656\n"
"ds_read_b64_tr_b16 v[170:171], v213 offset:20960\n"
"v_mfma_f32_16x16x32_bf16 v[56:59], v[172:175], v[200:203], v[56:59]\n"
"ds_read_b64_tr_b16 v[188:189], v211 offset:0\n"
"ds_read_b64_tr_b16 v[190:191], v211 offset:4096\n"
"v_mfma_f32_16x16x32_bf16 v[52:55], v[172:175], v[196:199], v[52:55]\n"
"v_lshl_add_u32 v157, v158, 7, v157\n"
"s_lshl_b32 s54, s51, 3\n"
"s_and_b32 s55, s52, 7\n"
"s_add_u32 s54, s54, s55\n"
"s_lshl_b32 s54, s54, 2\n"
"s_lshr_b32 s55, s52, 3\n"
"s_add_u32 s54, s54, s55\n"
"s_lshl_b32 s54, s54, 9\n"
"s_cmp_eq_u32 s50, 0\n"
"s_cselect_b32 s55, 131072, 0x40000000\n"
"s_add_u32 s98, s54, s55\n"
"v_mfma_f32_16x16x32_bf16 v[48:51], v[172:175], v[192:195], v[48:51]\n"
"s_waitcnt lgkmcnt(10)\n"
"v_add_f32 v140, v140, v144\n"
"v_add_f32 v141, v141, v145\n"
"v_mfma_f32_16x16x32_bf16 v[64:67], v[160:163], v[192:195], v[64:67]\n"
"s_add_u32 m0, s62, 36864\n"
"s_cmp_ge_u32 s50, 2\n"
"s_cbranch_scc1 .Lf_s11\n"
"buffer_load_dwordx4 v218, s[28:31], s60 offen offset:0  lds\n"
".Lf_s11:\n"
"ds_read_b64_tr_b16 v[172:173], v213 offset:27872\n"
"v_mfma_f32_16x16x32_bf16 v[68:71], v[160:163], v[196:199], v[68:71]\n"
"ds_read_b64_tr_b16 v[174:175], v213 offset:30176\n"
"v_mfma_f32_16x16x32_bf16 v[72:75], v[160:163], v[200:203], v[72:75]\n"
"v_add_f32 v142, v142, v146\n"
"v_add_f32 v143, v143, v147\n"
"v_mfma_f32_16x16x32_bf16 v[76:79], v[160:163], v[204:207], v[76:79]\n"
"s_waitcnt lgkmcnt(6)\n"
"v_add_f32 v148, v148, v152\n"
"v_mfma_f32_16x16x32_bf16 v[92:95], v[164:167], v[204:207], v[92:95]\n"
"ds_read_b64_tr_b16 v[160:161], v212 offset:128\n"
"ds_read_b64_tr_b16 v[162:163], v212 offset:2432\n"
"v_mfma_f32_16x16x32_bf16 v[88:91], v[164:167], v[200:203], v[88:91]\n"
"v_add_f32 v149, v149, v153\n"
"v_mfma_f32_16x16x32_bf16 v[84:87], v[164:167], v[196:199], v[84:87]\n"
"v_add_f32 v150, v150, v154\n"
"v_add_f32 v151, v151, v155\n"
"v_mfma_f32_16x16x32_bf16 v[80:83], v[164:167], v[192:195], v[80:83]\n"
"v_add_f32 v128, v140, v148\n"
"s_waitcnt lgkmcnt(6)\n"
"v_mfma_f32_16x16x32_bf16 v[96:99], v[168:171], v[192:195], v[96:99]\n"
"ds_read_b64_tr_b16 v[164:165], v212 offset:9344\n"
"ds_read_b64_tr_b16 v[166:167], v212 offset:11648\n"
"v_mfma_f32_16x16x32_bf16 v[100:103], v[168:171], v[196:199], v[100:103]\n"
"v_add_f32 v132, v141, v149\n"
"v_mfma_f32_16x16x32_bf16 v[104:107], v[168:171], v[200:203], v[104:107]\n"
"v_add_f32 v130, v142, v150\n"
"v_add_f32 v134, v143, v151\n"
"v_mfma_f32_16x16x32_bf16 v[108:111], v[168:171], v[204:207], v[108:111]\n"
"v_mov_b32 v129, 1\n"
"s_waitcnt lgkmcnt(4)\n"
"v_mfma_f32_16x16x32_bf16 v[124:127], v[172:175], v[204:207], v[124:127]\n"
"ds_read_b64_tr_b16 v[168:169], v212 offset:160\n"
"ds_read_b64_tr_b16 v[170:171], v212 offset:2464\n"
"v_mfma_f32_16x16x32_bf16 v[120:123], v[172:175], v[200:203], v[120:123]\n"
"v_mov_b32 v131, 1\n"
"v_mfma_f32_16x16x32_bf16 v[116:119], v[172:175], v[196:199], v[116:119]\n"
"v_mov_b32 v133, 1\n"
"v_mov_b32 v135, 1\n"
"v_mfma_f32_16x16x32_bf16 v[112:115], v[172:175], v[192:195], v[112:115]\n"
"s_mov_b32 exec_lo, 0xf000f\n"
"s_mov_b32 exec_hi, 0xf000f\n"
"buffer_store_dwordx4 v[128:131], v157, s[64:67], s98 offen offset:0 sc1\n"
"buffer_store_dwordx4 v[132:135], v157, s[64:67], s98 offen offset:16 sc1\n"
"s_mov_b64 exec, -1\n"
"s_waitcnt vmcnt(6)\n"
"s_barrier\n"
"s_waitcnt lgkmcnt(4)\n"
"v_mfma_f32_16x16x32_bf16 v[0:3], v[160:163], v[176:179], v[0:3]\n"
"s_mov_b32 s60, 3244032\n"
"s_add_u32 m0, s61, 55296\n"
"s_nop 0\n"
"buffer_load_dwordx4 v214, s[40:43], s60 offen offset:0  lds\n"
"ds_read_b64_tr_b16 v[172:173], v212 offset:9376\n"
"v_mfma_f32_16x16x32_bf16 v[4:7], v[160:163], v[180:183], v[4:7]\n"
"ds_read_b64_tr_b16 v[174:175], v212 offset:11680\n"
"v_mfma_f32_16x16x32_bf16 v[8:11], v[160:163], v[184:187], v[8:11]\n"
"ds_read_b64_tr_b16 v[192:193], v208 offset:16384\n"
"v_mfma_f32_16x16x32_bf16 v[12:15], v[160:163], v[188:191], v[12:15]\n"
"ds_read_b64_tr_b16 v[194:195], v208 offset:20480\n"
"s_waitcnt lgkmcnt(6)\n"
"v_mfma_f32_16x16x32_bf16 v[28:31], v[164:167], v[188:191], v[28:31]\n"
"buffer_load_dwordx4 v215, s[44:47], s60 offen offset:1024  lds\n"
"ds_read_b64_tr_b16 v[160:161], v212 offset:192\n"
"v_mfma_f32_16x16x32_bf16 v[24:27], v[164:167], v[184:187], v[24:27]\n"
"ds_read_b64_tr_b16 v[162:163], v212 offset:2496\n"
"v_mfma_f32_16x16x32_bf16 v[20:23], v[164:167], v[180:183], v[20:23]\n"
"ds_read_b64_tr_b16 v[196:197], v209 offset:16384\n"
"ds_read_b64_tr_b16 v[198:199], v209 offset:20480\n"
"v_mfma_f32_16x16x32_bf16 v[16:19], v[164:167], v[176:179], v[16:19]\n"
"v_add_f32 v232, v232, v236\n"
"s_waitcnt lgkmcnt(8)\n"
"v_mfma_f32_16x16x32_bf16 v[32:35], v[168:171], v[176:179], v[32:35]\n"
"buffer_load_dwordx4 v216, s[44:47], s60 offen offset:2048  lds\n"
"ds_read_b64_tr_b16 v[164:165], v212 offset:9408\n"
"v_mfma_f32_16x16x32_bf16 v[36:39], v[168:171], v[180:183], v[36:39]\n"
"ds_read_b64_tr_b16 v[166:167], v212 offset:11712\n"
"ds_read_b64_tr_b16 v[200:201], v210 offset:16384\n"
"v_mfma_f32_16x16x32_bf16 v[40:43], v[168:171], v[184:187], v[40:43]\n"
"ds_read_b64_tr_b16 v[202:203], v210 offset:20480\n"
"v_add_f32 v233, v233, v237\n"
"v_mfma_f32_16x16x32_bf16 v[44:47], v[168:171], v[188:191], v[44:47]\n"
"v_add_f32 v234, v234, v238\n"
"s_waitcnt lgkmcnt(10)\n"
"v_mfma_f32_16x16x32_bf16 v[60:63], v[172:175], v[188:191], v[60:63]\n"
"buffer_load_dwordx4 v217, s[44:47], s60 offen offset:3072  lds\n"
"ds_read_b64_tr_b16 v[168:169], v212 offset:224\n"
"v_mfma_f32_16x16x32_bf16 v[56:59], v[172:175], v[184:187], v[56:59]\n"
"ds_read_b64_tr_b16 v[170:171], v212 offset:2528\n"
"ds_read_b64_tr_b16 v[204:205], v211 offset:16384\n"
"v_mfma_f32_16x16x32_bf16 v[52:55], v[172:175], v[180:183], v[52:55]\n"
"ds_read_b64_tr_b16 v[206:207], v211 offset:20480\n"
"v_add_f32 v235, v235, v239\n"
"v_mfma_f32_16x16x32_bf16 v[48:51], v[172:175], v[176:179], v[48:51]\n"
"v_add_f32 v240, v240, v244\n"
"s_waitcnt lgkmcnt(10)\n"
"v_mfma_f32_16x16x32_bf16 v[64:67], v[160:163], v[176:179], v[64:67]\n"
"s_add_u32 m0, s62, 55296\n"
"s_cmp_ge_u32 s50, 2\n"
"s_cbranch_scc1 .Lf_s12\n"
"buffer_load_dwordx4 v218, s[28:31], s60 offen offset:0  lds\n"
".Lf_s12:\n"
"ds_read_b64_tr_b16 v[172:173], v212 offset:9440\n"
"v_mfma_f32_16x16x32_bf16 v[68:71], v[160:163], v[180:183], v[68:71]\n"
"ds_read_b64_tr_b16 v[174:175], v212 offset:11744\n"
"v_mfma_f32_16x16x32_bf16 v[72:75], v[160:163], v[184:187], v[72:75]\n"
"v_add_f32 v241, v241, v245\n"
"v_mfma_f32_16x16x32_bf16 v[76:79], v[160:163], v[188:191], v[76:79]\n"
"v_add_f32 v242, v242, v246\n"
"s_waitcnt lgkmcnt(8)\n"
"v_mfma_f32_16x16x32_bf16 v[92:95], v[164:167], v[188:191], v[92:95]\n"
"ds_read_b64_tr_b16 v[160:161], v212 offset:18560\n"
"v_mfma_f32_16x16x32_bf16 v[88:91], v[164:167], v[184:187], v[88:91]\n"
"ds_read_b64_tr_b16 v[162:163], v212 offset:20864\n"
"v_mfma_f32_16x16x32_bf16 v[84:87], v[164:167], v[180:183], v[84:87]\n"
"v_add_f32 v243, v243, v247\n"
"v_mfma_f32_16x16x32_bf16 v[80:83], v[164:167], v[176:179], v[80:83]\n"
"v_add_f32 v232, v232, v240\n"
"s_waitcnt lgkmcnt(6)\n"
"v_mfma_f32_16x16x32_bf16 v[96:99], v[168:171], v[176:179], v[96:99]\n"
"ds_read_b64_tr_b16 v[164:165], v212 offset:27776\n"
"v_mfma_f32_16x16x32_bf16 v[100:103], v[168:171], v[180:183], v[100:103]\n"
"ds_read_b64_tr_b16 v[166:167], v212 offset:30080\n"
"v_mfma_f32_16x16x32_bf16 v[104:107], v[168:171], v[184:187], v[104:107]\n"
"v_add_f32 v233, v233, v241\n"
"v_mfma_f32_16x16x32_bf16 v[108:111], v[168:171], v[188:191], v[108:111]\n"
"v_add_f32 v234, v234, v242\n"
"s_waitcnt lgkmcnt(4)\n"
"v_mfma_f32_16x16x32_bf16 v[124:127], v[172:175], v[188:191], v[124:127]\n"
"ds_read_b64_tr_b16 v[168:169], v212 offset:18592\n"
"v_mfma_f32_16x16x32_bf16 v[120:123], v[172:175], v[184:187], v[120:123]\n"
"ds_read_b64_tr_b16 v[170:171], v212 offset:20896\n"
"v_mfma_f32_16x16x32_bf16 v[116:119], v[172:175], v[180:183], v[116:119]\n"
"v_add_f32 v235, v235, v243\n"
"v_mfma_f32_16x16x32_bf16 v[112:115], v[172:175], v[176:179], v[112:115]\n"
"ds_write_b128 v228, v[232:235] offset:0\n"
"s_waitcnt vmcnt(6)\n"
"s_barrier\n"
"s_waitcnt lgkmcnt(5)\n"
"v_mfma_f32_16x16x32_bf16 v[0:3], v[160:163], v[192:195], v[0:3]\n"
"s_mov_b32 s60, 3538944\n"
"s_add_u32 m0, s61, 0\n"
"s_nop 0\n"
"buffer_load_dwordx4 v214, s[40:43], s60 offen offset:0  lds\n"
"ds_read_b64_tr_b16 v[172:173], v212 offset:27808\n"
"v_mfma_f32_16x16x32_bf16 v[4:7], v[160:163], v[196:199], v[4:7]\n"
"ds_read_b64_tr_b16 v[174:175], v212 offset:30112\n"
"v_mfma_f32_16x16x32_bf16 v[8:11], v[160:163], v[200:203], v[8:11]\n"
"ds_read_b64_tr_b16 v[176:177], v208 offset:32768\n"
"v_mfma_f32_16x16x32_bf16 v[12:15], v[160:163], v[204:207], v[12:15]\n"
"ds_read_b64_tr_b16 v[178:179], v208 offset:36864\n"
"s_waitcnt lgkmcnt(7)\n"
"v_mfma_f32_16x16x32_bf16 v[28:31], v[164:167], v[204:207], v[28:31]\n"
"buffer_load_dwordx4 v215, s[44:47], s60 offen offset:1024  lds\n"
"ds_read_b64_tr_b16 v[160:161], v212 offset:18624\n"
"v_mfma_f32_16x16x32_bf16 v[24:27], v[164:167], v[200:203], v[24:27]\n"
"ds_read_b64_tr_b16 v[162:163], v212 offset:20928\n"
"v_mfma_f32_16x16x32_bf16 v[20:23], v[164:167], v[196:199], v[20:23]\n"
"ds_read_b64_tr_b16 v[180:181], v209 offset:32768\n"
"v_mfma_f32_16x16x32_bf16 v[16:19], v[164:167], v[192:195], v[16:19]\n"
"ds_read_b64_tr_b16 v[182:183], v209 offset:36864\n"
"s_waitcnt lgkmcnt(9)\n"
"v_mfma_f32_16x16x32_bf16 v[32:35], v[168:171], v[192:195], v[32:35]\n"
"buffer_load_dwordx4 v216, s[44:47], s60 offen offset:2048  lds\n"
"ds_read_b64_tr_b16 v[164:165], v212 offset:27840\n"
"v_mfma_f32_16x16x32_bf16 v[36:39], v[168:171], v[196:199], v[36:39]\n"
"ds_read_b64_tr_b16 v[166:167], v212 offset:30144\n"
"v_mfma_f32_16x16x32_bf16 v[40:43], v[168:171], v[200:203], v[40:43]\n"
"ds_read_b64_tr_b16 v[184:185], v210 offset:32768\n"
"v_mfma_f32_16x16x32_bf16 v[44:47], v[168:171], v[204:207], v[44:47]\n"
"ds_read_b64_tr_b16 v[186:187], v210 offset:36864\n"
"s_waitcnt lgkmcnt(10)\n"
"v_mfma_f32_16x16x32_bf16 v[60:63], v[172:175], v[204:207], v[60:63]\n"
"buffer_load_dwordx4 v217, s[44:47], s60 offen offset:3072  lds\n"
"ds_read_b64_tr_b16 v[168:169], v212 offset:18656\n"
"v_mfma_f32_16x16x32_bf16 v[56:59], v[172:175], v[200:203], v[56:59]\n"
"ds_read_b64_tr_b16 v[170:171], v212 offset:20960\n"
"v_mfma_f32_16x16x32_bf16 v[52:55], v[172:175], v[196:199], v[52:55]\n"
"ds_read_b64_tr_b16 v[188:189], v211 offset:32768\n"
"v_mfma_f32_16x16x32_bf16 v[48:51], v[172:175], v[192:195], v[48:51]\n"
"ds_read_b64_tr_b16 v[190:191], v211 offset:36864\n"
"s_waitcnt lgkmcnt(10)\n"
"v_mfma_f32_16x16x32_bf16 v[64:67], v[160:163], v[192:195], v[64:67]\n"
"s_add_u32 m0, s62, 0\n"
"s_cmp_ge_u32 s50, 2\n"
"s_cbranch_scc1 .Lf_s13\n"
"buffer_load_dwordx4 v218, s[28:31], s60 offen offset:0  lds\n"
".Lf_s13:\n"
"v_mfma_f32_16x16x32_bf16 v[68:71], v[160:163], v[196:199], v[68:71]\n"
"ds_read_b64_tr_b16 v[172:173], v212 offset:27872\n"
"v_mfma_f32_16x16x32_bf16 v[72:75], v[160:163], v[200:203], v[72:75]\n"
"ds_read_b64_tr_b16 v[174:175], v212 offset:30176\n"
"v_mfma_f32_16x16x32_bf16 v[76:79], v[160:163], v[204:207], v[76:79]\n"
"s_waitcnt lgkmcnt(8)\n"
"v_mfma_f32_16x16x32_bf16 v[92:95], v[164:167], v[204:207], v[92:95]\n"
"ds_read_b64_tr_b16 v[160:161], v213 offset:128\n"
"v_mfma_f32_16x16x32_bf16 v[88:91], v[164:167], v[200:203], v[88:91]\n"
"v_mfma_f32_16x16x32_bf16 v[84:87], v[164:167], v[196:199], v[84:87]\n"
"ds_read_b64_tr_b16 v[162:163], v213 offset:2432\n"
"v_mfma_f32_16x16x32_bf16 v[80:83], v[164:167], v[192:195], v[80:83]\n"
"s_waitcnt lgkmcnt(6)\n"
"v_mfma_f32_16x16x32_bf16 v[96:99], v[168:171], v[192:195], v[96:99]\n"
"ds_read_b64_tr_b16 v[164:165], v213 offset:9344\n"
"v_mfma_f32_16x16x32_bf16 v[100:103], v[168:171], v[196:199], v[100:103]\n"
"v_mfma_f32_16x16x32_bf16 v[104:107], v[168:171], v[200:203], v[104:107]\n"
"ds_read_b64_tr_b16 v[166:167], v213 offset:11648\n"
"v_mfma_f32_16x16x32_bf16 v[108:111], v[168:171], v[204:207], v[108:111]\n"
"s_waitcnt lgkmcnt(4)\n"
"v_mfma_f32_16x16x32_bf16 v[124:127], v[172:175], v[204:207], v[124:127]\n"
"ds_read_b64_tr_b16 v[168:169], v213 offset:160\n"
"v_mfma_f32_16x16x32_bf16 v[120:123], v[172:175], v[200:203], v[120:123]\n"
"v_mfma_f32_16x16x32_bf16 v[116:119], v[172:175], v[196:199], v[116:119]\n"
"ds_read_b64_tr_b16 v[170:171], v213 offset:2464\n"
"v_mfma_f32_16x16x32_bf16 v[112:115], v[172:175], v[192:195], v[112:115]\n"
"s_waitcnt vmcnt(4)\n"
"s_barrier\n"
"s_waitcnt lgkmcnt(4)\n"
"v_mfma_f32_16x16x32_bf16 v[0:3], v[160:163], v[176:179], v[0:3]\n"
"s_mov_b32 s60, 3833856\n"
"s_add_u32 m0, s61, 18432\n"
"s_nop 0\n"
"buffer_load_dwordx4 v214, s[40:43], s60 offen offset:0  lds\n"
"ds_read_b64_tr_b16 v[172:173], v213 offset:9376\n"
"v_mfma_f32_16x16x32_bf16 v[4:7], v[160:163], v[180:183], v[4:7]\n"
"ds_read_b64_tr_b16 v[174:175], v213 offset:11680\n"
"v_mfma_f32_16x16x32_bf16 v[8:11], v[160:163], v[184:187], v[8:11]\n"
"ds_read_b64_tr_b16 v[192:193], v208 offset:49152\n"
"v_mfma_f32_16x16x32_bf16 v[12:15], v[160:163], v[188:191], v[12:15]\n"
"ds_read_b64_tr_b16 v[194:195], v208 offset:53248\n"
"s_waitcnt lgkmcnt(6)\n"
"v_mfma_f32_16x16x32_bf16 v[28:31], v[164:167], v[188:191], v[28:31]\n"
"buffer_load_dwordx4 v215, s[44:47], s60 offen offset:1024  lds\n"
"ds_read_b64_tr_b16 v[160:161], v213 offset:192\n"
"v_mfma_f32_16x16x32_bf16 v[24:27], v[164:167], v[184:187], v[24:27]\n"
"ds_read_b64_tr_b16 v[162:163], v213 offset:2496\n"
"v_mfma_f32_16x16x32_bf16 v[20:23], v[164:167], v[180:183], v[20:23]\n"
"ds_read_b64_tr_b16 v[196:197], v209 offset:49152\n"
"v_mfma_f32_16x16x32_bf16 v[16:19], v[164:167], v[176:179], v[16:19]\n"
"ds_read_b64_tr_b16 v[198:199], v209 offset:53248\n"
"s_waitcnt lgkmcnt(8)\n"
"v_mfma_f32_16x16x32_bf16 v[32:35], v[168:171], v[176:179], v[32:35]\n"
"buffer_load_dwordx4 v216, s[44:47], s60 offen offset:2048  lds\n"
"ds_read_b64_tr_b16 v[164:165], v213 offset:9408\n"
"v_mfma_f32_16x16x32_bf16 v[36:39], v[168:171], v[180:183], v[36:39]\n"
"ds_read_b64_tr_b16 v[166:167], v213 offset:11712\n"
"v_mfma_f32_16x16x32_bf16 v[40:43], v[168:171], v[184:187], v[40:43]\n"
"ds_read_b64_tr_b16 v[200:201], v210 offset:49152\n"
"v_mfma_f32_16x16x32_bf16 v[44:47], v[168:171], v[188:191], v[44:47]\n"
"ds_read_b64_tr_b16 v[202:203], v210 offset:53248\n"
"s_waitcnt lgkmcnt(10)\n"
"v_mfma_f32_16x16x32_bf16 v[60:63], v[172:175], v[188:191], v[60:63]\n"
"buffer_load_dwordx4 v217, s[44:47], s60 offen offset:3072  lds\n"
"ds_read_b64_tr_b16 v[168:169], v213 offset:224\n"
"v_mfma_f32_16x16x32_bf16 v[56:59], v[172:175], v[184:187], v[56:59]\n"
"ds_read_b64_tr_b16 v[170:171], v213 offset:2528\n"
"v_mfma_f32_16x16x32_bf16 v[52:55], v[172:175], v[180:183], v[52:55]\n"
"ds_read_b64_tr_b16 v[204:205], v211 offset:49152\n"
"v_mfma_f32_16x16x32_bf16 v[48:51], v[172:175], v[176:179], v[48:51]\n"
"ds_read_b64_tr_b16 v[206:207], v211 offset:53248\n"
"s_waitcnt lgkmcnt(10)\n"
"v_mfma_f32_16x16x32_bf16 v[64:67], v[160:163], v[176:179], v[64:67]\n"
"s_add_u32 m0, s62, 18432\n"
"s_cmp_ge_u32 s50, 2\n"
"s_cbranch_scc1 .Lf_s14\n"
"buffer_load_dwordx4 v218, s[28:31], s60 offen offset:0  lds\n"
".Lf_s14:\n"
"v_mfma_f32_16x16x32_bf16 v[68:71], v[160:163], v[180:183], v[68:71]\n"
"ds_read_b64_tr_b16 v[172:173], v213 offset:9440\n"
"v_mfma_f32_16x16x32_bf16 v[72:75], v[160:163], v[184:187], v[72:75]\n"
"ds_read_b64_tr_b16 v[174:175], v213 offset:11744\n"
"v_mfma_f32_16x16x32_bf16 v[76:79], v[160:163], v[188:191], v[76:79]\n"
"s_waitcnt lgkmcnt(8)\n"
"v_mfma_f32_16x16x32_bf16 v[92:95], v[164:167], v[188:191], v[92:95]\n"
"ds_read_b64_tr_b16 v[160:161], v213 offset:18560\n"
"v_mfma_f32_16x16x32_bf16 v[88:91], v[164:167], v[184:187], v[88:91]\n"
"v_mfma_f32_16x16x32_bf16 v[84:87], v[164:167], v[180:183], v[84:87]\n"
"ds_read_b64_tr_b16 v[162:163], v213 offset:20864\n"
"v_mfma_f32_16x16x32_bf16 v[80:83], v[164:167], v[176:179], v[80:83]\n"
"s_waitcnt lgkmcnt(6)\n"
"v_mfma_f32_16x16x32_bf16 v[96:99], v[168:171], v[176:179], v[96:99]\n"
"ds_read_b64_tr_b16 v[164:165], v213 offset:27776\n"
"v_mfma_f32_16x16x32_bf16 v[100:103], v[168:171], v[180:183], v[100:103]\n"
"v_mfma_f32_16x16x32_bf16 v[104:107], v[168:171], v[184:187], v[104:107]\n"
"ds_read_b64_tr_b16 v[166:167], v213 offset:30080\n"
"v_mfma_f32_16x16x32_bf16 v[108:111], v[168:171], v[188:191], v[108:111]\n"
"s_waitcnt lgkmcnt(4)\n"
"v_mfma_f32_16x16x32_bf16 v[124:127], v[172:175], v[188:191], v[124:127]\n"
"ds_read_b64_tr_b16 v[168:169], v213 offset:18592\n"
"v_mfma_f32_16x16x32_bf16 v[120:123], v[172:175], v[184:187], v[120:123]\n"
"ds_read_b64_tr_b16 v[170:171], v213 offset:20896\n"
"v_mfma_f32_16x16x32_bf16 v[116:119], v[172:175], v[180:183], v[116:119]\n"
"s_and_b32 s54, s48, 7\n"
"s_lshl_b32 s54, s54, 6\n"
"s_add_u32 s98, s54, 315392\n"
"v_mov_b32 v156, 0\n"
"v_mov_b32 v157, 64\n"
"s_cmp_lg_u32 s50, 3\n"
"s_cbranch_scc1 .Lf_w1\n"
"v_mov_b32 v157, 0\n"
"s_mov_b64 exec, 1\n"
"buffer_atomic_add v157, v156, s[64:67], s98 offen offset:0 sc0\n"
"s_mov_b64 exec, -1\n"
".Lf_w1:\n"
"v_mfma_f32_16x16x32_bf16 v[112:115], v[172:175], v[176:179], v[112:115]\n"
"s_waitcnt vmcnt(4)\n"
"s_barrier\n"
"s_waitcnt lgkmcnt(4)\n"
"v_mfma_f32_16x16x32_bf16 v[0:3], v[160:163], v[192:195], v[0:3]\n"
"s_mov_b32 s60, 4128768\n"
"s_add_u32 m0, s61, 36864\n"
"s_nop 0\n"
"buffer_load_dwordx4 v214, s[40:43], s60 offen offset:0  lds\n"
"ds_read_b64_tr_b16 v[172:173], v213 offset:27808\n"
"v_mfma_f32_16x16x32_bf16 v[4:7], v[160:163], v[196:199], v[4:7]\n"
"ds_read_b64_tr_b16 v[174:175], v213 offset:30112\n"
"v_mfma_f32_16x16x32_bf16 v[8:11], v[160:163], v[200:203], v[8:11]\n"
"ds_read_b64_tr_b16 v[176:177], v208 offset:0\n"
"v_mfma_f32_16x16x32_bf16 v[12:15], v[160:163], v[204:207], v[12:15]\n"
"ds_read_b64_tr_b16 v[178:179], v208 offset:4096\n"
"s_waitcnt lgkmcnt(6)\n"
"v_mfma_f32_16x16x32_bf16 v[28:31], v[164:167], v[204:207], v[28:31]\n"
"buffer_load_dwordx4 v215, s[44:47], s60 offen offset:1024  lds\n"
"ds_read_b64_tr_b16 v[160:161], v213 offset:18624\n"
"v_mfma_f32_16x16x32_bf16 v[24:27], v[164:167], v[200:203], v[24:27]\n"
"ds_read_b64_tr_b16 v[162:163], v213 offset:20928\n"
"v_mfma_f32_16x16x32_bf16 v[20:23], v[164:167], v[196:199], v[20:23]\n"
"ds_read_b64_tr_b16 v[180:181], v209 offset:0\n"
"ds_read_b64_tr_b16 v[182:183], v209 offset:4096\n"
"v_mfma_f32_16x16x32_bf16 v[16:19], v[164:167], v[192:195], v[16:19]\n"
"v_lshrrev_b32 v236, 5, v219\n"
"s_waitcnt lgkmcnt(8)\n"
"v_mfma_f32_16x16x32_bf16 v[32:35], v[168:171], v[192:195], v[32:35]\n"
"buffer_load_dwordx4 v216, s[44:47], s60 offen offset:2048  lds\n"
"ds_read_b64_tr_b16 v[164:165], v213 offset:27840\n"
"v_mfma_f32_16x16x32_bf16 v[36:39], v[168:171], v[196:199], v[36:39]\n"
"ds_read_b64_tr_b16 v[166:167], v213 offset:30144\n"
"v_mfma_f32_16x16x32_bf16 v[40:43], v[168:171], v[200:203], v[40:43]\n"
"ds_read_b64_tr_b16 v[184:185], v210 offset:0\n"
"ds_read_b64_tr_b16 v[186:187], v210 offset:4096\n"
"v_mfma_f32_16x16x32_bf16 v[44:47], v[168:171], v[204:207], v[44:47]\n"
"v_lshlrev_b32 v236, 11, v236\n"
"s_waitcnt lgkmcnt(10)\n"
"v_mfma_f32_16x16x32_bf16 v[60:63], v[172:175], v[204:207], v[60:63]\n"
"buffer_load_dwordx4 v217, s[44:47], s60 offen offset:3072  lds\n"
"ds_read_b64_tr_b16 v[168:169], v213 offset:18656\n"
"v_mfma_f32_16x16x32_bf16 v[56:59], v[172:175], v[200:203], v[56:59]\n"
"ds_read_b64_tr_b16 v[170:171], v213 offset:20960\n"
"v_mfma_f32_16x16x32_bf16 v[52:55], v[172:175], v[196:199], v[52:55]\n"
"ds_read_b64_tr_b16 v[188:189], v211 offset:0\n"
"ds_read_b64_tr_b16 v[190:191], v211 offset:4096\n"
"v_mfma_f32_16x16x32_bf16 v[48:51], v[172:175], v[192:195], v[48:51]\n"
"v_and_b32 v237, 31, v219\n"
"s_waitcnt lgkmcnt(10)\n"
"v_mfma_f32_16x16x32_bf16 v[64:67], v[160:163], v[192:195], v[64:67]\n"
"s_add_u32 m0, s62, 36864\n"
"s_cmp_ge_u32 s50, 2\n"
"s_cbranch_scc1 .Lf_s15\n"
"buffer_load_dwordx4 v218, s[28:31], s60 offen offset:0  lds\n"
".Lf_s15:\n"
"v_mfma_f32_16x16x32_bf16 v[68:71], v[160:163], v[196:199], v[68:71]\n"
"ds_read_b64_tr_b16 v[172:173], v213 offset:27872\n"
"v_mfma_f32_16x16x32_bf16 v[72:75], v[160:163], v[200:203], v[72:75]\n"
"ds_read_b64_tr_b16 v[174:175], v213 offset:30176\n"
"v_mfma_f32_16x16x32_bf16 v[76:79], v[160:163], v[204:207], v[76:79]\n"
"v_lshl_add_u32 v236, v237, 4, v236\n"
"s_waitcnt lgkmcnt(8)\n"
"v_mfma_f32_16x16x32_bf16 v[92:95], v[164:167], v[204:207], v[92:95]\n"
"ds_read_b64_tr_b16 v[160:161], v212 offset:128\n"
"v_mfma_f32_16x16x32_bf16 v[88:91], v[164:167], v[200:203], v[88:91]\n"
"ds_read_b64_tr_b16 v[162:163], v212 offset:2432\n"
"v_mfma_f32_16x16x32_bf16 v[84:87], v[164:167], v[196:199], v[84:87]\n"
"s_lshl_b32 s54, s51, 14\n"
"v_mfma_f32_16x16x32_bf16 v[80:83], v[164:167], v[192:195], v[80:83]\n"
"s_waitcnt lgkmcnt(6)\n"
"v_mfma_f32_16x16x32_bf16 v[96:99], v[168:171], v[192:195], v[96:99]\n"
"ds_read_b64_tr_b16 v[164:165], v212 offset:9344\n"
"v_mfma_f32_16x16x32_bf16 v[100:103], v[168:171], v[196:199], v[100:103]\n"
"ds_read_b64_tr_b16 v[166:167], v212 offset:11648\n"
"v_mfma_f32_16x16x32_bf16 v[104:107], v[168:171], v[200:203], v[104:107]\n"
"s_lshl_b32 s55, s50, 12\n"
"v_mfma_f32_16x16x32_bf16 v[108:111], v[168:171], v[204:207], v[108:111]\n"
"s_waitcnt lgkmcnt(4)\n"
"v_mfma_f32_16x16x32_bf16 v[124:127], v[172:175], v[204:207], v[124:127]\n"
"ds_read_b64_tr_b16 v[168:169], v212 offset:160\n"
"v_mfma_f32_16x16x32_bf16 v[120:123], v[172:175], v[200:203], v[120:123]\n"
"ds_read_b64_tr_b16 v[170:171], v212 offset:2464\n"
"v_mfma_f32_16x16x32_bf16 v[116:119], v[172:175], v[196:199], v[116:119]\n"
"s_add_u32 s54, s54, s55\n"
"v_mfma_f32_16x16x32_bf16 v[112:115], v[172:175], v[192:195], v[112:115]\n"
"s_add_u32 s99, s54, 131072\n"
"s_waitcnt vmcnt(4)\n"
"s_barrier\n"
"s_waitcnt lgkmcnt(4)\n"
"v_mfma_f32_16x16x32_bf16 v[0:3], v[160:163], v[176:179], v[0:3]\n"
"s_mov_b32 s60, 4423680\n"
"s_add_u32 m0, s61, 55296\n"
"s_nop 0\n"
"buffer_load_dwordx4 v214, s[40:43], s60 offen offset:0  lds\n"
"ds_read_b64_tr_b16 v[172:173], v212 offset:9376\n"
"v_mfma_f32_16x16x32_bf16 v[4:7], v[160:163], v[180:183], v[4:7]\n"
"ds_read_b64_tr_b16 v[174:175], v212 offset:11680\n"
"v_mfma_f32_16x16x32_bf16 v[8:11], v[160:163], v[184:187], v[8:11]\n"
"ds_read_b64_tr_b16 v[192:193], v208 offset:16384\n"
"v_mfma_f32_16x16x32_bf16 v[12:15], v[160:163], v[188:191], v[12:15]\n"
"ds_read_b64_tr_b16 v[194:195], v208 offset:20480\n"
"s_waitcnt lgkmcnt(6)\n"
"v_mfma_f32_16x16x32_bf16 v[28:31], v[164:167], v[188:191], v[28:31]\n"
"buffer_load_dwordx4 v215, s[44:47], s60 offen offset:1024  lds\n"
"ds_read_b64_tr_b16 v[160:161], v212 offset:192\n"
"v_mfma_f32_16x16x32_bf16 v[24:27], v[164:167], v[184:187], v[24:27]\n"
"ds_read_b64_tr_b16 v[162:163], v212 offset:2496\n"
"v_mfma_f32_16x16x32_bf16 v[20:23], v[164:167], v[180:183], v[20:23]\n"
"ds_read_b64_tr_b16 v[196:197], v209 offset:16384\n"
"v_mfma_f32_16x16x32_bf16 v[16:19], v[164:167], v[176:179], v[16:19]\n"
"ds_read_b64_tr_b16 v[198:199], v209 offset:20480\n"
"s_waitcnt lgkmcnt(8)\n"
"v_mfma_f32_16x16x32_bf16 v[32:35], v[168:171], v[176:179], v[32:35]\n"
"buffer_load_dwordx4 v216, s[44:47], s60 offen offset:2048  lds\n"
"ds_read_b64_tr_b16 v[164:165], v212 offset:9408\n"
"v_mfma_f32_16x16x32_bf16 v[36:39], v[168:171], v[180:183], v[36:39]\n"
"ds_read_b64_tr_b16 v[166:167], v212 offset:11712\n"
"v_mfma_f32_16x16x32_bf16 v[40:43], v[168:171], v[184:187], v[40:43]\n"
"ds_read_b64_tr_b16 v[200:201], v210 offset:16384\n"
"v_mfma_f32_16x16x32_bf16 v[44:47], v[168:171], v[188:191], v[44:47]\n"
"ds_read_b64_tr_b16 v[202:203], v210 offset:20480\n"
"s_waitcnt lgkmcnt(10)\n"
"v_mfma_f32_16x16x32_bf16 v[60:63], v[172:175], v[188:191], v[60:63]\n"
"buffer_load_dwordx4 v217, s[44:47], s60 offen offset:3072  lds\n"
"ds_read_b64_tr_b16 v[168:169], v212 offset:224\n"
"v_mfma_f32_16x16x32_bf16 v[56:59], v[172:175], v[184:187], v[56:59]\n"
"ds_read_b64_tr_b16 v[170:171], v212 offset:2528\n"
"v_mfma_f32_16x16x32_bf16 v[52:55], v[172:175], v[180:183], v[52:55]\n"
"ds_read_b64_tr_b16 v[204:205], v211 offset:16384\n"
"v_mfma_f32_16x16x32_bf16 v[48:51], v[172:175], v[176:179], v[48:51]\n"
"ds_read_b64_tr_b16 v[206:207], v211 offset:20480\n"
"s_waitcnt lgkmcnt(10)\n"
"v_mfma_f32_16x16x32_bf16 v[64:67], v[160:163], v[176:179], v[64:67]\n"
"s_add_u32 m0, s62, 55296\n"
"s_cmp_ge_u32 s50, 2\n"
"s_cbranch_scc1 .Lf_s16\n"
"buffer_load_dwordx4 v218, s[28:31], s60 offen offset:0  lds\n"
".Lf_s16:\n"
"v_mfma_f32_16x16x32_bf16 v[68:71], v[160:163], v[180:183], v[68:71]\n"
"ds_read_b64_tr_b16 v[172:173], v212 offset:9440\n"
"v_mfma_f32_16x16x32_bf16 v[72:75], v[160:163], v[184:187], v[72:75]\n"
"ds_read_b64_tr_b16 v[174:175], v212 offset:11744\n"
"v_mfma_f32_16x16x32_bf16 v[76:79], v[160:163], v[188:191], v[76:79]\n"
"s_waitcnt lgkmcnt(8)\n"
"v_mfma_f32_16x16x32_bf16 v[92:95], v[164:167], v[188:191], v[92:95]\n"
"ds_read_b64_tr_b16 v[160:161], v212 offset:18560\n"
"v_mfma_f32_16x16x32_bf16 v[88:91], v[164:167], v[184:187], v[88:91]\n"
"ds_read_b64_tr_b16 v[162:163], v212 offset:20864\n"
"v_mfma_f32_16x16x32_bf16 v[84:87], v[164:167], v[180:183], v[84:87]\n"
"buffer_load_dwordx4 v[220:223], v236, s[64:67], s99 offen offset:0 sc1\n"
"v_mfma_f32_16x16x32_bf16 v[80:83], v[164:167], v[176:179], v[80:83]\n"
"s_waitcnt lgkmcnt(6)\n"
"v_mfma_f32_16x16x32_bf16 v[96:99], v[168:171], v[176:179], v[96:99]\n"
"ds_read_b64_tr_b16 v[164:165], v212 offset:27776\n"
"v_mfma_f32_16x16x32_bf16 v[100:103], v[168:171], v[180:183], v[100:103]\n"
"ds_read_b64_tr_b16 v[166:167], v212 offset:30080\n"
"v_mfma_f32_16x16x32_bf16 v[104:107], v[168:171], v[184:187], v[104:107]\n"
"buffer_load_dwordx4 v[224:227], v236, s[64:67], s99 offen offset:512 sc1\n"
"v_mfma_f32_16x16x32_bf16 v[108:111], v[168:171], v[188:191], v[108:111]\n"
"s_waitcnt lgkmcnt(4)\n"
"v_mfma_f32_16x16x32_bf16 v[124:127], v[172:175], v[188:191], v[124:127]\n"
"ds_read_b64_tr_b16 v[168:169], v212 offset:18592\n"
"ds_read_b64_tr_b16 v[170:171], v212 offset:20896\n"
"v_mfma_f32_16x16x32_bf16 v[120:123], v[172:175], v[184:187], v[120:123]\n"
"buffer_load_dwordx4 v[228:231], v236, s[64:67], s99 offen offset:1024 sc1\n"
"v_mfma_f32_16x16x32_bf16 v[116:119], v[172:175], v[180:183], v[116:119]\n"
"buffer_load_dwordx4 v[232:235], v236, s[64:67], s99 offen offset:1536 sc1\n"
"v_mfma_f32_16x16x32_bf16 v[112:115], v[172:175], v[176:179], v[112:115]\n"
"s_waitcnt vmcnt(12)\n"
"v_readfirstlane_b32 s54, v157\n"
"s_nop 0\n"
"s_cmp_lg_u32 s54, 64\n"
"s_cbranch_scc1 .Lf_wps\n"
".Lf_wpok:\n"
"s_waitcnt vmcnt(8)\n"
"s_barrier\n"
"s_waitcnt lgkmcnt(4)\n"
"v_mfma_f32_16x16x32_bf16 v[0:3], v[160:163], v[192:195], v[0:3]\n"
"s_mov_b32 s60, 4718592\n"
"s_add_u32 m0, s61, 0\n"
"s_nop 0\n"
"buffer_load_dwordx4 v214, s[40:43], s60 offen offset:0  lds\n"
"ds_read_b64_tr_b16 v[172:173], v212 offset:27808\n"
"v_mfma_f32_16x16x32_bf16 v[4:7], v[160:163], v[196:199], v[4:7]\n"
"ds_read_b64_tr_b16 v[174:175], v212 offset:30112\n"
"v_mfma_f32_16x16x32_bf16 v[8:11], v[160:163], v[200:203], v[8:11]\n"
"ds_read_b64_tr_b16 v[176:177], v208 offset:32768\n"
"v_mfma_f32_16x16x32_bf16 v[12:15], v[160:163], v[204:207], v[12:15]\n"
"ds_read_b64_tr_b16 v[178:179], v208 offset:36864\n"
"s_waitcnt lgkmcnt(6)\n"
"v_mfma_f32_16x16x32_bf16 v[28:31], v[164:167], v[204:207], v[28:31]\n"
"buffer_load_dwordx4 v215, s[44:47], s60 offen offset:1024  lds\n"
"ds_read_b64_tr_b16 v[160:161], v212 offset:18624\n"
"v_mfma_f32_16x16x32_bf16 v[24:27], v[164:167], v[200:203], v[24:27]\n"
"ds_read_b64_tr_b16 v[162:163], v212 offset:20928\n"
"v_mfma_f32_16x16x32_bf16 v[20:23], v[164:167], v[196:199], v[20:23]\n"
"ds_read_b64_tr_b16 v[180:181], v209 offset:32768\n"
"v_mfma_f32_16x16x32_bf16 v[16:19], v[164:167], v[192:195], v[16:19]\n"
"ds_read_b64_tr_b16 v[182:183], v209 offset:36864\n"
"s_waitcnt lgkmcnt(8)\n"
"v_mfma_f32_16x16x32_bf16 v[32:35], v[168:171], v[192:195], v[32:35]\n"
"buffer_load_dwordx4 v216, s[44:47], s60 offen offset:2048  lds\n"
"ds_read_b64_tr_b16 v[164:165], v212 offset:27840\n"
"v_mfma_f32_16x16x32_bf16 v[36:39], v[168:171], v[196:199], v[36:39]\n"
"ds_read_b64_tr_b16 v[166:167], v212 offset:30144\n"
"v_mfma_f32_16x16x32_bf16 v[40:43], v[168:171], v[200:203], v[40:43]\n"
"ds_read_b64_tr_b16 v[184:185], v210 offset:32768\n"
"v_mfma_f32_16x16x32_bf16 v[44:47], v[168:171], v[204:207], v[44:47]\n"
"ds_read_b64_tr_b16 v[186:187], v210 offset:36864\n"
"s_waitcnt lgkmcnt(10)\n"
"v_mfma_f32_16x16x32_bf16 v[60:63], v[172:175], v[204:207], v[60:63]\n"
"buffer_load_dwordx4 v217, s[44:47], s60 offen offset:3072  lds\n"
"ds_read_b64_tr_b16 v[168:169], v212 offset:18656\n"
"v_mfma_f32_16x16x32_bf16 v[56:59], v[172:175], v[200:203], v[56:59]\n"
"ds_read_b64_tr_b16 v[170:171], v212 offset:20960\n"
"v_mfma_f32_16x16x32_bf16 v[52:55], v[172:175], v[196:199], v[52:55]\n"
"ds_read_b64_tr_b16 v[188:189], v211 offset:32768\n"
"v_mfma_f32_16x16x32_bf16 v[48:51], v[172:175], v[192:195], v[48:51]\n"
"ds_read_b64_tr_b16 v[190:191], v211 offset:36864\n"
"s_waitcnt lgkmcnt(10)\n"
"v_mfma_f32_16x16x32_bf16 v[64:67], v[160:163], v[192:195], v[64:67]\n"
"s_add_u32 m0, s62, 0\n"
"s_cmp_ge_u32 s50, 2\n"
"s_cbranch_scc1 .Lf_s17\n"
"buffer_load_dwordx4 v218, s[28:31], s60 offen offset:0  lds\n"
".Lf_s17:\n"
"v_mfma_f32_16x16x32_bf16 v[68:71], v[160:163], v[196:199], v[68:71]\n"
"ds_read_b64_tr_b16 v[172:173], v212 offset:27872\n"
"v_mfma_f32_16x16x32_bf16 v[72:75], v[160:163], v[200:203], v[72:75]\n"
"ds_read_b64_tr_b16 v[174:175], v212 offset:30176\n"
"v_mfma_f32_16x16x32_bf16 v[76:79], v[160:163], v[204:207], v[76:79]\n"
"s_waitcnt lgkmcnt(8)\n"
"v_mfma_f32_16x16x32_bf16 v[92:95], v[164:167], v[204:207], v[92:95]\n"
"ds_read_b64_tr_b16 v[160:161], v213 offset:128\n"
"v_mfma_f32_16x16x32_bf16 v[88:91], v[164:167], v[200:203], v[88:91]\n"
"v_mfma_f32_16x16x32_bf16 v[84:87], v[164:167], v[196:199], v[84:87]\n"
"ds_read_b64_tr_b16 v[162:163], v213 offset:2432\n"
"v_mfma_f32_16x16x32_bf16 v[80:83], v[164:167], v[192:195], v[80:83]\n"
"s_waitcnt lgkmcnt(6)\n"
"v_mfma_f32_16x16x32_bf16 v[96:99], v[168:171], v[192:195], v[96:99]\n"
"ds_read_b64_tr_b16 v[164:165], v213 offset:9344\n"
"v_mfma_f32_16x16x32_bf16 v[100:103], v[168:171], v[196:199], v[100:103]\n"
"v_mfma_f32_16x16x32_bf16 v[104:107], v[168:171], v[200:203], v[104:107]\n"
"ds_read_b64_tr_b16 v[166:167], v213 offset:11648\n"
"v_mfma_f32_16x16x32_bf16 v[108:111], v[168:171], v[204:207], v[108:111]\n"
"s_waitcnt lgkmcnt(4)\n"
"v_mfma_f32_16x16x32_bf16 v[124:127], v[172:175], v[204:207], v[124:127]\n"
"ds_read_b64_tr_b16 v[168:169], v213 offset:160\n"
"v_mfma_f32_16x16x32_bf16 v[120:123], v[172:175], v[200:203], v[120:123]\n"
"v_mfma_f32_16x16x32_bf16 v[116:119], v[172:175], v[196:199], v[116:119]\n"
"ds_read_b64_tr_b16 v[170:171], v213 offset:2464\n"
"v_mfma_f32_16x16x32_bf16 v[112:115], v[172:175], v[192:195], v[112:115]\n"
"s_waitcnt vmcnt(8)\n"
"s_barrier\n"
"s_waitcnt lgkmcnt(4)\n"
"v_mfma_f32_16x16x32_bf16 v[0:3], v[160:163], v[176:179], v[0:3]\n"
"s_mov_b32 s60, 5013504\n"
"s_add_u32 m0, s61, 18432\n"
"s_nop 0\n"
"buffer_load_dwordx4 v214, s[40:43], s60 offen offset:0  lds\n"
"ds_read_b64_tr_b16 v[172:173], v213 offset:9376\n"
"v_mfma_f32_16x16x32_bf16 v[4:7], v[160:163], v[180:183], v[4:7]\n"
"ds_read_b64_tr_b16 v[174:175], v213 offset:11680\n"
"v_mfma_f32_16x16x32_bf16 v[8:11], v[160:163], v[184:187], v[8:11]\n"
"ds_read_b64_tr_b16 v[192:193], v208 offset:49152\n"
"v_mfma_f32_16x16x32_bf16 v[12:15], v[160:163], v[188:191], v[12:15]\n"
"ds_read_b64_tr_b16 v[194:195], v208 offset:53248\n"
"s_waitcnt lgkmcnt(6)\n"
"v_mfma_f32_16x16x32_bf16 v[28:31], v[164:167], v[188:191], v[28:31]\n"
"buffer_load_dwordx4 v215, s[44:47], s60 offen offset:1024  lds\n"
"ds_read_b64_tr_b16 v[160:161], v213 offset:192\n"
"v_mfma_f32_16x16x32_bf16 v[24:27], v[164:167], v[184:187], v[24:27]\n"
"ds_read_b64_tr_b16 v[162:163], v213 offset:2496\n"
"v_mfma_f32_16x16x32_bf16 v[20:23], v[164:167], v[180:183], v[20:23]\n"
"ds_read_b64_tr_b16 v[196:197], v209 offset:49152\n"
"v_mfma_f32_16x16x32_bf16 v[16:19], v[164:167], v[176:179], v[16:19]\n"
"ds_read_b64_tr_b16 v[198:199], v209 offset:53248\n"
"s_waitcnt lgkmcnt(8)\n"
"v_mfma_f32_16x16x32_bf16 v[32:35], v[168:171], v[176:179], v[32:35]\n"
"buffer_load_dwordx4 v216, s[44:47], s60 offen offset:2048  lds\n"
"ds_read_b64_tr_b16 v[164:165], v213 offset:9408\n"
"v_mfma_f32_16x16x32_bf16 v[36:39], v[168:171], v[180:183], v[36:39]\n"
"ds_read_b64_tr_b16 v[166:167], v213 offset:11712\n"
"v_mfma_f32_16x16x32_bf16 v[40:43], v[168:171], v[184:187], v[40:43]\n"
"ds_read_b64_tr_b16 v[200:201], v210 offset:49152\n"
"v_mfma_f32_16x16x32_bf16 v[44:47], v[168:171], v[188:191], v[44:47]\n"
"ds_read_b64_tr_b16 v[202:203], v210 offset:53248\n"
"s_waitcnt lgkmcnt(10)\n"
"v_mfma_f32_16x16x32_bf16 v[60:63], v[172:175], v[188:191], v[60:63]\n"
"buffer_load_dwordx4 v217, s[44:47], s60 offen offset:3072  lds\n"
"ds_read_b64_tr_b16 v[168:169], v213 offset:224\n"
"v_mfma_f32_16x16x32_bf16 v[56:59], v[172:175], v[184:187], v[56:59]\n"
"ds_read_b64_tr_b16 v[170:171], v213 offset:2528\n"
"v_mfma_f32_16x16x32_bf16 v[52:55], v[172:175], v[180:183], v[52:55]\n"
"ds_read_b64_tr_b16 v[204:205], v211 offset:49152\n"
"v_mfma_f32_16x16x32_bf16 v[48:51], v[172:175], v[176:179], v[48:51]\n"
"ds_read_b64_tr_b16 v[206:207], v211 offset:53248\n"
"s_waitcnt lgkmcnt(10)\n"
"v_mfma_f32_16x16x32_bf16 v[64:67], v[160:163], v[176:179], v[64:67]\n"
"s_add_u32 m0, s62, 18432\n"
"s_cmp_ge_u32 s50, 2\n"
"s_cbranch_scc1 .Lf_s18\n"
"buffer_load_dwordx4 v218, s[28:31], s60 offen offset:0  lds\n"
".Lf_s18:\n"
"v_mfma_f32_16x16x32_bf16 v[68:71], v[160:163], v[180:183], v[68:71]\n"
"ds_read_b64_tr_b16 v[172:173], v213 offset:9440\n"
"v_mfma_f32_16x16x32_bf16 v[72:75], v[160:163], v[184:187], v[72:75]\n"
"ds_read_b64_tr_b16 v[174:175], v213 offset:11744\n"
"v_mfma_f32_16x16x32_bf16 v[76:79], v[160:163], v[188:191], v[76:79]\n"
"s_waitcnt lgkmcnt(8)\n"
"v_mfma_f32_16x16x32_bf16 v[92:95], v[164:167], v[188:191], v[92:95]\n"
"ds_read_b64_tr_b16 v[160:161], v213 offset:18560\n"
"ds_read_b64_tr_b16 v[162:163], v213 offset:20864\n"
"s_waitcnt vmcnt(11)\n"
"v_and_b32 v237, v221, v223\n"
"s_waitcnt vmcnt(10)\n"
"v_and_b32 v238, v225, v227\n"
"s_waitcnt vmcnt(9)\n"
"v_and_b32 v237, v237, v229\n"
"v_and_b32 v238, v238, v231\n"
"s_waitcnt vmcnt(8)\n"
"v_and_b32 v237, v237, v233\n"
"v_and_b32 v238, v238, v235\n"
"v_and_b32 v237, v237, v238\n"
"v_cmp_ne_u32 s[96:97], 1, v237\n"
"s_nop 0\n"
"s_cmp_lg_u64 s[96:97], 0\n"
"s_cbranch_scc1 .Lf_stslowb\n"
".Lf_stokb:\n"
"v_mfma_f32_16x16x32_bf16 v[88:91], v[164:167], v[184:187], v[88:91]\n"
"v_add_f32 v220, v220, v224\n"
"v_add_f32 v222, v222, v226\n"
"v_add_f32 v228, v228, v232\n"
"v_mfma_f32_16x16x32_bf16 v[84:87], v[164:167], v[180:183], v[84:87]\n"
"v_add_f32 v230, v230, v234\n"
"v_add_f32 v220, v220, v228\n"
"v_add_f32 v222, v222, v230\n"
"v_mfma_f32_16x16x32_bf16 v[80:83], v[164:167], v[176:179], v[80:83]\n"
"v_mul_f32 v220, 0x3ae38e39, v220\n"
"v_mul_f32 v222, 0x3ae38e39, v222\n"
"s_waitcnt lgkmcnt(6)\n"
"v_mfma_f32_16x16x32_bf16 v[96:99], v[168:171], v[176:179], v[96:99]\n"
"ds_read_b64_tr_b16 v[164:165], v213 offset:27776\n"
"ds_read_b64_tr_b16 v[166:167], v213 offset:30080\n"
"v_fma_f32 v222, -v220, v220, v222\n"
"v_mfma_f32_16x16x32_bf16 v[100:103], v[168:171], v[180:183], v[100:103]\n"
"v_max_f32 v222, 0, v222\n"
"v_cvt_pk_bf16_f32 v222, v222, v220\n"
"v_and_b32 v240, 0xffff0000, v222\n"
"v_mfma_f32_16x16x32_bf16 v[104:107], v[168:171], v[184:187], v[104:107]\n"
"v_lshlrev_b32 v222, 16, v222\n"
"v_xor_b32 v240, 0x80000000, v240\n"
"v_add_f32 v222, 0x3727c5ac, v222\n"
"v_mfma_f32_16x16x32_bf16 v[108:111], v[168:171], v[188:191], v[108:111]\n"
"v_cvt_pk_bf16_f32 v222, v222, v222\n"
"v_lshlrev_b32 v243, 16, v222\n"
"s_waitcnt lgkmcnt(4)\n"
"v_mfma_f32_16x16x32_bf16 v[124:127], v[172:175], v[188:191], v[124:127]\n"
"ds_read_b64_tr_b16 v[168:169], v213 offset:18592\n"
"ds_read_b64_tr_b16 v[170:171], v213 offset:20896\n"
"v_rsq_f32 v220, v243\n"
"v_mfma_f32_16x16x32_bf16 v[120:123], v[172:175], v[184:187], v[120:123]\n"
"v_mul_f32 v241, 0xc0135761, v220\n"
"v_mul_f32 v242, v220, v220\n"
"v_mul_f32 v242, v242, v241\n"
"v_mfma_f32_16x16x32_bf16 v[116:119], v[172:175], v[180:183], v[116:119]\n"
"v_mul_f32 v242, 0x3d372713, v242\n"
"s_lshl_b32 s54, s50, 10\n"
"s_add_u32 s54, s54, 141312\n"
"v_mfma_f32_16x16x32_bf16 v[112:115], v[172:175], v[176:179], v[112:115]\n"
"v_add_u32 v237, s54, v254\n"
"ds_write_b128 v237, v[240:243] offset:0\n"
"s_waitcnt vmcnt(4)\n"
"s_barrier\n"
"s_waitcnt lgkmcnt(5)\n"
"v_mfma_f32_16x16x32_bf16 v[0:3], v[160:163], v[192:195], v[0:3]\n"
"ds_read_b64_tr_b16 v[172:173], v213 offset:27808\n"
"v_mfma_f32_16x16x32_bf16 v[4:7], v[160:163], v[196:199], v[4:7]\n"
"ds_read_b64_tr_b16 v[174:175], v213 offset:30112\n"
"v_mfma_f32_16x16x32_bf16 v[8:11], v[160:163], v[200:203], v[8:11]\n"
"ds_read_b64_tr_b16 v[176:177], v208 offset:0\n"
"v_mfma_f32_16x16x32_bf16 v[12:15], v[160:163], v[204:207], v[12:15]\n"
"ds_read_b64_tr_b16 v[178:179], v208 offset:4096\n"
"s_waitcnt lgkmcnt(7)\n"
"v_mfma_f32_16x16x32_bf16 v[28:31], v[164:167], v[204:207], v[28:31]\n"
"ds_read_b64_tr_b16 v[160:161], v213 offset:18624\n"
"ds_read_b64_tr_b16 v[162:163], v213 offset:20928\n"
"v_mfma_f32_16x16x32_bf16 v[24:27], v[164:167], v[200:203], v[24:27]\n"
"ds_read_b64_tr_b16 v[180:181], v209 offset:0\n"
"ds_read_b64_tr_b16 v[182:183], v209 offset:4096\n"
"v_mfma_f32_16x16x32_bf16 v[20:23], v[164:167], v[196:199], v[20:23]\n"
"s_add_u32 s24, s56, 0xc00000\n"
"s_addc_u32 s25, s57, 0\n"
"s_and_b32 s25, s25, 0xffff\n"
"s_mov_b32 s26, 0x510000\n"
"s_mov_b32 s27, 0x20000\n"
"s_add_u32 s28, s56, 0x2500000\n"
"s_addc_u32 s29, s57, 0\n"
"s_and_b32 s29, s29, 0xffff\n"
"s_mov_b32 s30, 0x900000\n"
"s_mov_b32 s31, 0x20000\n"
"v_mfma_f32_16x16x32_bf16 v[16:19], v[164:167], v[192:195], v[16:19]\n"
"s_add_u32 s40, s68, 0x0\n"
"s_addc_u32 s41, s69, 0\n"
"s_and_b32 s41, s41, 0xffff\n"
"s_mov_b32 s42, 0x240000\n"
"s_mov_b32 s43, 0x20000\n"
"s_waitcnt lgkmcnt(9)\n"
"v_mfma_f32_16x16x32_bf16 v[32:35], v[168:171], v[192:195], v[32:35]\n"
"ds_read_b64_tr_b16 v[164:165], v213 offset:27840\n"
"ds_read_b64_tr_b16 v[166:167], v213 offset:30144\n"
"v_mfma_f32_16x16x32_bf16 v[36:39], v[168:171], v[196:199], v[36:39]\n"
"ds_read_b64_tr_b16 v[184:185], v210 offset:0\n"
"ds_read_b64_tr_b16 v[186:187], v210 offset:4096\n"
"v_mfma_f32_16x16x32_bf16 v[40:43], v[168:171], v[200:203], v[40:43]\n"
"s_bfe_u32 s70, s48, 0x30003\n"
"s_and_b32 s54, s48, 4\n"
"s_lshl_b32 s54, s54, 1\n"
"s_add_u32 s70, s70, s54\n"
"s_lshr_b32 s71, s48, 6\n"
"s_lshl_b32 s72, s70, 2\n"
"s_add_u32 s72, s72, s71\n"
"s_lshl_b32 s73, s70, 7\n"
"s_lshl_b32 s54, s50, 5\n"
"s_add_u32 s73, s73, s54\n"
"v_mfma_f32_16x16x32_bf16 v[44:47], v[168:171], v[204:207], v[44:47]\n"
"s_mul_i32 s75, s71, 144\n"
"s_and_b32 s54, s71, 1\n"
"s_lshl_b32 s55, s54, 4\n"
"s_add_u32 s74, s75, s55\n"
"s_xor_b32 s54, s54, 1\n"
"s_lshl_b32 s54, s54, 7\n"
"s_add_u32 s75, s75, s54\n"
"s_waitcnt lgkmcnt(10)\n"
"v_mfma_f32_16x16x32_bf16 v[60:63], v[172:175], v[204:207], v[60:63]\n"
"ds_read_b64_tr_b16 v[168:169], v213 offset:18656\n"
"ds_read_b64_tr_b16 v[170:171], v213 offset:20960\n"
"v_mfma_f32_16x16x32_bf16 v[56:59], v[172:175], v[200:203], v[56:59]\n"
"ds_read_b64_tr_b16 v[188:189], v211 offset:0\n"
"ds_read_b64_tr_b16 v[190:191], v211 offset:4096\n"
"v_mfma_f32_16x16x32_bf16 v[52:55], v[172:175], v[196:199], v[52:55]\n"
"s_and_b32 s55, s53, 1\n"
"s_lshl_b32 s55, s55, 5\n"
"s_add_u32 s74, s74, s55\n"
"s_add_u32 s74, s74, 64\n"
"s_lshr_b32 s54, s53, 1\n"
"s_lshl_b32 s54, s54, 4\n"
"s_add_u32 s76, s73, s54\n"
"s_lshl_b32 s54, s49, 10\n"
"s_add_u32 s77, s54, 149504\n"
"v_mfma_f32_16x16x32_bf16 v[48:51], v[172:175], v[192:195], v[48:51]\n"
"s_lshl_b32 s54, s70, 3\n"
"s_lshl_b32 s55, s50, 1\n"
"s_add_u32 s54, s54, s55\n"
"s_mul_i32 s63, s54, 147456\n"
"v_add_u32 v216, s63, v254\n"
"s_waitcnt lgkmcnt(10)\n"
"v_mfma_f32_16x16x32_bf16 v[64:67], v[160:163], v[192:195], v[64:67]\n"
"ds_read_b64_tr_b16 v[172:173], v213 offset:27872\n"
"ds_read_b64_tr_b16 v[174:175], v213 offset:30176\n"
"v_mfma_f32_16x16x32_bf16 v[68:71], v[160:163], v[196:199], v[68:71]\n"
"v_add_u32 v217, 0x23800, v216\n"
"v_mfma_f32_16x16x32_bf16 v[72:75], v[160:163], v[200:203], v[72:75]\n"
"s_mul_i32 s47, s53, 36864\n"
"s_lshl_b32 s44, s50, 12\n"
"v_mfma_f32_16x16x32_bf16 v[76:79], v[160:163], v[204:207], v[76:79]\n"
"v_add_u32 v214, s44, v254\n"
"s_waitcnt lgkmcnt(8)\n"
"v_mfma_f32_16x16x32_bf16 v[92:95], v[164:167], v[204:207], v[92:95]\n"
"ds_read_b64_tr_b16 v[160:161], v212 offset:128\n"
"ds_read_b64_tr_b16 v[162:163], v212 offset:2432\n"
"v_mfma_f32_16x16x32_bf16 v[88:91], v[164:167], v[200:203], v[88:91]\n"
"s_lshl_b32 s63, s50, 10\n"
"s_add_u32 s63, s63, 16384\n"
"v_mfma_f32_16x16x32_bf16 v[84:87], v[164:167], v[196:199], v[84:87]\n"
"v_add_u32 v215, s63, v254\n"
"s_lshl_b32 s63, s53, 2\n"
"s_add_u32 s63, s63, s71\n"
"s_mul_i32 s63, s63, 331776\n"
"v_mfma_f32_16x16x32_bf16 v[80:83], v[164:167], v[192:195], v[80:83]\n"
"v_lshrrev_b32 v244, 1, v219\n"
"s_waitcnt lgkmcnt(6)\n"
"v_mfma_f32_16x16x32_bf16 v[96:99], v[168:171], v[192:195], v[96:99]\n"
"ds_read_b64_tr_b16 v[164:165], v212 offset:9344\n"
"ds_read_b64_tr_b16 v[166:167], v212 offset:11648\n"
"v_mfma_f32_16x16x32_bf16 v[100:103], v[168:171], v[196:199], v[100:103]\n"
"v_lshlrev_b32 v244, 12, v244\n"
"v_mfma_f32_16x16x32_bf16 v[104:107], v[168:171], v[200:203], v[104:107]\n"
"v_and_b32 v245, 1, v219\n"
"v_mfma_f32_16x16x32_bf16 v[108:111], v[168:171], v[204:207], v[108:111]\n"
"v_lshl_add_u32 v244, v245, 4, v244\n"
"s_waitcnt lgkmcnt(4)\n"
"v_mfma_f32_16x16x32_bf16 v[124:127], v[172:175], v[204:207], v[124:127]\n"
"ds_read_b64_tr_b16 v[168:169], v212 offset:160\n"
"ds_read_b64_tr_b16 v[170:171], v212 offset:2464\n"
"v_mfma_f32_16x16x32_bf16 v[120:123], v[172:175], v[200:203], v[120:123]\n"
"v_lshrrev_b32 v245, 2, v219\n"
"v_mfma_f32_16x16x32_bf16 v[116:119], v[172:175], v[196:199], v[116:119]\n"
"v_lshlrev_b32 v245, 12, v245\n"
"v_and_b32 v246, 3, v219\n"
"v_mfma_f32_16x16x32_bf16 v[112:115], v[172:175], v[192:195], v[112:115]\n"
"v_lshl_add_u32 v245, v246, 4, v245\n"
"s_waitcnt vmcnt(0)\n"
"s_waitcnt lgkmcnt(0)\n"
"s_barrier\n"
"v_mfma_f32_16x16x32_bf16 v[0:3], v[160:163], v[176:179], v[0:3]\n"
"s_add_u32 s60, s63, 0\n"
"s_add_u32 m0, s61, 36864\n"
"s_nop 0\n"
"buffer_load_dwordx4 v214, s[24:27], s60 offen offset:0 sc1 lds\n"
"ds_read_b64_tr_b16 v[172:173], v212 offset:9376\n"
"v_mfma_f32_16x16x32_bf16 v[4:7], v[160:163], v[180:183], v[4:7]\n"
"ds_read_b64_tr_b16 v[174:175], v212 offset:11680\n"
"v_mfma_f32_16x16x32_bf16 v[8:11], v[160:163], v[184:187], v[8:11]\n"
"ds_read_b64_tr_b16 v[192:193], v208 offset:16384\n"
"v_mfma_f32_16x16x32_bf16 v[12:15], v[160:163], v[188:191], v[12:15]\n"
"ds_read_b64_tr_b16 v[194:195], v208 offset:20480\n"
"v_mfma_f32_16x16x32_bf16 v[28:31], v[164:167], v[188:191], v[28:31]\n"
"buffer_load_dwordx4 v214, s[24:27], s60 offen offset:1024 sc1 lds\n"
"ds_read_b64_tr_b16 v[160:161], v212 offset:192\n"
"v_mfma_f32_16x16x32_bf16 v[24:27], v[164:167], v[184:187], v[24:27]\n"
"ds_read_b64_tr_b16 v[162:163], v212 offset:2496\n"
"v_mfma_f32_16x16x32_bf16 v[20:23], v[164:167], v[180:183], v[20:23]\n"
"ds_read_b64_tr_b16 v[196:197], v209 offset:16384\n"
"v_mfma_f32_16x16x32_bf16 v[16:19], v[164:167], v[176:179], v[16:19]\n"
"ds_read_b64_tr_b16 v[198:199], v209 offset:20480\n"
"v_mfma_f32_16x16x32_bf16 v[32:35], v[168:171], v[176:179], v[32:35]\n"
"buffer_load_dwordx4 v214, s[24:27], s60 offen offset:2048 sc1 lds\n"
"ds_read_b64_tr_b16 v[164:165], v212 offset:9408\n"
"v_mfma_f32_16x16x32_bf16 v[36:39], v[168:171], v[180:183], v[36:39]\n"
"ds_read_b64_tr_b16 v[166:167], v212 offset:11712\n"
"v_mfma_f32_16x16x32_bf16 v[40:43], v[168:171], v[184:187], v[40:43]\n"
"ds_read_b64_tr_b16 v[200:201], v210 offset:16384\n"
"v_mfma_f32_16x16x32_bf16 v[44:47], v[168:171], v[188:191], v[44:47]\n"
"ds_read_b64_tr_b16 v[202:203], v210 offset:20480\n"
"s_waitcnt lgkmcnt(10)\n"
"v_mfma_f32_16x16x32_bf16 v[60:63], v[172:175], v[188:191], v[60:63]\n"
"buffer_load_dwordx4 v214, s[24:27], s60 offen offset:3072 sc1 lds\n"
"ds_read_b64_tr_b16 v[168:169], v212 offset:224\n"
"v_mfma_f32_16x16x32_bf16 v[56:59], v[172:175], v[184:187], v[56:59]\n"
"ds_read_b64_tr_b16 v[170:171], v212 offset:2528\n"
"v_mfma_f32_16x16x32_bf16 v[52:55], v[172:175], v[180:183], v[52:55]\n"
"ds_read_b64_tr_b16 v[204:205], v211 offset:16384\n"
"v_mfma_f32_16x16x32_bf16 v[48:51], v[172:175], v[176:179], v[48:51]\n"
"ds_read_b64_tr_b16 v[206:207], v211 offset:20480\n"
"s_waitcnt lgkmcnt(10)\n"
"v_mfma_f32_16x16x32_bf16 v[64:67], v[160:163], v[176:179], v[64:67]\n"
"s_add_u32 m0, s62, 36864\n"
"s_cmp_ge_u32 s50, 2\n"
"s_cbranch_scc1 .Lf_s19\n"
"buffer_load_dwordx4 v215, s[24:27], s60 offen offset:0 sc1 lds\n"
".Lf_s19:\n"
"v_mfma_f32_16x16x32_bf16 v[68:71], v[160:163], v[180:183], v[68:71]\n"
"ds_read_b64_tr_b16 v[172:173], v212 offset:9440\n"
"v_mfma_f32_16x16x32_bf16 v[72:75], v[160:163], v[184:187], v[72:75]\n"
"ds_read_b64_tr_b16 v[174:175], v212 offset:11744\n"
"v_mfma_f32_16x16x32_bf16 v[76:79], v[160:163], v[188:191], v[76:79]\n"
"s_waitcnt lgkmcnt(8)\n"
"v_mfma_f32_16x16x32_bf16 v[92:95], v[164:167], v[188:191], v[92:95]\n"
"ds_read_b64_tr_b16 v[160:161], v212 offset:18560\n"
"v_mfma_f32_16x16x32_bf16 v[88:91], v[164:167], v[184:187], v[88:91]\n"
"v_mfma_f32_16x16x32_bf16 v[84:87], v[164:167], v[180:183], v[84:87]\n"
"ds_read_b64_tr_b16 v[162:163], v212 offset:20864\n"
"v_mfma_f32_16x16x32_bf16 v[80:83], v[164:167], v[176:179], v[80:83]\n"
"s_waitcnt lgkmcnt(6)\n"
"v_mfma_f32_16x16x32_bf16 v[96:99], v[168:171], v[176:179], v[96:99]\n"
"ds_read_b64_tr_b16 v[164:165], v212 offset:27776\n"
"v_mfma_f32_16x16x32_bf16 v[100:103], v[168:171], v[180:183], v[100:103]\n"
"ds_read_b64_tr_b16 v[166:167], v212 offset:30080\n"
"v_mfma_f32_16x16x32_bf16 v[104:107], v[168:171], v[184:187], v[104:107]\n"
"s_lshl_b32 s54, s74, 12\n"
"s_lshl_b32 s55, s76, 1\n"
"s_add_u32 s60, s54, s55\n"
"s_mov_b32 m0, s77\n"
"s_nop 0\n"
"buffer_load_dwordx4 v244, s[20:23], s60 offen offset:0 sc1 lds\n"
"v_mfma_f32_16x16x32_bf16 v[108:111], v[168:171], v[188:191], v[108:111]\n"
"s_waitcnt lgkmcnt(4)\n"
"v_mfma_f32_16x16x32_bf16 v[124:127], v[172:175], v[188:191], v[124:127]\n"
"ds_read_b64_tr_b16 v[168:169], v212 offset:18592\n"
"v_mfma_f32_16x16x32_bf16 v[120:123], v[172:175], v[184:187], v[120:123]\n"
"ds_read_b64_tr_b16 v[170:171], v212 offset:20896\n"
"v_mfma_f32_16x16x32_bf16 v[116:119], v[172:175], v[180:183], v[116:119]\n"
"s_lshl_b32 s54, s50, 10\n"
"s_add_u32 s54, s54, 141312\n"
"v_and_b32 v238, 0xf0, v254\n"
"v_add_u32 v238, s54, v238\n"
"v_mfma_f32_16x16x32_bf16 v[112:115], v[172:175], v[176:179], v[112:115]\n"
"s_waitcnt lgkmcnt(4)\n"
"v_mfma_f32_16x16x32_bf16 v[0:3], v[160:163], v[192:195], v[0:3]\n"
"s_add_u32 s60, s63, 18432\n"
"s_add_u32 m0, s61, 55296\n"
"s_nop 0\n"
"buffer_load_dwordx4 v214, s[24:27], s60 offen offset:0 sc1 lds\n"
"v_mfma_f32_16x16x32_bf16 v[4:7], v[160:163], v[196:199], v[4:7]\n"
"ds_read_b64_tr_b16 v[172:173], v212 offset:27808\n"
"v_mfma_f32_16x16x32_bf16 v[8:11], v[160:163], v[200:203], v[8:11]\n"
"ds_read_b64_tr_b16 v[174:175], v212 offset:30112\n"
"v_mfma_f32_16x16x32_bf16 v[12:15], v[160:163], v[204:207], v[12:15]\n"
"s_waitcnt lgkmcnt(4)\n"
"v_mfma_f32_16x16x32_bf16 v[28:31], v[164:167], v[204:207], v[28:31]\n"
"buffer_load_dwordx4 v214, s[24:27], s60 offen offset:1024 sc1 lds\n"
"v_mfma_f32_16x16x32_bf16 v[24:27], v[164:167], v[200:203], v[24:27]\n"
"ds_read_b64_tr_b16 v[160:161], v212 offset:18624\n"
"v_mfma_f32_16x16x32_bf16 v[20:23], v[164:167], v[196:199], v[20:23]\n"
"ds_read_b64_tr_b16 v[162:163], v212 offset:20928\n"
"v_mfma_f32_16x16x32_bf16 v[16:19], v[164:167], v[192:195], v[16:19]\n"
"s_waitcnt lgkmcnt(4)\n"
"v_mfma_f32_16x16x32_bf16 v[32:35], v[168:171], v[192:195], v[32:35]\n"
"buffer_load_dwordx4 v214, s[24:27], s60 offen offset:2048 sc1 lds\n"
"v_mfma_f32_16x16x32_bf16 v[36:39], v[168:171], v[196:199], v[36:39]\n"
"ds_read_b64_tr_b16 v[164:165], v212 offset:27840\n"
"v_mfma_f32_16x16x32_bf16 v[40:43], v[168:171], v[200:203], v[40:43]\n"
"ds_read_b64_tr_b16 v[166:167], v212 offset:30144\n"
"v_mfma_f32_16x16x32_bf16 v[44:47], v[168:171], v[204:207], v[44:47]\n"
"s_waitcnt lgkmcnt(4)\n"
"v_mfma_f32_16x16x32_bf16 v[60:63], v[172:175], v[204:207], v[60:63]\n"
"buffer_load_dwordx4 v214, s[24:27], s60 offen offset:3072 sc1 lds\n"
"v_mfma_f32_16x16x32_bf16 v[56:59], v[172:175], v[200:203], v[56:59]\n"
"ds_read_b64_tr_b16 v[168:169], v212 offset:18656\n"
"v_mfma_f32_16x16x32_bf16 v[52:55], v[172:175], v[196:199], v[52:55]\n"
"ds_read_b64_tr_b16 v[170:171], v212 offset:20960\n"
"v_mfma_f32_16x16x32_bf16 v[48:51], v[172:175], v[192:195], v[48:51]\n"
"s_waitcnt lgkmcnt(4)\n"
"v_mfma_f32_16x16x32_bf16 v[64:67], v[160:163], v[192:195], v[64:67]\n"
"s_add_u32 m0, s62, 55296\n"
"s_cmp_ge_u32 s50, 2\n"
"s_cbranch_scc1 .Lf_s20\n"
"buffer_load_dwordx4 v215, s[24:27], s60 offen offset:0 sc1 lds\n"
".Lf_s20:\n"
"v_mfma_f32_16x16x32_bf16 v[68:71], v[160:163], v[196:199], v[68:71]\n"
"ds_read_b64_tr_b16 v[172:173], v212 offset:27872\n"
"v_mfma_f32_16x16x32_bf16 v[72:75], v[160:163], v[200:203], v[72:75]\n"
"ds_read_b64_tr_b16 v[174:175], v212 offset:30176\n"
"v_mfma_f32_16x16x32_bf16 v[76:79], v[160:163], v[204:207], v[76:79]\n"
"s_waitcnt lgkmcnt(4)\n"
"v_mfma_f32_16x16x32_bf16 v[92:95], v[164:167], v[204:207], v[92:95]\n"
"v_mfma_f32_16x16x32_bf16 v[88:91], v[164:167], v[200:203], v[88:91]\n"
"v_mfma_f32_16x16x32_bf16 v[84:87], v[164:167], v[196:199], v[84:87]\n"
"v_mfma_f32_16x16x32_bf16 v[80:83], v[164:167], v[192:195], v[80:83]\n"
"s_waitcnt lgkmcnt(2)\n"
"v_mfma_f32_16x16x32_bf16 v[96:99], v[168:171], v[192:195], v[96:99]\n"
"v_mfma_f32_16x16x32_bf16 v[100:103], v[168:171], v[196:199], v[100:103]\n"
"v_mfma_f32_16x16x32_bf16 v[104:107], v[168:171], v[200:203], v[104:107]\n"
"v_mfma_f32_16x16x32_bf16 v[108:111], v[168:171], v[204:207], v[108:111]\n"
"s_waitcnt lgkmcnt(0)\n"
"v_mfma_f32_16x16x32_bf16 v[124:127], v[172:175], v[204:207], v[124:127]\n"
"v_mfma_f32_16x16x32_bf16 v[120:123], v[172:175], v[200:203], v[120:123]\n"
"v_mfma_f32_16x16x32_bf16 v[116:119], v[172:175], v[196:199], v[116:119]\n"
"v_mfma_f32_16x16x32_bf16 v[112:115], v[172:175], v[192:195], v[112:115]\n"
"ds_read_b128 v[160:163], v238 offset:0\n"
"ds_read_b128 v[164:167], v238 offset:256\n"
"ds_read_b128 v[168:171], v238 offset:512\n"
"ds_read_b128 v[172:175], v238 offset:768\n"
"v_and_b32 v221, 48, v219\n"
"v_add_u32 v221, 0x22000, v221\n"
"s_lshl_b32 s54, s51, 4\n"
"s_lshl_b32 s55, s50, 2\n"
"s_add_u32 s54, s54, s55\n"
"s_mul_i32 s78, s54, 147456\n"
"s_mul_i32 s54, s53, 36864\n"
"s_add_u32 s78, s78, s54\n"
"s_and_b32 s55, s52, 7\n"
"s_lshr_b32 s54, s55, 1\n"
"s_lshl_b32 s54, s54, 10\n"
"s_add_u32 s79, s78, s54\n"
"s_add_u32 s79, s79, 32768\n"
"s_lshl_b32 s54, s55, 11\n"
"s_add_u32 s78, s78, s54\n"
"s_and_b32 s54, s52, 1\n"
"s_lshl_b32 s54, s54, 9\n"
"v_lshlrev_b32 v223, 3, v219\n"
"v_add_u32 v223, s54, v223\n"
"s_lshl_b32 s54, s51, 2\n"
"s_add_u32 s54, s54, s50\n"
"s_lshl_b32 s54, s54, 2\n"
"s_add_u32 s54, s54, s53\n"
"s_mul_i32 s98, s54, 192\n"
"s_add_u32 s98, s98, 262144\n"
"v_mov_b32 v224, 1\n"
"v_lshlrev_b32 v225, 6, v219\n"
"s_lshl_b32 s54, s70, 1\n"
"s_lshr_b32 s55, s50, 1\n"
"s_add_u32 s54, s54, s55\n"
"s_lshl_b32 s54, s54, 2\n"
"s_add_u32 s54, s54, s53\n"
"s_mul_i32 s99, s54, 192\n"
"s_add_u32 s99, s99, 262144\n"
"v_mov_b32 v226, 0\n"
"s_barrier\n"
"s_add_u32 s60, s63, 36864\n"
"s_add_u32 m0, s61, 0\n"
"s_nop 0\n"
"buffer_load_dwordx4 v214, s[24:27], s60 offen offset:0 sc1 lds\n"
"buffer_load_dwordx4 v214, s[24:27], s60 offen offset:1024 sc1 lds\n"
"buffer_load_dwordx4 v214, s[24:27], s60 offen offset:2048 sc1 lds\n"
"buffer_load_dwordx4 v214, s[24:27], s60 offen offset:3072 sc1 lds\n"
"s_add_u32 m0, s62, 0\n"
"s_cmp_ge_u32 s50, 2\n"
"s_cbranch_scc1 .Lf_s21\n"
"buffer_load_dwordx4 v215, s[24:27], s60 offen offset:0 sc1 lds\n"
".Lf_s21:\n"
"s_add_u32 s60, s63, 55296\n"
"s_add_u32 m0, s61, 18432\n"
"s_nop 0\n"
"buffer_load_dwordx4 v214, s[24:27], s60 offen offset:0 sc1 lds\n"
"buffer_load_dwordx4 v214, s[24:27], s60 offen offset:1024 sc1 lds\n"
"buffer_load_dwordx4 v214, s[24:27], s60 offen offset:2048 sc1 lds\n"
"buffer_load_dwordx4 v214, s[24:27], s60 offen offset:3072 sc1 lds\n"
"s_add_u32 m0, s62, 18432\n"
"s_cmp_ge_u32 s50, 2\n"
"s_cbranch_scc1 .Lf_s22\n"
"buffer_load_dwordx4 v215, s[24:27], s60 offen offset:0 sc1 lds\n"
".Lf_s22:\n"
"s_setprio 2\n"
"ds_read_b128 v[176:179], v221 offset:256\n"
"ds_read_b128 v[180:183], v221 offset:1280\n"
"ds_read_b128 v[184:187], v221 offset:320\n"
"ds_read_b128 v[188:191], v221 offset:1344\n"
"s_waitcnt lgkmcnt(3)\n"
"v_pk_fma_f32 v[0:1], v[176:177], v[160:161], v[0:1] op_sel_hi:[1,0,1]\n"
"v_pk_fma_f32 v[2:3], v[178:179], v[160:161], v[2:3] op_sel_hi:[1,0,1]\n"
"s_waitcnt lgkmcnt(1)\n"
"v_pk_fma_f32 v[32:33], v[184:185], v[160:161], v[32:33] op_sel_hi:[1,0,1]\n"
"v_pk_fma_f32 v[34:35], v[186:187], v[160:161], v[34:35] op_sel_hi:[1,0,1]\n"
"v_pk_mul_f32 v[200:201], v[0:1], v[0:1]\n"
"v_pk_mul_f32 v[202:203], v[2:3], v[2:3]\n"
"v_pk_mul_f32 v[204:205], v[32:33], v[32:33]\n"
"v_pk_mul_f32 v[206:207], v[34:35], v[34:35]\n"
"v_pk_fma_f32 v[200:201], v[200:201], v[162:163], v[160:161] op_sel:[0,0,1] op_sel_hi:[1,0,1]\n"
"v_pk_fma_f32 v[202:203], v[202:203], v[162:163], v[160:161] op_sel:[0,0,1] op_sel_hi:[1,0,1]\n"
"v_pk_fma_f32 v[204:205], v[204:205], v[162:163], v[160:161] op_sel:[0,0,1] op_sel_hi:[1,0,1]\n"
"v_pk_fma_f32 v[206:207], v[206:207], v[162:163], v[160:161] op_sel:[0,0,1] op_sel_hi:[1,0,1]\n"
"v_pk_mul_f32 v[200:201], v[0:1], v[200:201]\n"
"v_pk_mul_f32 v[202:203], v[2:3], v[202:203]\n"
"v_pk_mul_f32 v[204:205], v[32:33], v[204:205]\n"
"v_pk_mul_f32 v[206:207], v[34:35], v[206:207]\n"
"v_exp_f32 v200, v200\n"
"v_exp_f32 v201, v201\n"
"v_pk_fma_f32 v[16:17], v[180:181], v[160:161], v[16:17] op_sel_hi:[1,0,1]\n"
"v_exp_f32 v202, v202\n"
"v_exp_f32 v203, v203\n"
"v_pk_fma_f32 v[18:19], v[182:183], v[160:161], v[18:19] op_sel_hi:[1,0,1]\n"
"v_exp_f32 v204, v204\n"
"v_exp_f32 v205, v205\n"
"s_waitcnt lgkmcnt(0)\n"
"v_pk_fma_f32 v[48:49], v[188:189], v[160:161], v[48:49] op_sel_hi:[1,0,1]\n"
"v_exp_f32 v206, v206\n"
"v_exp_f32 v207, v207\n"
"v_pk_fma_f32 v[50:51], v[190:191], v[160:161], v[50:51] op_sel_hi:[1,0,1]\n"
"v_pk_fma_f32 v[200:201], v[162:163], v[200:201], v[162:163] op_sel:[1,0,1] op_sel_hi:[1,1,1]\n"
"v_pk_fma_f32 v[202:203], v[162:163], v[202:203], v[162:163] op_sel:[1,0,1] op_sel_hi:[1,1,1]\n"
"v_pk_fma_f32 v[204:205], v[162:163], v[204:205], v[162:163] op_sel:[1,0,1] op_sel_hi:[1,1,1]\n"
"v_pk_fma_f32 v[206:207], v[162:163], v[206:207], v[162:163] op_sel:[1,0,1] op_sel_hi:[1,1,1]\n"
"v_rcp_f32 v200, v200\n"
"v_rcp_f32 v201, v201\n"
"v_pk_mul_f32 v[16:17], v[0:1], v[16:17]\n"
"v_rcp_f32 v202, v202\n"
"v_rcp_f32 v203, v203\n"
"v_pk_mul_f32 v[18:19], v[2:3], v[18:19]\n"
"v_rcp_f32 v204, v204\n"
"v_rcp_f32 v205, v205\n"
"v_pk_mul_f32 v[48:49], v[32:33], v[48:49]\n"
"v_rcp_f32 v206, v206\n"
"v_rcp_f32 v207, v207\n"
"v_pk_mul_f32 v[50:51], v[34:35], v[50:51]\n"
"v_pk_mul_f32 v[16:17], v[16:17], v[200:201]\n"
"v_pk_mul_f32 v[18:19], v[18:19], v[202:203]\n"
"v_pk_mul_f32 v[48:49], v[48:49], v[204:205]\n"
"v_pk_mul_f32 v[50:51], v[50:51], v[206:207]\n"
"v_cvt_pk_bf16_f32 v192, v16, v17\n"
"v_cvt_pk_bf16_f32 v193, v18, v19\n"
"v_cvt_pk_bf16_f32 v194, v48, v49\n"
"v_cvt_pk_bf16_f32 v195, v50, v51\n"
"s_add_u32 s60, s78, 1024\n"
"buffer_store_dwordx4 v[192:195], v254, s[32:35], s60 offen offset:0 sc1\n"
"v_pk_fma_f32 v[4:5], v[176:177], v[164:165], v[4:5] op_sel_hi:[1,0,1]\n"
"v_pk_fma_f32 v[6:7], v[178:179], v[164:165], v[6:7] op_sel_hi:[1,0,1]\n"
"v_pk_fma_f32 v[36:37], v[184:185], v[164:165], v[36:37] op_sel_hi:[1,0,1]\n"
"v_pk_fma_f32 v[38:39], v[186:187], v[164:165], v[38:39] op_sel_hi:[1,0,1]\n"
"v_pk_mul_f32 v[200:201], v[4:5], v[4:5]\n"
"v_pk_mul_f32 v[202:203], v[6:7], v[6:7]\n"
"v_pk_mul_f32 v[204:205], v[36:37], v[36:37]\n"
"v_pk_mul_f32 v[206:207], v[38:39], v[38:39]\n"
"v_pk_fma_f32 v[200:201], v[200:201], v[166:167], v[164:165] op_sel:[0,0,1] op_sel_hi:[1,0,1]\n"
"v_pk_fma_f32 v[202:203], v[202:203], v[166:167], v[164:165] op_sel:[0,0,1] op_sel_hi:[1,0,1]\n"
"v_pk_fma_f32 v[204:205], v[204:205], v[166:167], v[164:165] op_sel:[0,0,1] op_sel_hi:[1,0,1]\n"
"v_pk_fma_f32 v[206:207], v[206:207], v[166:167], v[164:165] op_sel:[0,0,1] op_sel_hi:[1,0,1]\n"
"v_pk_mul_f32 v[200:201], v[4:5], v[200:201]\n"
"v_pk_mul_f32 v[202:203], v[6:7], v[202:203]\n"
"v_pk_mul_f32 v[204:205], v[36:37], v[204:205]\n"
"v_pk_mul_f32 v[206:207], v[38:39], v[206:207]\n"
"v_exp_f32 v200, v200\n"
"v_exp_f32 v201, v201\n"
"v_pk_fma_f32 v[20:21], v[180:181], v[164:165], v[20:21] op_sel_hi:[1,0,1]\n"
"v_exp_f32 v202, v202\n"
"v_exp_f32 v203, v203\n"
"v_pk_fma_f32 v[22:23], v[182:183], v[164:165], v[22:23] op_sel_hi:[1,0,1]\n"
"v_exp_f32 v204, v204\n"
"v_exp_f32 v205, v205\n"
"v_pk_fma_f32 v[52:53], v[188:189], v[164:165], v[52:53] op_sel_hi:[1,0,1]\n"
"v_exp_f32 v206, v206\n"
"v_exp_f32 v207, v207\n"
"v_pk_fma_f32 v[54:55], v[190:191], v[164:165], v[54:55] op_sel_hi:[1,0,1]\n"
"v_pk_fma_f32 v[200:201], v[166:167], v[200:201], v[166:167] op_sel:[1,0,1] op_sel_hi:[1,1,1]\n"
"v_pk_fma_f32 v[202:203], v[166:167], v[202:203], v[166:167] op_sel:[1,0,1] op_sel_hi:[1,1,1]\n"
"v_pk_fma_f32 v[204:205], v[166:167], v[204:205], v[166:167] op_sel:[1,0,1] op_sel_hi:[1,1,1]\n"
"v_pk_fma_f32 v[206:207], v[166:167], v[206:207], v[166:167] op_sel:[1,0,1] op_sel_hi:[1,1,1]\n"
"v_rcp_f32 v200, v200\n"
"v_rcp_f32 v201, v201\n"
"v_pk_mul_f32 v[20:21], v[4:5], v[20:21]\n"
"v_rcp_f32 v202, v202\n"
"v_rcp_f32 v203, v203\n"
"v_pk_mul_f32 v[22:23], v[6:7], v[22:23]\n"
"v_rcp_f32 v204, v204\n"
"v_rcp_f32 v205, v205\n"
"v_pk_mul_f32 v[52:53], v[36:37], v[52:53]\n"
"v_rcp_f32 v206, v206\n"
"v_rcp_f32 v207, v207\n"
"v_pk_mul_f32 v[54:55], v[38:39], v[54:55]\n"
"v_pk_mul_f32 v[20:21], v[20:21], v[200:201]\n"
"v_pk_mul_f32 v[22:23], v[22:23], v[202:203]\n"
"v_pk_mul_f32 v[52:53], v[52:53], v[204:205]\n"
"v_pk_mul_f32 v[54:55], v[54:55], v[206:207]\n"
"v_cvt_pk_bf16_f32 v196, v20, v21\n"
"v_cvt_pk_bf16_f32 v197, v22, v23\n"
"v_cvt_pk_bf16_f32 v198, v52, v53\n"
"v_cvt_pk_bf16_f32 v199, v54, v55\n"
"s_add_u32 s60, s78, 148480\n"
"buffer_store_dwordx4 v[196:199], v254, s[32:35], s60 offen offset:0 sc1\n"
"v_pk_fma_f32 v[8:9], v[176:177], v[168:169], v[8:9] op_sel_hi:[1,0,1]\n"
"v_pk_fma_f32 v[10:11], v[178:179], v[168:169], v[10:11] op_sel_hi:[1,0,1]\n"
"v_pk_fma_f32 v[40:41], v[184:185], v[168:169], v[40:41] op_sel_hi:[1,0,1]\n"
"v_pk_fma_f32 v[42:43], v[186:187], v[168:169], v[42:43] op_sel_hi:[1,0,1]\n"
"v_pk_mul_f32 v[200:201], v[8:9], v[8:9]\n"
"v_pk_mul_f32 v[202:203], v[10:11], v[10:11]\n"
"v_pk_mul_f32 v[204:205], v[40:41], v[40:41]\n"
"v_pk_mul_f32 v[206:207], v[42:43], v[42:43]\n"
"v_pk_fma_f32 v[200:201], v[200:201], v[170:171], v[168:169] op_sel:[0,0,1] op_sel_hi:[1,0,1]\n"
"v_pk_fma_f32 v[202:203], v[202:203], v[170:171], v[168:169] op_sel:[0,0,1] op_sel_hi:[1,0,1]\n"
"v_pk_fma_f32 v[204:205], v[204:205], v[170:171], v[168:169] op_sel:[0,0,1] op_sel_hi:[1,0,1]\n"
"v_pk_fma_f32 v[206:207], v[206:207], v[170:171], v[168:169] op_sel:[0,0,1] op_sel_hi:[1,0,1]\n"
"v_pk_mul_f32 v[200:201], v[8:9], v[200:201]\n"
"v_pk_mul_f32 v[202:203], v[10:11], v[202:203]\n"
"v_pk_mul_f32 v[204:205], v[40:41], v[204:205]\n"
"v_pk_mul_f32 v[206:207], v[42:43], v[206:207]\n"
"v_exp_f32 v200, v200\n"
"v_exp_f32 v201, v201\n"
"v_pk_fma_f32 v[24:25], v[180:181], v[168:169], v[24:25] op_sel_hi:[1,0,1]\n"
"v_exp_f32 v202, v202\n"
"v_exp_f32 v203, v203\n"
"v_pk_fma_f32 v[26:27], v[182:183], v[168:169], v[26:27] op_sel_hi:[1,0,1]\n"
"v_exp_f32 v204, v204\n"
"v_exp_f32 v205, v205\n"
"v_pk_fma_f32 v[56:57], v[188:189], v[168:169], v[56:57] op_sel_hi:[1,0,1]\n"
"v_exp_f32 v206, v206\n"
"v_exp_f32 v207, v207\n"
"v_pk_fma_f32 v[58:59], v[190:191], v[168:169], v[58:59] op_sel_hi:[1,0,1]\n"
"v_pk_fma_f32 v[200:201], v[170:171], v[200:201], v[170:171] op_sel:[1,0,1] op_sel_hi:[1,1,1]\n"
"v_pk_fma_f32 v[202:203], v[170:171], v[202:203], v[170:171] op_sel:[1,0,1] op_sel_hi:[1,1,1]\n"
"v_pk_fma_f32 v[204:205], v[170:171], v[204:205], v[170:171] op_sel:[1,0,1] op_sel_hi:[1,1,1]\n"
"v_pk_fma_f32 v[206:207], v[170:171], v[206:207], v[170:171] op_sel:[1,0,1] op_sel_hi:[1,1,1]\n"
"v_rcp_f32 v200, v200\n"
"v_rcp_f32 v201, v201\n"
"v_pk_mul_f32 v[24:25], v[8:9], v[24:25]\n"
"v_rcp_f32 v202, v202\n"
"v_rcp_f32 v203, v203\n"
"v_pk_mul_f32 v[26:27], v[10:11], v[26:27]\n"
"v_rcp_f32 v204, v204\n"
"v_rcp_f32 v205, v205\n"
"v_pk_mul_f32 v[56:57], v[40:41], v[56:57]\n"
"v_rcp_f32 v206, v206\n"
"v_rcp_f32 v207, v207\n"
"v_pk_mul_f32 v[58:59], v[42:43], v[58:59]\n"
"v_pk_mul_f32 v[24:25], v[24:25], v[200:201]\n"
"v_pk_mul_f32 v[26:27], v[26:27], v[202:203]\n"
"v_pk_mul_f32 v[56:57], v[56:57], v[204:205]\n"
"v_pk_mul_f32 v[58:59], v[58:59], v[206:207]\n"
"v_cvt_pk_bf16_f32 v192, v24, v25\n"
"v_cvt_pk_bf16_f32 v193, v26, v27\n"
"v_cvt_pk_bf16_f32 v194, v56, v57\n"
"v_cvt_pk_bf16_f32 v195, v58, v59\n"
"s_add_u32 s60, s78, 295936\n"
"buffer_store_dwordx4 v[192:195], v254, s[32:35], s60 offen offset:0 sc1\n"
"v_pk_fma_f32 v[12:13], v[176:177], v[172:173], v[12:13] op_sel_hi:[1,0,1]\n"
"v_pk_fma_f32 v[14:15], v[178:179], v[172:173], v[14:15] op_sel_hi:[1,0,1]\n"
"v_pk_fma_f32 v[44:45], v[184:185], v[172:173], v[44:45] op_sel_hi:[1,0,1]\n"
"v_pk_fma_f32 v[46:47], v[186:187], v[172:173], v[46:47] op_sel_hi:[1,0,1]\n"
"v_pk_mul_f32 v[200:201], v[12:13], v[12:13]\n"
"v_pk_mul_f32 v[202:203], v[14:15], v[14:15]\n"
"v_pk_mul_f32 v[204:205], v[44:45], v[44:45]\n"
"v_pk_mul_f32 v[206:207], v[46:47], v[46:47]\n"
"v_pk_fma_f32 v[200:201], v[200:201], v[174:175], v[172:173] op_sel:[0,0,1] op_sel_hi:[1,0,1]\n"
"v_pk_fma_f32 v[202:203], v[202:203], v[174:175], v[172:173] op_sel:[0,0,1] op_sel_hi:[1,0,1]\n"
"v_pk_fma_f32 v[204:205], v[204:205], v[174:175], v[172:173] op_sel:[0,0,1] op_sel_hi:[1,0,1]\n"
"v_pk_fma_f32 v[206:207], v[206:207], v[174:175], v[172:173] op_sel:[0,0,1] op_sel_hi:[1,0,1]\n"
"v_pk_mul_f32 v[200:201], v[12:13], v[200:201]\n"
"v_pk_mul_f32 v[202:203], v[14:15], v[202:203]\n"
"v_pk_mul_f32 v[204:205], v[44:45], v[204:205]\n"
"v_pk_mul_f32 v[206:207], v[46:47], v[206:207]\n"
"v_exp_f32 v200, v200\n"
"v_exp_f32 v201, v201\n"
"v_pk_fma_f32 v[28:29], v[180:181], v[172:173], v[28:29] op_sel_hi:[1,0,1]\n"
"v_exp_f32 v202, v202\n"
"v_exp_f32 v203, v203\n"
"v_pk_fma_f32 v[30:31], v[182:183], v[172:173], v[30:31] op_sel_hi:[1,0,1]\n"
"v_exp_f32 v204, v204\n"
"v_exp_f32 v205, v205\n"
"v_pk_fma_f32 v[60:61], v[188:189], v[172:173], v[60:61] op_sel_hi:[1,0,1]\n"
"v_exp_f32 v206, v206\n"
"v_exp_f32 v207, v207\n"
"v_pk_fma_f32 v[62:63], v[190:191], v[172:173], v[62:63] op_sel_hi:[1,0,1]\n"
"v_pk_fma_f32 v[200:201], v[174:175], v[200:201], v[174:175] op_sel:[1,0,1] op_sel_hi:[1,1,1]\n"
"v_pk_fma_f32 v[202:203], v[174:175], v[202:203], v[174:175] op_sel:[1,0,1] op_sel_hi:[1,1,1]\n"
"v_pk_fma_f32 v[204:205], v[174:175], v[204:205], v[174:175] op_sel:[1,0,1] op_sel_hi:[1,1,1]\n"
"v_pk_fma_f32 v[206:207], v[174:175], v[206:207], v[174:175] op_sel:[1,0,1] op_sel_hi:[1,1,1]\n"
"v_rcp_f32 v200, v200\n"
"v_rcp_f32 v201, v201\n"
"v_pk_mul_f32 v[28:29], v[12:13], v[28:29]\n"
"v_rcp_f32 v202, v202\n"
"v_rcp_f32 v203, v203\n"
"v_pk_mul_f32 v[30:31], v[14:15], v[30:31]\n"
"v_rcp_f32 v204, v204\n"
"v_rcp_f32 v205, v205\n"
"v_pk_mul_f32 v[60:61], v[44:45], v[60:61]\n"
"v_rcp_f32 v206, v206\n"
"v_rcp_f32 v207, v207\n"
"v_pk_mul_f32 v[62:63], v[46:47], v[62:63]\n"
"v_pk_mul_f32 v[28:29], v[28:29], v[200:201]\n"
"v_pk_mul_f32 v[30:31], v[30:31], v[202:203]\n"
"v_pk_mul_f32 v[60:61], v[60:61], v[204:205]\n"
"v_pk_mul_f32 v[62:63], v[62:63], v[206:207]\n"
"v_cvt_pk_bf16_f32 v196, v28, v29\n"
"v_cvt_pk_bf16_f32 v197, v30, v31\n"
"v_cvt_pk_bf16_f32 v198, v60, v61\n"
"v_cvt_pk_bf16_f32 v199, v62, v63\n"
"s_add_u32 s60, s78, 443392\n"
"buffer_store_dwordx4 v[196:199], v254, s[32:35], s60 offen offset:0 sc1\n"
"s_waitcnt vmcnt(0)\n"
"s_mov_b64 exec, 1\n"
"buffer_atomic_add v224, v225, s[64:67], s98 offen offset:0\n"
"s_mov_b64 exec, -1\n"
"s_setprio 1\n"
"ds_read_b128 v[176:179], v221 offset:384\n"
"ds_read_b128 v[180:183], v221 offset:1408\n"
"ds_read_b128 v[184:187], v221 offset:448\n"
"ds_read_b128 v[188:191], v221 offset:1472\n"
"s_waitcnt lgkmcnt(3)\n"
"v_pk_fma_f32 v[64:65], v[176:177], v[160:161], v[64:65] op_sel_hi:[1,0,1]\n"
"v_pk_fma_f32 v[66:67], v[178:179], v[160:161], v[66:67] op_sel_hi:[1,0,1]\n"
"s_waitcnt lgkmcnt(1)\n"
"v_pk_fma_f32 v[96:97], v[184:185], v[160:161], v[96:97] op_sel_hi:[1,0,1]\n"
"v_pk_fma_f32 v[98:99], v[186:187], v[160:161], v[98:99] op_sel_hi:[1,0,1]\n"
"v_pk_mul_f32 v[200:201], v[64:65], v[64:65]\n"
"v_pk_mul_f32 v[202:203], v[66:67], v[66:67]\n"
"v_pk_mul_f32 v[204:205], v[96:97], v[96:97]\n"
"v_pk_mul_f32 v[206:207], v[98:99], v[98:99]\n"
"v_pk_fma_f32 v[200:201], v[200:201], v[162:163], v[160:161] op_sel:[0,0,1] op_sel_hi:[1,0,1]\n"
"v_pk_fma_f32 v[202:203], v[202:203], v[162:163], v[160:161] op_sel:[0,0,1] op_sel_hi:[1,0,1]\n"
"v_pk_fma_f32 v[204:205], v[204:205], v[162:163], v[160:161] op_sel:[0,0,1] op_sel_hi:[1,0,1]\n"
"v_pk_fma_f32 v[206:207], v[206:207], v[162:163], v[160:161] op_sel:[0,0,1] op_sel_hi:[1,0,1]\n"
"v_pk_mul_f32 v[200:201], v[64:65], v[200:201]\n"
"v_pk_mul_f32 v[202:203], v[66:67], v[202:203]\n"
"v_pk_mul_f32 v[204:205], v[96:97], v[204:205]\n"
"v_pk_mul_f32 v[206:207], v[98:99], v[206:207]\n"
"v_exp_f32 v200, v200\n"
"v_exp_f32 v201, v201\n"
"v_pk_fma_f32 v[80:81], v[180:181], v[160:161], v[80:81] op_sel_hi:[1,0,1]\n"
"v_exp_f32 v202, v202\n"
"v_exp_f32 v203, v203\n"
"v_pk_fma_f32 v[82:83], v[182:183], v[160:161], v[82:83] op_sel_hi:[1,0,1]\n"
"v_exp_f32 v204, v204\n"
"v_exp_f32 v205, v205\n"
"s_waitcnt lgkmcnt(0)\n"
"v_pk_fma_f32 v[112:113], v[188:189], v[160:161], v[112:113] op_sel_hi:[1,0,1]\n"
"v_exp_f32 v206, v206\n"
"v_exp_f32 v207, v207\n"
"v_pk_fma_f32 v[114:115], v[190:191], v[160:161], v[114:115] op_sel_hi:[1,0,1]\n"
"v_pk_fma_f32 v[200:201], v[162:163], v[200:201], v[162:163] op_sel:[1,0,1] op_sel_hi:[1,1,1]\n"
"v_pk_fma_f32 v[202:203], v[162:163], v[202:203], v[162:163] op_sel:[1,0,1] op_sel_hi:[1,1,1]\n"
"v_pk_fma_f32 v[204:205], v[162:163], v[204:205], v[162:163] op_sel:[1,0,1] op_sel_hi:[1,1,1]\n"
"v_pk_fma_f32 v[206:207], v[162:163], v[206:207], v[162:163] op_sel:[1,0,1] op_sel_hi:[1,1,1]\n"
"v_rcp_f32 v200, v200\n"
"v_rcp_f32 v201, v201\n"
"v_pk_mul_f32 v[80:81], v[64:65], v[80:81]\n"
"v_rcp_f32 v202, v202\n"
"v_rcp_f32 v203, v203\n"
"v_pk_mul_f32 v[82:83], v[66:67], v[82:83]\n"
"v_rcp_f32 v204, v204\n"
"v_rcp_f32 v205, v205\n"
"v_pk_mul_f32 v[112:113], v[96:97], v[112:113]\n"
"v_rcp_f32 v206, v206\n"
"v_rcp_f32 v207, v207\n"
"v_pk_mul_f32 v[114:115], v[98:99], v[114:115]\n"
"v_pk_mul_f32 v[80:81], v[80:81], v[200:201]\n"
"v_pk_mul_f32 v[82:83], v[82:83], v[202:203]\n"
"v_pk_mul_f32 v[112:113], v[112:113], v[204:205]\n"
"v_pk_mul_f32 v[114:115], v[114:115], v[206:207]\n"
"v_cvt_pk_bf16_f32 v192, v80, v81\n"
"v_cvt_pk_bf16_f32 v193, v82, v83\n"
"v_cvt_pk_bf16_f32 v194, v112, v113\n"
"v_cvt_pk_bf16_f32 v195, v114, v115\n"
"s_add_u32 s60, s78, 17408\n"
"buffer_store_dwordx4 v[192:195], v254, s[32:35], s60 offen offset:0 sc1\n"
"v_pk_fma_f32 v[68:69], v[176:177], v[164:165], v[68:69] op_sel_hi:[1,0,1]\n"
"v_pk_fma_f32 v[70:71], v[178:179], v[164:165], v[70:71] op_sel_hi:[1,0,1]\n"
"v_pk_fma_f32 v[100:101], v[184:185], v[164:165], v[100:101] op_sel_hi:[1,0,1]\n"
"v_pk_fma_f32 v[102:103], v[186:187], v[164:165], v[102:103] op_sel_hi:[1,0,1]\n"
"v_pk_mul_f32 v[200:201], v[68:69], v[68:69]\n"
"v_pk_mul_f32 v[202:203], v[70:71], v[70:71]\n"
"v_pk_mul_f32 v[204:205], v[100:101], v[100:101]\n"
"v_pk_mul_f32 v[206:207], v[102:103], v[102:103]\n"
"v_pk_fma_f32 v[200:201], v[200:201], v[166:167], v[164:165] op_sel:[0,0,1] op_sel_hi:[1,0,1]\n"
"v_pk_fma_f32 v[202:203], v[202:203], v[166:167], v[164:165] op_sel:[0,0,1] op_sel_hi:[1,0,1]\n"
"v_pk_fma_f32 v[204:205], v[204:205], v[166:167], v[164:165] op_sel:[0,0,1] op_sel_hi:[1,0,1]\n"
"v_pk_fma_f32 v[206:207], v[206:207], v[166:167], v[164:165] op_sel:[0,0,1] op_sel_hi:[1,0,1]\n"
"v_pk_mul_f32 v[200:201], v[68:69], v[200:201]\n"
"v_pk_mul_f32 v[202:203], v[70:71], v[202:203]\n"
"v_pk_mul_f32 v[204:205], v[100:101], v[204:205]\n"
"v_pk_mul_f32 v[206:207], v[102:103], v[206:207]\n"
"v_exp_f32 v200, v200\n"
"v_exp_f32 v201, v201\n"
"v_pk_fma_f32 v[84:85], v[180:181], v[164:165], v[84:85] op_sel_hi:[1,0,1]\n"
"v_exp_f32 v202, v202\n"
"v_exp_f32 v203, v203\n"
"v_pk_fma_f32 v[86:87], v[182:183], v[164:165], v[86:87] op_sel_hi:[1,0,1]\n"
"v_exp_f32 v204, v204\n"
"v_exp_f32 v205, v205\n"
"v_pk_fma_f32 v[116:117], v[188:189], v[164:165], v[116:117] op_sel_hi:[1,0,1]\n"
"v_exp_f32 v206, v206\n"
"v_exp_f32 v207, v207\n"
"v_pk_fma_f32 v[118:119], v[190:191], v[164:165], v[118:119] op_sel_hi:[1,0,1]\n"
"v_pk_fma_f32 v[200:201], v[166:167], v[200:201], v[166:167] op_sel:[1,0,1] op_sel_hi:[1,1,1]\n"
"v_pk_fma_f32 v[202:203], v[166:167], v[202:203], v[166:167] op_sel:[1,0,1] op_sel_hi:[1,1,1]\n"
"v_pk_fma_f32 v[204:205], v[166:167], v[204:205], v[166:167] op_sel:[1,0,1] op_sel_hi:[1,1,1]\n"
"v_pk_fma_f32 v[206:207], v[166:167], v[206:207], v[166:167] op_sel:[1,0,1] op_sel_hi:[1,1,1]\n"
"v_rcp_f32 v200, v200\n"
"v_rcp_f32 v201, v201\n"
"v_pk_mul_f32 v[84:85], v[68:69], v[84:85]\n"
"v_rcp_f32 v202, v202\n"
"v_rcp_f32 v203, v203\n"
"v_pk_mul_f32 v[86:87], v[70:71], v[86:87]\n"
"v_rcp_f32 v204, v204\n"
"v_rcp_f32 v205, v205\n"
"v_pk_mul_f32 v[116:117], v[100:101], v[116:117]\n"
"v_rcp_f32 v206, v206\n"
"v_rcp_f32 v207, v207\n"
"v_pk_mul_f32 v[118:119], v[102:103], v[118:119]\n"
"v_pk_mul_f32 v[84:85], v[84:85], v[200:201]\n"
"v_pk_mul_f32 v[86:87], v[86:87], v[202:203]\n"
"v_pk_mul_f32 v[116:117], v[116:117], v[204:205]\n"
"v_pk_mul_f32 v[118:119], v[118:119], v[206:207]\n"
"v_cvt_pk_bf16_f32 v196, v84, v85\n"
"v_cvt_pk_bf16_f32 v197, v86, v87\n"
"v_cvt_pk_bf16_f32 v198, v116, v117\n"
"v_cvt_pk_bf16_f32 v199, v118, v119\n"
"s_add_u32 s60, s78, 164864\n"
"buffer_store_dwordx4 v[196:199], v254, s[32:35], s60 offen offset:0 sc1\n"
"v_pk_fma_f32 v[72:73], v[176:177], v[168:169], v[72:73] op_sel_hi:[1,0,1]\n"
"v_pk_fma_f32 v[74:75], v[178:179], v[168:169], v[74:75] op_sel_hi:[1,0,1]\n"
"v_pk_fma_f32 v[104:105], v[184:185], v[168:169], v[104:105] op_sel_hi:[1,0,1]\n"
"v_pk_fma_f32 v[106:107], v[186:187], v[168:169], v[106:107] op_sel_hi:[1,0,1]\n"
"v_pk_mul_f32 v[200:201], v[72:73], v[72:73]\n"
"v_pk_mul_f32 v[202:203], v[74:75], v[74:75]\n"
"v_pk_mul_f32 v[204:205], v[104:105], v[104:105]\n"
"v_pk_mul_f32 v[206:207], v[106:107], v[106:107]\n"
"v_pk_fma_f32 v[200:201], v[200:201], v[170:171], v[168:169] op_sel:[0,0,1] op_sel_hi:[1,0,1]\n"
"v_pk_fma_f32 v[202:203], v[202:203], v[170:171], v[168:169] op_sel:[0,0,1] op_sel_hi:[1,0,1]\n"
"v_pk_fma_f32 v[204:205], v[204:205], v[170:171], v[168:169] op_sel:[0,0,1] op_sel_hi:[1,0,1]\n"
"v_pk_fma_f32 v[206:207], v[206:207], v[170:171], v[168:169] op_sel:[0,0,1] op_sel_hi:[1,0,1]\n"
"v_pk_mul_f32 v[200:201], v[72:73], v[200:201]\n"
"v_pk_mul_f32 v[202:203], v[74:75], v[202:203]\n"
"v_pk_mul_f32 v[204:205], v[104:105], v[204:205]\n"
"v_pk_mul_f32 v[206:207], v[106:107], v[206:207]\n"
"v_exp_f32 v200, v200\n"
"v_exp_f32 v201, v201\n"
"v_pk_fma_f32 v[88:89], v[180:181], v[168:169], v[88:89] op_sel_hi:[1,0,1]\n"
"v_exp_f32 v202, v202\n"
"v_exp_f32 v203, v203\n"
"v_pk_fma_f32 v[90:91], v[182:183], v[168:169], v[90:91] op_sel_hi:[1,0,1]\n"
"v_exp_f32 v204, v204\n"
"v_exp_f32 v205, v205\n"
"v_pk_fma_f32 v[120:121], v[188:189], v[168:169], v[120:121] op_sel_hi:[1,0,1]\n"
"v_exp_f32 v206, v206\n"
"v_exp_f32 v207, v207\n"
"v_pk_fma_f32 v[122:123], v[190:191], v[168:169], v[122:123] op_sel_hi:[1,0,1]\n"
"v_pk_fma_f32 v[200:201], v[170:171], v[200:201], v[170:171] op_sel:[1,0,1] op_sel_hi:[1,1,1]\n"
"v_pk_fma_f32 v[202:203], v[170:171], v[202:203], v[170:171] op_sel:[1,0,1] op_sel_hi:[1,1,1]\n"
"v_pk_fma_f32 v[204:205], v[170:171], v[204:205], v[170:171] op_sel:[1,0,1] op_sel_hi:[1,1,1]\n"
"v_pk_fma_f32 v[206:207], v[170:171], v[206:207], v[170:171] op_sel:[1,0,1] op_sel_hi:[1,1,1]\n"
"v_rcp_f32 v200, v200\n"
"v_rcp_f32 v201, v201\n"
"v_pk_mul_f32 v[88:89], v[72:73], v[88:89]\n"
"v_rcp_f32 v202, v202\n"
"v_rcp_f32 v203, v203\n"
"v_pk_mul_f32 v[90:91], v[74:75], v[90:91]\n"
"v_rcp_f32 v204, v204\n"
"v_rcp_f32 v205, v205\n"
"v_pk_mul_f32 v[120:121], v[104:105], v[120:121]\n"
"v_rcp_f32 v206, v206\n"
"v_rcp_f32 v207, v207\n"
"v_pk_mul_f32 v[122:123], v[106:107], v[122:123]\n"
"v_pk_mul_f32 v[88:89], v[88:89], v[200:201]\n"
"v_pk_mul_f32 v[90:91], v[90:91], v[202:203]\n"
"v_pk_mul_f32 v[120:121], v[120:121], v[204:205]\n"
"v_pk_mul_f32 v[122:123], v[122:123], v[206:207]\n"
"v_cvt_pk_bf16_f32 v192, v88, v89\n"
"v_cvt_pk_bf16_f32 v193, v90, v91\n"
"v_cvt_pk_bf16_f32 v194, v120, v121\n"
"v_cvt_pk_bf16_f32 v195, v122, v123\n"
"s_add_u32 s60, s78, 312320\n"
"buffer_store_dwordx4 v[192:195], v254, s[32:35], s60 offen offset:0 sc1\n"
"v_pk_fma_f32 v[76:77], v[176:177], v[172:173], v[76:77] op_sel_hi:[1,0,1]\n"
"v_pk_fma_f32 v[78:79], v[178:179], v[172:173], v[78:79] op_sel_hi:[1,0,1]\n"
"v_pk_fma_f32 v[108:109], v[184:185], v[172:173], v[108:109] op_sel_hi:[1,0,1]\n"
"v_pk_fma_f32 v[110:111], v[186:187], v[172:173], v[110:111] op_sel_hi:[1,0,1]\n"
"v_pk_mul_f32 v[200:201], v[76:77], v[76:77]\n"
"v_pk_mul_f32 v[202:203], v[78:79], v[78:79]\n"
"v_pk_mul_f32 v[204:205], v[108:109], v[108:109]\n"
"v_pk_mul_f32 v[206:207], v[110:111], v[110:111]\n"
"v_pk_fma_f32 v[200:201], v[200:201], v[174:175], v[172:173] op_sel:[0,0,1] op_sel_hi:[1,0,1]\n"
"v_pk_fma_f32 v[202:203], v[202:203], v[174:175], v[172:173] op_sel:[0,0,1] op_sel_hi:[1,0,1]\n"
"v_pk_fma_f32 v[204:205], v[204:205], v[174:175], v[172:173] op_sel:[0,0,1] op_sel_hi:[1,0,1]\n"
"v_pk_fma_f32 v[206:207], v[206:207], v[174:175], v[172:173] op_sel:[0,0,1] op_sel_hi:[1,0,1]\n"
"v_pk_mul_f32 v[200:201], v[76:77], v[200:201]\n"
"v_pk_mul_f32 v[202:203], v[78:79], v[202:203]\n"
"v_pk_mul_f32 v[204:205], v[108:109], v[204:205]\n"
"v_pk_mul_f32 v[206:207], v[110:111], v[206:207]\n"
"v_exp_f32 v200, v200\n"
"v_exp_f32 v201, v201\n"
"v_pk_fma_f32 v[92:93], v[180:181], v[172:173], v[92:93] op_sel_hi:[1,0,1]\n"
"v_exp_f32 v202, v202\n"
"v_exp_f32 v203, v203\n"
"v_pk_fma_f32 v[94:95], v[182:183], v[172:173], v[94:95] op_sel_hi:[1,0,1]\n"
"v_exp_f32 v204, v204\n"
"v_exp_f32 v205, v205\n"
"v_pk_fma_f32 v[124:125], v[188:189], v[172:173], v[124:125] op_sel_hi:[1,0,1]\n"
"v_exp_f32 v206, v206\n"
"v_exp_f32 v207, v207\n"
"v_pk_fma_f32 v[126:127], v[190:191], v[172:173], v[126:127] op_sel_hi:[1,0,1]\n"
"v_pk_fma_f32 v[200:201], v[174:175], v[200:201], v[174:175] op_sel:[1,0,1] op_sel_hi:[1,1,1]\n"
"v_pk_fma_f32 v[202:203], v[174:175], v[202:203], v[174:175] op_sel:[1,0,1] op_sel_hi:[1,1,1]\n"
"v_pk_fma_f32 v[204:205], v[174:175], v[204:205], v[174:175] op_sel:[1,0,1] op_sel_hi:[1,1,1]\n"
"v_pk_fma_f32 v[206:207], v[174:175], v[206:207], v[174:175] op_sel:[1,0,1] op_sel_hi:[1,1,1]\n"
"v_rcp_f32 v200, v200\n"
"v_rcp_f32 v201, v201\n"
"v_pk_mul_f32 v[92:93], v[76:77], v[92:93]\n"
"v_rcp_f32 v202, v202\n"
"v_rcp_f32 v203, v203\n"
"v_pk_mul_f32 v[94:95], v[78:79], v[94:95]\n"
"v_rcp_f32 v204, v204\n"
"v_rcp_f32 v205, v205\n"
"v_pk_mul_f32 v[124:125], v[108:109], v[124:125]\n"
"v_rcp_f32 v206, v206\n"
"v_rcp_f32 v207, v207\n"
"v_pk_mul_f32 v[126:127], v[110:111], v[126:127]\n"
"v_pk_mul_f32 v[92:93], v[92:93], v[200:201]\n"
"v_pk_mul_f32 v[94:95], v[94:95], v[202:203]\n"
"v_pk_mul_f32 v[124:125], v[124:125], v[204:205]\n"
"v_pk_mul_f32 v[126:127], v[126:127], v[206:207]\n"
"v_cvt_pk_bf16_f32 v196, v92, v93\n"
"v_cvt_pk_bf16_f32 v197, v94, v95\n"
"v_cvt_pk_bf16_f32 v198, v124, v125\n"
"v_cvt_pk_bf16_f32 v199, v126, v127\n"
"s_add_u32 s60, s78, 459776\n"
"buffer_store_dwordx4 v[196:199], v254, s[32:35], s60 offen offset:0 sc1\n"
"s_setprio 0\n"
"v_lshrrev_b32 v166, 4, v219\n"
"v_bfe_u32 v167, v219, 2, 2\n"
"v_and_b32 v168, 3, v219\n"
"v_lshlrev_b32 v168, 3, v168\n"
"v_lshl_add_u32 v169, v166, 3, v167\n"
"v_lshl_add_u32 v169, v169, 5, v168\n"
"v_add_u32 v169, s77, v169\n"
"s_waitcnt vmcnt(21)\n"
"ds_read_b64_tr_b16 v[200:201], v169 offset:0\n"
"ds_read_b64_tr_b16 v[202:203], v169 offset:128\n"
"s_lshr_b32 s54, s53, 1\n"
"s_lshl_b32 s54, s54, 11\n"
"s_lshl_b32 s55, s50, 12\n"
"s_add_u32 s54, s54, s55\n"
"v_add_u32 v152, s54, v254\n"
"v_xor_b32 v153, 0x800, v152\n"
"s_and_b32 s54, s53, 1\n"
"s_lshl_b32 s54, s54, 11\n"
"s_add_u32 s55, s54, 69632\n"
"v_add_u32 v154, s55, v254\n"
"s_xor_b32 s54, s54, 2048\n"
"s_add_u32 s55, s54, 69632\n"
"v_add_u32 v156, s55, v254\n"
"v_add_u32 v155, 0x9000, v154\n"
"v_add_u32 v157, 0x9000, v156\n"
"v_lshlrev_b32 v162, 3, v219\n"
"v_sub_u32 v163, v153, v162\n"
"v_sub_u32 v162, v152, v162\n"
"s_waitcnt lgkmcnt(1)\n"
"v_lshlrev_b32 v0, 16, v200\n"
"v_and_b32 v1, 0xffff0000, v200\n"
"v_lshlrev_b32 v2, 16, v201\n"
"v_and_b32 v3, 0xffff0000, v201\n"
"s_waitcnt lgkmcnt(0)\n"
"v_lshlrev_b32 v8, 16, v202\n"
"v_and_b32 v9, 0xffff0000, v202\n"
"v_lshlrev_b32 v10, 16, v203\n"
"v_and_b32 v11, 0xffff0000, v203\n"
"s_barrier\n"
"ds_read_b128 v[40:43], v152 offset:32768\n"
"ds_read_b128 v[44:47], v152 offset:33792\n"
"ds_read_b128 v[48:51], v153 offset:32768\n"
"ds_read_b128 v[52:55], v153 offset:33792\n"
"ds_read_b128 v[72:75], v157 offset:0\n"
"ds_read_b128 v[76:79], v157 offset:9216\n"
"ds_read_b128 v[80:83], v157 offset:1024\n"
"ds_read_b128 v[84:87], v157 offset:10240\n"
"ds_read_b128 v[56:59], v155 offset:0\n"
"ds_read_b128 v[60:63], v155 offset:9216\n"
"ds_read_b128 v[64:67], v155 offset:1024\n"
"ds_read_b128 v[68:71], v155 offset:10240\n"
"s_waitcnt lgkmcnt(0)\n"
"s_barrier\n"
"v_mfma_f32_16x16x32_bf16 v[20:23], v[72:75], v[48:51], 0\n"
"s_add_u32 s60, s63, 73728\n"
"s_add_u32 m0, s61, 36864\n"
"s_nop 0\n"
"buffer_load_dwordx4 v214, s[24:27], s60 offen offset:0 sc1 lds\n"
"ds_read_b128 v[96:99], v152 offset:49152\n"
"v_mfma_f32_16x16x32_bf16 v[20:23], v[76:79], v[52:55], v[20:23]\n"
"buffer_load_dwordx4 v214, s[24:27], s60 offen offset:1024 sc1 lds\n"
"v_mfma_f32_16x16x32_bf16 v[16:19], v[72:75], v[40:43], 0\n"
"ds_read_b128 v[100:103], v152 offset:50176\n"
"v_mfma_f32_16x16x32_bf16 v[16:19], v[76:79], v[44:47], v[16:19]\n"
"buffer_load_dwordx4 v214, s[24:27], s60 offen offset:2048 sc1 lds\n"
"v_mfma_f32_16x16x32_bf16 v[28:31], v[80:83], v[48:51], 0\n"
"ds_read_b128 v[104:107], v153 offset:49152\n"
"buffer_load_dwordx4 v214, s[24:27], s60 offen offset:3072 sc1 lds\n"
"v_mfma_f32_16x16x32_bf16 v[28:31], v[84:87], v[52:55], v[28:31]\n"
"ds_read_b128 v[108:111], v153 offset:50176\n"
"v_mfma_f32_16x16x32_bf16 v[24:27], v[80:83], v[40:43], 0\n"
"s_add_u32 m0, s62, 36864\n"
"s_cmp_ge_u32 s50, 2\n"
"s_cbranch_scc1 .Lf_s23\n"
"buffer_load_dwordx4 v215, s[24:27], s60 offen offset:0 sc1 lds\n"
".Lf_s23:\n"
"v_mfma_f32_16x16x32_bf16 v[24:27], v[84:87], v[44:47], v[24:27]\n"
"ds_read_b128 v[128:131], v157 offset:18432\n"
"v_mfma_f32_16x16x32_bf16 v[4:7], v[56:59], v[48:51], 0\n"
"ds_read_b128 v[132:135], v157 offset:27648\n"
"v_mfma_f32_16x16x32_bf16 v[4:7], v[60:63], v[52:55], v[4:7]\n"
"ds_read_b128 v[136:139], v157 offset:19456\n"
"ds_read_b128 v[140:143], v157 offset:28672\n"
"v_mfma_f32_16x16x32_bf16 v[0:3], v[56:59], v[40:43], v[0:3]\n"
"ds_read_b128 v[112:115], v155 offset:18432\n"
"v_mfma_f32_16x16x32_bf16 v[0:3], v[60:63], v[44:47], v[0:3]\n"
"ds_read_b128 v[116:119], v155 offset:27648\n"
"v_mfma_f32_16x16x32_bf16 v[12:15], v[64:67], v[48:51], 0\n"
"ds_read_b128 v[120:123], v155 offset:19456\n"
"v_mfma_f32_16x16x32_bf16 v[12:15], v[68:71], v[52:55], v[12:15]\n"
"ds_read_b128 v[124:127], v155 offset:28672\n"
"v_mfma_f32_16x16x32_bf16 v[8:11], v[64:67], v[40:43], v[8:11]\n"
"v_mfma_f32_16x16x32_bf16 v[8:11], v[68:71], v[44:47], v[8:11]\n"
"s_waitcnt vmcnt(17)\n"
"s_waitcnt lgkmcnt(0)\n"
"s_barrier\n"
"v_mfma_f32_16x16x32_bf16 v[20:23], v[128:131], v[104:107], v[20:23]\n"
"s_add_u32 s60, s63, 92160\n"
"s_add_u32 m0, s61, 55296\n"
"s_nop 0\n"
"buffer_load_dwordx4 v214, s[24:27], s60 offen offset:0 sc1 lds\n"
"ds_read_b128 v[40:43], v152 offset:0\n"
"v_mfma_f32_16x16x32_bf16 v[20:23], v[132:135], v[108:111], v[20:23]\n"
"buffer_load_dwordx4 v214, s[24:27], s60 offen offset:1024 sc1 lds\n"
"v_mfma_f32_16x16x32_bf16 v[16:19], v[128:131], v[96:99], v[16:19]\n"
"ds_read_b128 v[44:47], v152 offset:1024\n"
"v_mfma_f32_16x16x32_bf16 v[16:19], v[132:135], v[100:103], v[16:19]\n"
"buffer_load_dwordx4 v214, s[24:27], s60 offen offset:2048 sc1 lds\n"
"ds_read_b128 v[48:51], v153 offset:0\n"
"v_mfma_f32_16x16x32_bf16 v[28:31], v[136:139], v[104:107], v[28:31]\n"
"buffer_load_dwordx4 v214, s[24:27], s60 offen offset:3072 sc1 lds\n"
"v_mfma_f32_16x16x32_bf16 v[28:31], v[140:143], v[108:111], v[28:31]\n"
"ds_read_b128 v[52:55], v153 offset:1024\n"
"v_mfma_f32_16x16x32_bf16 v[24:27], v[136:139], v[96:99], v[24:27]\n"
"s_add_u32 m0, s62, 55296\n"
"s_cmp_ge_u32 s50, 2\n"
"s_cbranch_scc1 .Lf_s24\n"
"buffer_load_dwordx4 v215, s[24:27], s60 offen offset:0 sc1 lds\n"
".Lf_s24:\n"
"v_mfma_f32_16x16x32_bf16 v[24:27], v[140:143], v[100:103], v[24:27]\n"
"ds_read_b128 v[72:75], v156 offset:0\n"
"s_waitcnt vmcnt(8)\n"
"s_mov_b64 exec, 6\n"
"buffer_atomic_add v224, v225, s[64:67], s98 offen offset:0\n"
"s_mov_b64 exec, -1\n"
"v_mfma_f32_16x16x32_bf16 v[4:7], v[112:115], v[104:107], v[4:7]\n"
"ds_read_b128 v[76:79], v156 offset:9216\n"
"v_mfma_f32_16x16x32_bf16 v[4:7], v[116:119], v[108:111], v[4:7]\n"
"ds_read_b128 v[80:83], v156 offset:1024\n"
"v_mfma_f32_16x16x32_bf16 v[0:3], v[112:115], v[96:99], v[0:3]\n"
"ds_read_b128 v[84:87], v156 offset:10240\n"
"ds_read_b128 v[56:59], v154 offset:0\n"
"v_mfma_f32_16x16x32_bf16 v[0:3], v[116:119], v[100:103], v[0:3]\n"
"ds_read_b128 v[60:63], v154 offset:9216\n"
"v_mfma_f32_16x16x32_bf16 v[12:15], v[120:123], v[104:107], v[12:15]\n"
"ds_read_b128 v[64:67], v154 offset:1024\n"
"v_mfma_f32_16x16x32_bf16 v[12:15], v[124:127], v[108:111], v[12:15]\n"
"ds_read_b128 v[68:71], v154 offset:10240\n"
"v_mfma_f32_16x16x32_bf16 v[8:11], v[120:123], v[96:99], v[8:11]\n"
"v_mfma_f32_16x16x32_bf16 v[8:11], v[124:127], v[100:103], v[8:11]\n"
"s_waitcnt vmcnt(18)\n"
"s_waitcnt lgkmcnt(0)\n"
"s_barrier\n"
"v_mfma_f32_16x16x32_bf16 v[20:23], v[72:75], v[48:51], v[20:23]\n"
"s_add_u32 s60, s63, 110592\n"
"s_add_u32 m0, s61, 0\n"
"s_nop 0\n"
"buffer_load_dwordx4 v214, s[24:27], s60 offen offset:0 sc1 lds\n"
"ds_read_b128 v[96:99], v152 offset:16384\n"
"v_mfma_f32_16x16x32_bf16 v[20:23], v[76:79], v[52:55], v[20:23]\n"
"buffer_load_dwordx4 v214, s[24:27], s60 offen offset:1024 sc1 lds\n"
"v_mfma_f32_16x16x32_bf16 v[16:19], v[72:75], v[40:43], v[16:19]\n"
"ds_read_b128 v[100:103], v152 offset:17408\n"
"v_mfma_f32_16x16x32_bf16 v[16:19], v[76:79], v[44:47], v[16:19]\n"
"buffer_load_dwordx4 v214, s[24:27], s60 offen offset:2048 sc1 lds\n"
"v_mfma_f32_16x16x32_bf16 v[28:31], v[80:83], v[48:51], v[28:31]\n"
"ds_read_b128 v[104:107], v153 offset:16384\n"
"buffer_load_dwordx4 v214, s[24:27], s60 offen offset:3072 sc1 lds\n"
"v_mfma_f32_16x16x32_bf16 v[28:31], v[84:87], v[52:55], v[28:31]\n"
"ds_read_b128 v[108:111], v153 offset:17408\n"
"v_mfma_f32_16x16x32_bf16 v[24:27], v[80:83], v[40:43], v[24:27]\n"
"s_add_u32 m0, s62, 0\n"
"s_cmp_ge_u32 s50, 2\n"
"s_cbranch_scc1 .Lf_s25\n"
"buffer_load_dwordx4 v215, s[24:27], s60 offen offset:0 sc1 lds\n"
".Lf_s25:\n"
"v_mfma_f32_16x16x32_bf16 v[24:27], v[84:87], v[44:47], v[24:27]\n"
"ds_read_b128 v[128:131], v156 offset:18432\n"
"v_mfma_f32_16x16x32_bf16 v[4:7], v[56:59], v[48:51], v[4:7]\n"
"ds_read_b128 v[132:135], v156 offset:27648\n"
"v_mfma_f32_16x16x32_bf16 v[4:7], v[60:63], v[52:55], v[4:7]\n"
"ds_read_b128 v[136:139], v156 offset:19456\n"
"ds_read_b128 v[140:143], v156 offset:28672\n"
"v_mfma_f32_16x16x32_bf16 v[0:3], v[56:59], v[40:43], v[0:3]\n"
"ds_read_b128 v[112:115], v154 offset:18432\n"
"v_mfma_f32_16x16x32_bf16 v[0:3], v[60:63], v[44:47], v[0:3]\n"
"ds_read_b128 v[116:119], v154 offset:27648\n"
"v_mfma_f32_16x16x32_bf16 v[12:15], v[64:67], v[48:51], v[12:15]\n"
"ds_read_b128 v[120:123], v154 offset:19456\n"
"v_mfma_f32_16x16x32_bf16 v[12:15], v[68:71], v[52:55], v[12:15]\n"
"ds_read_b128 v[124:127], v154 offset:28672\n"
"v_mfma_f32_16x16x32_bf16 v[8:11], v[64:67], v[40:43], v[8:11]\n"
"v_mfma_f32_16x16x32_bf16 v[8:11], v[68:71], v[44:47], v[8:11]\n"
"s_waitcnt vmcnt(9)\n"
"s_waitcnt lgkmcnt(0)\n"
"s_barrier\n"
"v_mfma_f32_16x16x32_bf16 v[20:23], v[128:131], v[104:107], v[20:23]\n"
"s_add_u32 s60, s63, 129024\n"
"s_add_u32 m0, s61, 18432\n"
"s_nop 0\n"
"buffer_load_dwordx4 v214, s[24:27], s60 offen offset:0 sc1 lds\n"
"ds_read_b128 v[40:43], v152 offset:32768\n"
"v_mfma_f32_16x16x32_bf16 v[20:23], v[132:135], v[108:111], v[20:23]\n"
"buffer_load_dwordx4 v214, s[24:27], s60 offen offset:1024 sc1 lds\n"
"v_mfma_f32_16x16x32_bf16 v[16:19], v[128:131], v[96:99], v[16:19]\n"
"ds_read_b128 v[44:47], v152 offset:33792\n"
"v_mfma_f32_16x16x32_bf16 v[16:19], v[132:135], v[100:103], v[16:19]\n"
"buffer_load_dwordx4 v214, s[24:27], s60 offen offset:2048 sc1 lds\n"
"v_mfma_f32_16x16x32_bf16 v[28:31], v[136:139], v[104:107], v[28:31]\n"
"ds_read_b128 v[48:51], v153 offset:32768\n"
"buffer_load_dwordx4 v214, s[24:27], s60 offen offset:3072 sc1 lds\n"
"v_mfma_f32_16x16x32_bf16 v[28:31], v[140:143], v[108:111], v[28:31]\n"
"ds_read_b128 v[52:55], v153 offset:33792\n"
"v_mfma_f32_16x16x32_bf16 v[24:27], v[136:139], v[96:99], v[24:27]\n"
"s_add_u32 m0, s62, 18432\n"
"s_cmp_ge_u32 s50, 2\n"
"s_cbranch_scc1 .Lf_s26\n"
"buffer_load_dwordx4 v215, s[24:27], s60 offen offset:0 sc1 lds\n"
".Lf_s26:\n"
"v_mfma_f32_16x16x32_bf16 v[24:27], v[140:143], v[100:103], v[24:27]\n"
"ds_read_b128 v[72:75], v157 offset:0\n"
"v_mfma_f32_16x16x32_bf16 v[4:7], v[112:115], v[104:107], v[4:7]\n"
"ds_read_b128 v[76:79], v157 offset:9216\n"
"v_mfma_f32_16x16x32_bf16 v[4:7], v[116:119], v[108:111], v[4:7]\n"
"ds_read_b128 v[80:83], v157 offset:1024\n"
"ds_read_b128 v[84:87], v157 offset:10240\n"
"v_mfma_f32_16x16x32_bf16 v[0:3], v[112:115], v[96:99], v[0:3]\n"
"ds_read_b128 v[56:59], v155 offset:0\n"
"v_mfma_f32_16x16x32_bf16 v[0:3], v[116:119], v[100:103], v[0:3]\n"
"ds_read_b128 v[60:63], v155 offset:9216\n"
"v_mfma_f32_16x16x32_bf16 v[12:15], v[120:123], v[104:107], v[12:15]\n"
"ds_read_b128 v[64:67], v155 offset:1024\n"
"v_mfma_f32_16x16x32_bf16 v[12:15], v[124:127], v[108:111], v[12:15]\n"
"ds_read_b128 v[68:71], v155 offset:10240\n"
"v_mfma_f32_16x16x32_bf16 v[8:11], v[120:123], v[96:99], v[8:11]\n"
"v_mfma_f32_16x16x32_bf16 v[8:11], v[124:127], v[100:103], v[8:11]\n"
"s_waitcnt vmcnt(9)\n"
"s_waitcnt lgkmcnt(0)\n"
"s_barrier\n"
"v_mfma_f32_16x16x32_bf16 v[20:23], v[72:75], v[48:51], v[20:23]\n"
"s_add_u32 s60, s63, 147456\n"
"s_add_u32 m0, s61, 36864\n"
"s_nop 0\n"
"buffer_load_dwordx4 v214, s[24:27], s60 offen offset:0 sc1 lds\n"
"ds_read_b128 v[96:99], v152 offset:49152\n"
"v_mfma_f32_16x16x32_bf16 v[20:23], v[76:79], v[52:55], v[20:23]\n"
"buffer_load_dwordx4 v214, s[24:27], s60 offen offset:1024 sc1 lds\n"
"v_mfma_f32_16x16x32_bf16 v[16:19], v[72:75], v[40:43], v[16:19]\n"
"ds_read_b128 v[100:103], v152 offset:50176\n"
"v_mfma_f32_16x16x32_bf16 v[16:19], v[76:79], v[44:47], v[16:19]\n"
"buffer_load_dwordx4 v214, s[24:27], s60 offen offset:2048 sc1 lds\n"
"v_mfma_f32_16x16x32_bf16 v[28:31], v[80:83], v[48:51], v[28:31]\n"
"ds_read_b128 v[104:107], v153 offset:49152\n"
"buffer_load_dwordx4 v214, s[24:27], s60 offen offset:3072 sc1 lds\n"
"v_mfma_f32_16x16x32_bf16 v[28:31], v[84:87], v[52:55], v[28:31]\n"
"ds_read_b128 v[108:111], v153 offset:50176\n"
"v_mfma_f32_16x16x32_bf16 v[24:27], v[80:83], v[40:43], v[24:27]\n"
"s_add_u32 m0, s62, 36864\n"
"s_cmp_ge_u32 s50, 2\n"
"s_cbranch_scc1 .Lf_s27\n"
"buffer_load_dwordx4 v215, s[24:27], s60 offen offset:0 sc1 lds\n"
".Lf_s27:\n"
"v_mfma_f32_16x16x32_bf16 v[24:27], v[84:87], v[44:47], v[24:27]\n"
"ds_read_b128 v[128:131], v157 offset:18432\n"
"v_mfma_f32_16x16x32_bf16 v[4:7], v[56:59], v[48:51], v[4:7]\n"
"ds_read_b128 v[132:135], v157 offset:27648\n"
"v_mfma_f32_16x16x32_bf16 v[4:7], v[60:63], v[52:55], v[4:7]\n"
"ds_read_b128 v[136:139], v157 offset:19456\n"
"ds_read_b128 v[140:143], v157 offset:28672\n"
"v_mfma_f32_16x16x32_bf16 v[0:3], v[56:59], v[40:43], v[0:3]\n"
"ds_read_b128 v[112:115], v155 offset:18432\n"
"v_mfma_f32_16x16x32_bf16 v[0:3], v[60:63], v[44:47], v[0:3]\n"
"ds_read_b128 v[116:119], v155 offset:27648\n"
"v_mfma_f32_16x16x32_bf16 v[12:15], v[64:67], v[48:51], v[12:15]\n"
"ds_read_b128 v[120:123], v155 offset:19456\n"
"v_mfma_f32_16x16x32_bf16 v[12:15], v[68:71], v[52:55], v[12:15]\n"
"ds_read_b128 v[124:127], v155 offset:28672\n"
"v_mfma_f32_16x16x32_bf16 v[8:11], v[64:67], v[40:43], v[8:11]\n"
"v_mfma_f32_16x16x32_bf16 v[8:11], v[68:71], v[44:47], v[8:11]\n"
"s_waitcnt vmcnt(8)\n"
"s_waitcnt lgkmcnt(0)\n"
"s_barrier\n"
"v_mfma_f32_16x16x32_bf16 v[20:23], v[128:131], v[104:107], v[20:23]\n"
"s_add_u32 s60, s63, 165888\n"
"s_add_u32 m0, s61, 55296\n"
"s_nop 0\n"
"buffer_load_dwordx4 v214, s[24:27], s60 offen offset:0 sc1 lds\n"
"ds_read_b128 v[40:43], v152 offset:0\n"
"v_mfma_f32_16x16x32_bf16 v[20:23], v[132:135], v[108:111], v[20:23]\n"
"buffer_load_dwordx4 v214, s[24:27], s60 offen offset:1024 sc1 lds\n"
"v_mfma_f32_16x16x32_bf16 v[16:19], v[128:131], v[96:99], v[16:19]\n"
"ds_read_b128 v[44:47], v152 offset:1024\n"
"v_mfma_f32_16x16x32_bf16 v[16:19], v[132:135], v[100:103], v[16:19]\n"
"buffer_load_dwordx4 v214, s[24:27], s60 offen offset:2048 sc1 lds\n"
"v_mfma_f32_16x16x32_bf16 v[28:31], v[136:139], v[104:107], v[28:31]\n"
"ds_read_b128 v[48:51], v153 offset:0\n"
"buffer_load_dwordx4 v214, s[24:27], s60 offen offset:3072 sc1 lds\n"
"v_mfma_f32_16x16x32_bf16 v[28:31], v[140:143], v[108:111], v[28:31]\n"
"ds_read_b128 v[52:55], v153 offset:1024\n"
"v_mfma_f32_16x16x32_bf16 v[24:27], v[136:139], v[96:99], v[24:27]\n"
"s_add_u32 m0, s62, 55296\n"
"s_cmp_ge_u32 s50, 2\n"
"s_cbranch_scc1 .Lf_s28\n"
"buffer_load_dwordx4 v215, s[24:27], s60 offen offset:0 sc1 lds\n"
".Lf_s28:\n"
"v_mfma_f32_16x16x32_bf16 v[24:27], v[140:143], v[100:103], v[24:27]\n"
"ds_read_b128 v[72:75], v156 offset:0\n"
"v_mfma_f32_16x16x32_bf16 v[4:7], v[112:115], v[104:107], v[4:7]\n"
"ds_read_b128 v[76:79], v156 offset:9216\n"
"v_mfma_f32_16x16x32_bf16 v[4:7], v[116:119], v[108:111], v[4:7]\n"
"ds_read_b128 v[80:83], v156 offset:1024\n"
"ds_read_b128 v[84:87], v156 offset:10240\n"
"v_mfma_f32_16x16x32_bf16 v[0:3], v[112:115], v[96:99], v[0:3]\n"
"ds_read_b128 v[56:59], v154 offset:0\n"
"v_mfma_f32_16x16x32_bf16 v[0:3], v[116:119], v[100:103], v[0:3]\n"
"ds_read_b128 v[60:63], v154 offset:9216\n"
"v_mfma_f32_16x16x32_bf16 v[12:15], v[120:123], v[104:107], v[12:15]\n"
"ds_read_b128 v[64:67], v154 offset:1024\n"
"v_mfma_f32_16x16x32_bf16 v[12:15], v[124:127], v[108:111], v[12:15]\n"
"ds_read_b128 v[68:71], v154 offset:10240\n"
"v_mfma_f32_16x16x32_bf16 v[8:11], v[120:123], v[96:99], v[8:11]\n"
"v_mfma_f32_16x16x32_bf16 v[8:11], v[124:127], v[100:103], v[8:11]\n"
"s_waitcnt vmcnt(8)\n"
"s_waitcnt lgkmcnt(0)\n"
"s_barrier\n"
"v_mfma_f32_16x16x32_bf16 v[20:23], v[72:75], v[48:51], v[20:23]\n"
"s_add_u32 s60, s63, 184320\n"
"s_add_u32 m0, s61, 0\n"
"s_nop 0\n"
"buffer_load_dwordx4 v214, s[24:27], s60 offen offset:0 sc1 lds\n"
"ds_read_b128 v[96:99], v152 offset:16384\n"
"v_mfma_f32_16x16x32_bf16 v[20:23], v[76:79], v[52:55], v[20:23]\n"
"buffer_load_dwordx4 v214, s[24:27], s60 offen offset:1024 sc1 lds\n"
"v_mfma_f32_16x16x32_bf16 v[16:19], v[72:75], v[40:43], v[16:19]\n"
"ds_read_b128 v[100:103], v152 offset:17408\n"
"v_mfma_f32_16x16x32_bf16 v[16:19], v[76:79], v[44:47], v[16:19]\n"
"buffer_load_dwordx4 v214, s[24:27], s60 offen offset:2048 sc1 lds\n"
"v_mfma_f32_16x16x32_bf16 v[28:31], v[80:83], v[48:51], v[28:31]\n"
"ds_read_b128 v[104:107], v153 offset:16384\n"
"buffer_load_dwordx4 v214, s[24:27], s60 offen offset:3072 sc1 lds\n"
"v_mfma_f32_16x16x32_bf16 v[28:31], v[84:87], v[52:55], v[28:31]\n"
"ds_read_b128 v[108:111], v153 offset:17408\n"
"v_mfma_f32_16x16x32_bf16 v[24:27], v[80:83], v[40:43], v[24:27]\n"
"s_add_u32 m0, s62, 0\n"
"s_cmp_ge_u32 s50, 2\n"
"s_cbranch_scc1 .Lf_s29\n"
"buffer_load_dwordx4 v215, s[24:27], s60 offen offset:0 sc1 lds\n"
".Lf_s29:\n"
"v_mfma_f32_16x16x32_bf16 v[24:27], v[84:87], v[44:47], v[24:27]\n"
"ds_read_b128 v[128:131], v156 offset:18432\n"
"v_mfma_f32_16x16x32_bf16 v[4:7], v[56:59], v[48:51], v[4:7]\n"
"ds_read_b128 v[132:135], v156 offset:27648\n"
"v_mfma_f32_16x16x32_bf16 v[4:7], v[60:63], v[52:55], v[4:7]\n"
"ds_read_b128 v[136:139], v156 offset:19456\n"
"ds_read_b128 v[140:143], v156 offset:28672\n"
"v_mfma_f32_16x16x32_bf16 v[0:3], v[56:59], v[40:43], v[0:3]\n"
"ds_read_b128 v[112:115], v154 offset:18432\n"
"v_mfma_f32_16x16x32_bf16 v[0:3], v[60:63], v[44:47], v[0:3]\n"
"ds_read_b128 v[116:119], v154 offset:27648\n"
"v_mfma_f32_16x16x32_bf16 v[12:15], v[64:67], v[48:51], v[12:15]\n"
"ds_read_b128 v[120:123], v154 offset:19456\n"
"v_mfma_f32_16x16x32_bf16 v[12:15], v[68:71], v[52:55], v[12:15]\n"
"ds_read_b128 v[124:127], v154 offset:28672\n"
"v_mfma_f32_16x16x32_bf16 v[8:11], v[64:67], v[40:43], v[8:11]\n"
"v_mfma_f32_16x16x32_bf16 v[8:11], v[68:71], v[44:47], v[8:11]\n"
"s_waitcnt vmcnt(8)\n"
"s_waitcnt lgkmcnt(0)\n"
"s_barrier\n"
"v_mfma_f32_16x16x32_bf16 v[20:23], v[128:131], v[104:107], v[20:23]\n"
"s_add_u32 s60, s63, 202752\n"
"s_add_u32 m0, s61, 18432\n"
"s_nop 0\n"
"buffer_load_dwordx4 v214, s[24:27], s60 offen offset:0 sc1 lds\n"
"ds_read_b128 v[40:43], v152 offset:32768\n"
"v_mfma_f32_16x16x32_bf16 v[20:23], v[132:135], v[108:111], v[20:23]\n"
"buffer_load_dwordx4 v214, s[24:27], s60 offen offset:1024 sc1 lds\n"
"v_mfma_f32_16x16x32_bf16 v[16:19], v[128:131], v[96:99], v[16:19]\n"
"ds_read_b128 v[44:47], v152 offset:33792\n"
"v_mfma_f32_16x16x32_bf16 v[16:19], v[132:135], v[100:103], v[16:19]\n"
"buffer_load_dwordx4 v214, s[24:27], s60 offen offset:2048 sc1 lds\n"
"v_mfma_f32_16x16x32_bf16 v[28:31], v[136:139], v[104:107], v[28:31]\n"
"ds_read_b128 v[48:51], v153 offset:32768\n"
"buffer_load_dwordx4 v214, s[24:27], s60 offen offset:3072 sc1 lds\n"
"v_mfma_f32_16x16x32_bf16 v[28:31], v[140:143], v[108:111], v[28:31]\n"
"ds_read_b128 v[52:55], v153 offset:33792\n"
"v_mfma_f32_16x16x32_bf16 v[24:27], v[136:139], v[96:99], v[24:27]\n"
"s_add_u32 m0, s62, 18432\n"
"s_cmp_ge_u32 s50, 2\n"
"s_cbranch_scc1 .Lf_s30\n"
"buffer_load_dwordx4 v215, s[24:27], s60 offen offset:0 sc1 lds\n"
".Lf_s30:\n"
"v_mfma_f32_16x16x32_bf16 v[24:27], v[140:143], v[100:103], v[24:27]\n"
"ds_read_b128 v[72:75], v157 offset:0\n"
"v_mfma_f32_16x16x32_bf16 v[4:7], v[112:115], v[104:107], v[4:7]\n"
"ds_read_b128 v[76:79], v157 offset:9216\n"
"v_mfma_f32_16x16x32_bf16 v[4:7], v[116:119], v[108:111], v[4:7]\n"
"ds_read_b128 v[80:83], v157 offset:1024\n"
"ds_read_b128 v[84:87], v157 offset:10240\n"
"v_mfma_f32_16x16x32_bf16 v[0:3], v[112:115], v[96:99], v[0:3]\n"
"ds_read_b128 v[56:59], v155 offset:0\n"
"v_mfma_f32_16x16x32_bf16 v[0:3], v[116:119], v[100:103], v[0:3]\n"
"ds_read_b128 v[60:63], v155 offset:9216\n"
"v_mfma_f32_16x16x32_bf16 v[12:15], v[120:123], v[104:107], v[12:15]\n"
"ds_read_b128 v[64:67], v155 offset:1024\n"
"v_mfma_f32_16x16x32_bf16 v[12:15], v[124:127], v[108:111], v[12:15]\n"
"ds_read_b128 v[68:71], v155 offset:10240\n"
"v_mfma_f32_16x16x32_bf16 v[8:11], v[120:123], v[96:99], v[8:11]\n"
"v_mfma_f32_16x16x32_bf16 v[8:11], v[124:127], v[100:103], v[8:11]\n"
"s_waitcnt vmcnt(8)\n"
"s_waitcnt lgkmcnt(0)\n"
"s_barrier\n"
"v_mfma_f32_16x16x32_bf16 v[20:23], v[72:75], v[48:51], v[20:23]\n"
"s_add_u32 s60, s63, 221184\n"
"s_add_u32 m0, s61, 36864\n"
"s_nop 0\n"
"buffer_load_dwordx4 v214, s[24:27], s60 offen offset:0 sc1 lds\n"
"ds_read_b128 v[96:99], v152 offset:49152\n"
"v_mfma_f32_16x16x32_bf16 v[20:23], v[76:79], v[52:55], v[20:23]\n"
"buffer_load_dwordx4 v214, s[24:27], s60 offen offset:1024 sc1 lds\n"
"v_mfma_f32_16x16x32_bf16 v[16:19], v[72:75], v[40:43], v[16:19]\n"
"ds_read_b128 v[100:103], v152 offset:50176\n"
"v_mfma_f32_16x16x32_bf16 v[16:19], v[76:79], v[44:47], v[16:19]\n"
"buffer_load_dwordx4 v214, s[24:27], s60 offen offset:2048 sc1 lds\n"
"v_mfma_f32_16x16x32_bf16 v[28:31], v[80:83], v[48:51], v[28:31]\n"
"ds_read_b128 v[104:107], v153 offset:49152\n"
"buffer_load_dwordx4 v214, s[24:27], s60 offen offset:3072 sc1 lds\n"
"v_mfma_f32_16x16x32_bf16 v[28:31], v[84:87], v[52:55], v[28:31]\n"
"ds_read_b128 v[108:111], v153 offset:50176\n"
"v_mfma_f32_16x16x32_bf16 v[24:27], v[80:83], v[40:43], v[24:27]\n"
"s_add_u32 m0, s62, 36864\n"
"s_cmp_ge_u32 s50, 2\n"
"s_cbranch_scc1 .Lf_s31\n"
"buffer_load_dwordx4 v215, s[24:27], s60 offen offset:0 sc1 lds\n"
".Lf_s31:\n"
"v_mfma_f32_16x16x32_bf16 v[24:27], v[84:87], v[44:47], v[24:27]\n"
"ds_read_b128 v[128:131], v157 offset:18432\n"
"v_mfma_f32_16x16x32_bf16 v[4:7], v[56:59], v[48:51], v[4:7]\n"
"ds_read_b128 v[132:135], v157 offset:27648\n"
"v_mfma_f32_16x16x32_bf16 v[4:7], v[60:63], v[52:55], v[4:7]\n"
"ds_read_b128 v[136:139], v157 offset:19456\n"
"ds_read_b128 v[140:143], v157 offset:28672\n"
"v_mfma_f32_16x16x32_bf16 v[0:3], v[56:59], v[40:43], v[0:3]\n"
"ds_read_b128 v[112:115], v155 offset:18432\n"
"v_mfma_f32_16x16x32_bf16 v[0:3], v[60:63], v[44:47], v[0:3]\n"
"ds_read_b128 v[116:119], v155 offset:27648\n"
"v_mfma_f32_16x16x32_bf16 v[12:15], v[64:67], v[48:51], v[12:15]\n"
"ds_read_b128 v[120:123], v155 offset:19456\n"
"v_mfma_f32_16x16x32_bf16 v[12:15], v[68:71], v[52:55], v[12:15]\n"
"ds_read_b128 v[124:127], v155 offset:28672\n"
"v_mfma_f32_16x16x32_bf16 v[8:11], v[64:67], v[40:43], v[8:11]\n"
"v_mfma_f32_16x16x32_bf16 v[8:11], v[68:71], v[44:47], v[8:11]\n"
"s_waitcnt vmcnt(8)\n"
"s_waitcnt lgkmcnt(0)\n"
"s_barrier\n"
"v_mfma_f32_16x16x32_bf16 v[20:23], v[128:131], v[104:107], v[20:23]\n"
"s_add_u32 s60, s63, 239616\n"
"s_add_u32 m0, s61, 55296\n"
"s_nop 0\n"
"buffer_load_dwordx4 v214, s[24:27], s60 offen offset:0 sc1 lds\n"
"ds_read_b128 v[40:43], v152 offset:0\n"
"v_mfma_f32_16x16x32_bf16 v[20:23], v[132:135], v[108:111], v[20:23]\n"
"buffer_load_dwordx4 v214, s[24:27], s60 offen offset:1024 sc1 lds\n"
"v_mfma_f32_16x16x32_bf16 v[16:19], v[128:131], v[96:99], v[16:19]\n"
"ds_read_b128 v[44:47], v152 offset:1024\n"
"v_mfma_f32_16x16x32_bf16 v[16:19], v[132:135], v[100:103], v[16:19]\n"
"buffer_load_dwordx4 v214, s[24:27], s60 offen offset:2048 sc1 lds\n"
"v_mfma_f32_16x16x32_bf16 v[28:31], v[136:139], v[104:107], v[28:31]\n"
"ds_read_b128 v[48:51], v153 offset:0\n"
"buffer_load_dwordx4 v214, s[24:27], s60 offen offset:3072 sc1 lds\n"
"v_mfma_f32_16x16x32_bf16 v[28:31], v[140:143], v[108:111], v[28:31]\n"
"ds_read_b128 v[52:55], v153 offset:1024\n"
"v_mfma_f32_16x16x32_bf16 v[24:27], v[136:139], v[96:99], v[24:27]\n"
"s_add_u32 m0, s62, 55296\n"
"s_cmp_ge_u32 s50, 2\n"
"s_cbranch_scc1 .Lf_s32\n"
"buffer_load_dwordx4 v215, s[24:27], s60 offen offset:0 sc1 lds\n"
".Lf_s32:\n"
"v_mfma_f32_16x16x32_bf16 v[24:27], v[140:143], v[100:103], v[24:27]\n"
"ds_read_b128 v[72:75], v156 offset:0\n"
"v_mfma_f32_16x16x32_bf16 v[4:7], v[112:115], v[104:107], v[4:7]\n"
"ds_read_b128 v[76:79], v156 offset:9216\n"
"v_mfma_f32_16x16x32_bf16 v[4:7], v[116:119], v[108:111], v[4:7]\n"
"ds_read_b128 v[80:83], v156 offset:1024\n"
"ds_read_b128 v[84:87], v156 offset:10240\n"
"v_mfma_f32_16x16x32_bf16 v[0:3], v[112:115], v[96:99], v[0:3]\n"
"ds_read_b128 v[56:59], v154 offset:0\n"
"v_mfma_f32_16x16x32_bf16 v[0:3], v[116:119], v[100:103], v[0:3]\n"
"ds_read_b128 v[60:63], v154 offset:9216\n"
"v_mfma_f32_16x16x32_bf16 v[12:15], v[120:123], v[104:107], v[12:15]\n"
"ds_read_b128 v[64:67], v154 offset:1024\n"
"v_mfma_f32_16x16x32_bf16 v[12:15], v[124:127], v[108:111], v[12:15]\n"
"ds_read_b128 v[68:71], v154 offset:10240\n"
"v_mfma_f32_16x16x32_bf16 v[8:11], v[120:123], v[96:99], v[8:11]\n"
"v_mfma_f32_16x16x32_bf16 v[8:11], v[124:127], v[100:103], v[8:11]\n"
"s_waitcnt vmcnt(8)\n"
"s_waitcnt lgkmcnt(0)\n"
"s_barrier\n"
"v_mfma_f32_16x16x32_bf16 v[20:23], v[72:75], v[48:51], v[20:23]\n"
"s_add_u32 s60, s63, 258048\n"
"s_add_u32 m0, s61, 0\n"
"s_nop 0\n"
"buffer_load_dwordx4 v214, s[24:27], s60 offen offset:0 sc1 lds\n"
"ds_read_b128 v[96:99], v152 offset:16384\n"
"v_mfma_f32_16x16x32_bf16 v[20:23], v[76:79], v[52:55], v[20:23]\n"
"buffer_load_dwordx4 v214, s[24:27], s60 offen offset:1024 sc1 lds\n"
"v_mfma_f32_16x16x32_bf16 v[16:19], v[72:75], v[40:43], v[16:19]\n"
"ds_read_b128 v[100:103], v152 offset:17408\n"
"v_mfma_f32_16x16x32_bf16 v[16:19], v[76:79], v[44:47], v[16:19]\n"
"buffer_load_dwordx4 v214, s[24:27], s60 offen offset:2048 sc1 lds\n"
"v_mfma_f32_16x16x32_bf16 v[28:31], v[80:83], v[48:51], v[28:31]\n"
"ds_read_b128 v[104:107], v153 offset:16384\n"
"buffer_load_dwordx4 v214, s[24:27], s60 offen offset:3072 sc1 lds\n"
"v_mfma_f32_16x16x32_bf16 v[28:31], v[84:87], v[52:55], v[28:31]\n"
"ds_read_b128 v[108:111], v153 offset:17408\n"
"v_mfma_f32_16x16x32_bf16 v[24:27], v[80:83], v[40:43], v[24:27]\n"
"s_add_u32 m0, s62, 0\n"
"s_cmp_ge_u32 s50, 2\n"
"s_cbranch_scc1 .Lf_s33\n"
"buffer_load_dwordx4 v215, s[24:27], s60 offen offset:0 sc1 lds\n"
".Lf_s33:\n"
"v_mfma_f32_16x16x32_bf16 v[24:27], v[84:87], v[44:47], v[24:27]\n"
"ds_read_b128 v[128:131], v156 offset:18432\n"
"v_mfma_f32_16x16x32_bf16 v[4:7], v[56:59], v[48:51], v[4:7]\n"
"ds_read_b128 v[132:135], v156 offset:27648\n"
"v_mfma_f32_16x16x32_bf16 v[4:7], v[60:63], v[52:55], v[4:7]\n"
"ds_read_b128 v[136:139], v156 offset:19456\n"
"ds_read_b128 v[140:143], v156 offset:28672\n"
"v_mfma_f32_16x16x32_bf16 v[0:3], v[56:59], v[40:43], v[0:3]\n"
"ds_read_b128 v[112:115], v154 offset:18432\n"
"v_mfma_f32_16x16x32_bf16 v[0:3], v[60:63], v[44:47], v[0:3]\n"
"ds_read_b128 v[116:119], v154 offset:27648\n"
"v_mfma_f32_16x16x32_bf16 v[12:15], v[64:67], v[48:51], v[12:15]\n"
"ds_read_b128 v[120:123], v154 offset:19456\n"
"v_mfma_f32_16x16x32_bf16 v[12:15], v[68:71], v[52:55], v[12:15]\n"
"ds_read_b128 v[124:127], v154 offset:28672\n"
"v_mfma_f32_16x16x32_bf16 v[8:11], v[64:67], v[40:43], v[8:11]\n"
"v_mfma_f32_16x16x32_bf16 v[8:11], v[68:71], v[44:47], v[8:11]\n"
"s_waitcnt vmcnt(8)\n"
"s_waitcnt lgkmcnt(0)\n"
"s_barrier\n"
"v_mfma_f32_16x16x32_bf16 v[20:23], v[128:131], v[104:107], v[20:23]\n"
"s_add_u32 s60, s63, 276480\n"
"s_add_u32 m0, s61, 18432\n"
"s_nop 0\n"
"buffer_load_dwordx4 v214, s[24:27], s60 offen offset:0 sc1 lds\n"
"ds_read_b128 v[40:43], v152 offset:32768\n"
"v_mfma_f32_16x16x32_bf16 v[20:23], v[132:135], v[108:111], v[20:23]\n"
"buffer_load_dwordx4 v214, s[24:27], s60 offen offset:1024 sc1 lds\n"
"v_mfma_f32_16x16x32_bf16 v[16:19], v[128:131], v[96:99], v[16:19]\n"
"ds_read_b128 v[44:47], v152 offset:33792\n"
"v_mfma_f32_16x16x32_bf16 v[16:19], v[132:135], v[100:103], v[16:19]\n"
"buffer_load_dwordx4 v214, s[24:27], s60 offen offset:2048 sc1 lds\n"
"v_mfma_f32_16x16x32_bf16 v[28:31], v[136:139], v[104:107], v[28:31]\n"
"ds_read_b128 v[48:51], v153 offset:32768\n"
"buffer_load_dwordx4 v214, s[24:27], s60 offen offset:3072 sc1 lds\n"
"v_mfma_f32_16x16x32_bf16 v[28:31], v[140:143], v[108:111], v[28:31]\n"
"ds_read_b128 v[52:55], v153 offset:33792\n"
"v_mfma_f32_16x16x32_bf16 v[24:27], v[136:139], v[96:99], v[24:27]\n"
"s_add_u32 m0, s62, 18432\n"
"s_cmp_ge_u32 s50, 2\n"
"s_cbranch_scc1 .Lf_s34\n"
"buffer_load_dwordx4 v215, s[24:27], s60 offen offset:0 sc1 lds\n"
".Lf_s34:\n"
"v_mfma_f32_16x16x32_bf16 v[24:27], v[140:143], v[100:103], v[24:27]\n"
"ds_read_b128 v[72:75], v157 offset:0\n"
"v_mfma_f32_16x16x32_bf16 v[4:7], v[112:115], v[104:107], v[4:7]\n"
"ds_read_b128 v[76:79], v157 offset:9216\n"
"v_mfma_f32_16x16x32_bf16 v[4:7], v[116:119], v[108:111], v[4:7]\n"
"ds_read_b128 v[80:83], v157 offset:1024\n"
"ds_read_b128 v[84:87], v157 offset:10240\n"
"v_mfma_f32_16x16x32_bf16 v[0:3], v[112:115], v[96:99], v[0:3]\n"
"ds_read_b128 v[56:59], v155 offset:0\n"
"v_mfma_f32_16x16x32_bf16 v[0:3], v[116:119], v[100:103], v[0:3]\n"
"ds_read_b128 v[60:63], v155 offset:9216\n"
"v_mfma_f32_16x16x32_bf16 v[12:15], v[120:123], v[104:107], v[12:15]\n"
"ds_read_b128 v[64:67], v155 offset:1024\n"
"v_mfma_f32_16x16x32_bf16 v[12:15], v[124:127], v[108:111], v[12:15]\n"
"ds_read_b128 v[68:71], v155 offset:10240\n"
"v_mfma_f32_16x16x32_bf16 v[8:11], v[120:123], v[96:99], v[8:11]\n"
"v_mfma_f32_16x16x32_bf16 v[8:11], v[124:127], v[100:103], v[8:11]\n"
"s_waitcnt vmcnt(8)\n"
"s_waitcnt lgkmcnt(0)\n"
"s_barrier\n"
"v_mfma_f32_16x16x32_bf16 v[20:23], v[72:75], v[48:51], v[20:23]\n"
"s_add_u32 s60, s63, 294912\n"
"s_add_u32 m0, s61, 36864\n"
"s_nop 0\n"
"buffer_load_dwordx4 v214, s[24:27], s60 offen offset:0 sc1 lds\n"
"ds_read_b128 v[96:99], v152 offset:49152\n"
"v_mfma_f32_16x16x32_bf16 v[20:23], v[76:79], v[52:55], v[20:23]\n"
"buffer_load_dwordx4 v214, s[24:27], s60 offen offset:1024 sc1 lds\n"
"v_mfma_f32_16x16x32_bf16 v[16:19], v[72:75], v[40:43], v[16:19]\n"
"ds_read_b128 v[100:103], v152 offset:50176\n"
"v_mfma_f32_16x16x32_bf16 v[16:19], v[76:79], v[44:47], v[16:19]\n"
"buffer_load_dwordx4 v214, s[24:27], s60 offen offset:2048 sc1 lds\n"
"v_mfma_f32_16x16x32_bf16 v[28:31], v[80:83], v[48:51], v[28:31]\n"
"ds_read_b128 v[104:107], v153 offset:49152\n"
"buffer_load_dwordx4 v214, s[24:27], s60 offen offset:3072 sc1 lds\n"
"v_mfma_f32_16x16x32_bf16 v[28:31], v[84:87], v[52:55], v[28:31]\n"
"ds_read_b128 v[108:111], v153 offset:50176\n"
"v_mfma_f32_16x16x32_bf16 v[24:27], v[80:83], v[40:43], v[24:27]\n"
"s_add_u32 m0, s62, 36864\n"
"s_cmp_ge_u32 s50, 2\n"
"s_cbranch_scc1 .Lf_s35\n"
"buffer_load_dwordx4 v215, s[24:27], s60 offen offset:0 sc1 lds\n"
".Lf_s35:\n"
"v_mfma_f32_16x16x32_bf16 v[24:27], v[84:87], v[44:47], v[24:27]\n"
"ds_read_b128 v[128:131], v157 offset:18432\n"
"v_mfma_f32_16x16x32_bf16 v[4:7], v[56:59], v[48:51], v[4:7]\n"
"ds_read_b128 v[132:135], v157 offset:27648\n"
"v_mfma_f32_16x16x32_bf16 v[4:7], v[60:63], v[52:55], v[4:7]\n"
"ds_read_b128 v[136:139], v157 offset:19456\n"
"ds_read_b128 v[140:143], v157 offset:28672\n"
"v_mfma_f32_16x16x32_bf16 v[0:3], v[56:59], v[40:43], v[0:3]\n"
"ds_read_b128 v[112:115], v155 offset:18432\n"
"v_mfma_f32_16x16x32_bf16 v[0:3], v[60:63], v[44:47], v[0:3]\n"
"ds_read_b128 v[116:119], v155 offset:27648\n"
"v_mfma_f32_16x16x32_bf16 v[12:15], v[64:67], v[48:51], v[12:15]\n"
"ds_read_b128 v[120:123], v155 offset:19456\n"
"v_mfma_f32_16x16x32_bf16 v[12:15], v[68:71], v[52:55], v[12:15]\n"
"ds_read_b128 v[124:127], v155 offset:28672\n"
"v_mfma_f32_16x16x32_bf16 v[8:11], v[64:67], v[40:43], v[8:11]\n"
"v_mfma_f32_16x16x32_bf16 v[8:11], v[68:71], v[44:47], v[8:11]\n"
"s_waitcnt vmcnt(8)\n"
"s_waitcnt lgkmcnt(0)\n"
"s_barrier\n"
"v_mfma_f32_16x16x32_bf16 v[20:23], v[128:131], v[104:107], v[20:23]\n"
"s_add_u32 s60, s63, 313344\n"
"s_add_u32 m0, s61, 55296\n"
"s_nop 0\n"
"buffer_load_dwordx4 v214, s[24:27], s60 offen offset:0 sc1 lds\n"
"ds_read_b128 v[40:43], v152 offset:0\n"
"v_mfma_f32_16x16x32_bf16 v[20:23], v[132:135], v[108:111], v[20:23]\n"
"buffer_load_dwordx4 v214, s[24:27], s60 offen offset:1024 sc1 lds\n"
"v_mfma_f32_16x16x32_bf16 v[16:19], v[128:131], v[96:99], v[16:19]\n"
"ds_read_b128 v[44:47], v152 offset:1024\n"
"v_mfma_f32_16x16x32_bf16 v[16:19], v[132:135], v[100:103], v[16:19]\n"
"buffer_load_dwordx4 v214, s[24:27], s60 offen offset:2048 sc1 lds\n"
"v_mfma_f32_16x16x32_bf16 v[28:31], v[136:139], v[104:107], v[28:31]\n"
"ds_read_b128 v[48:51], v153 offset:0\n"
"buffer_load_dwordx4 v214, s[24:27], s60 offen offset:3072 sc1 lds\n"
"v_mfma_f32_16x16x32_bf16 v[28:31], v[140:143], v[108:111], v[28:31]\n"
"ds_read_b128 v[52:55], v153 offset:1024\n"
"v_mfma_f32_16x16x32_bf16 v[24:27], v[136:139], v[96:99], v[24:27]\n"
"s_add_u32 m0, s62, 55296\n"
"s_cmp_ge_u32 s50, 2\n"
"s_cbranch_scc1 .Lf_s36\n"
"buffer_load_dwordx4 v215, s[24:27], s60 offen offset:0 sc1 lds\n"
".Lf_s36:\n"
"v_mfma_f32_16x16x32_bf16 v[24:27], v[140:143], v[100:103], v[24:27]\n"
"ds_read_b128 v[72:75], v156 offset:0\n"
"v_mfma_f32_16x16x32_bf16 v[4:7], v[112:115], v[104:107], v[4:7]\n"
"ds_read_b128 v[76:79], v156 offset:9216\n"
"v_mfma_f32_16x16x32_bf16 v[4:7], v[116:119], v[108:111], v[4:7]\n"
"ds_read_b128 v[80:83], v156 offset:1024\n"
"ds_read_b128 v[84:87], v156 offset:10240\n"
"v_mfma_f32_16x16x32_bf16 v[0:3], v[112:115], v[96:99], v[0:3]\n"
"ds_read_b128 v[56:59], v154 offset:0\n"
"v_mfma_f32_16x16x32_bf16 v[0:3], v[116:119], v[100:103], v[0:3]\n"
"ds_read_b128 v[60:63], v154 offset:9216\n"
"v_mfma_f32_16x16x32_bf16 v[12:15], v[120:123], v[104:107], v[12:15]\n"
"ds_read_b128 v[64:67], v154 offset:1024\n"
"v_mfma_f32_16x16x32_bf16 v[12:15], v[124:127], v[108:111], v[12:15]\n"
"ds_read_b128 v[68:71], v154 offset:10240\n"
"v_mfma_f32_16x16x32_bf16 v[8:11], v[120:123], v[96:99], v[8:11]\n"
"v_mfma_f32_16x16x32_bf16 v[8:11], v[124:127], v[100:103], v[8:11]\n"
"s_waitcnt vmcnt(8)\n"
"s_waitcnt lgkmcnt(0)\n"
"s_barrier\n"
"v_mfma_f32_16x16x32_bf16 v[20:23], v[72:75], v[48:51], v[20:23]\n"
"s_lshl_b32 s54, s72, 4\n"
"s_add_u32 s54, s54, s53\n"
"s_mul_i32 s78, s54, 9216\n"
"s_lshl_b32 s54, s49, 10\n"
"s_add_u32 s79, s78, s54\n"
"ds_read_b128 v[96:99], v152 offset:16384\n"
"s_lshl_b32 s55, s72, 2\n"
"s_add_u32 s55, s55, s53\n"
"s_mul_i32 s58, s55, 36864\n"
"s_lshl_b32 s54, s49, 10\n"
"s_add_u32 s59, s58, s54\n"
"v_mfma_f32_16x16x32_bf16 v[20:23], v[76:79], v[52:55], v[20:23]\n"
"ds_read_b128 v[100:103], v152 offset:17408\n"
"s_lshr_b32 s54, s53, 1\n"
"s_lshl_b32 s52, s54, 3\n"
"v_mfma_f32_16x16x32_bf16 v[16:19], v[72:75], v[40:43], v[16:19]\n"
"ds_read_b128 v[104:107], v153 offset:16384\n"
"v_add_u32 v164, s52, v254\n"
"v_mfma_f32_16x16x32_bf16 v[16:19], v[76:79], v[44:47], v[16:19]\n"
"ds_read_b128 v[108:111], v153 offset:17408\n"
"v_xor_b32 v165, 8, v164\n"
"v_mfma_f32_16x16x32_bf16 v[28:31], v[80:83], v[48:51], v[28:31]\n"
"ds_read_b128 v[128:131], v156 offset:18432\n"
"s_lshl_b32 s54, s72, 11\n"
"s_lshl_b32 s55, s49, 6\n"
"s_add_u32 s98, s54, s55\n"
"s_lshl_b32 s54, s53, 9\n"
"s_add_u32 s99, s98, s54\n"
"v_mfma_f32_16x16x32_bf16 v[28:31], v[84:87], v[52:55], v[28:31]\n"
"ds_read_b128 v[132:135], v156 offset:27648\n"
"v_lshlrev_b32 v166, 9, v219\n"
"v_mfma_f32_16x16x32_bf16 v[24:27], v[80:83], v[40:43], v[24:27]\n"
"ds_read_b128 v[136:139], v156 offset:19456\n"
"v_mov_b32 v167, 1\n"
"v_mfma_f32_16x16x32_bf16 v[24:27], v[84:87], v[44:47], v[24:27]\n"
"ds_read_b128 v[140:143], v156 offset:28672\n"
"v_mov_b32 v168, 0\n"
"v_mfma_f32_16x16x32_bf16 v[4:7], v[56:59], v[48:51], v[4:7]\n"
"ds_read_b128 v[112:115], v154 offset:18432\n"
"v_and_b32 v169, 15, v219\n"
"v_mfma_f32_16x16x32_bf16 v[4:7], v[60:63], v[52:55], v[4:7]\n"
"ds_read_b128 v[116:119], v154 offset:27648\n"
"v_mul_u32_u24 v169, 0x480, v169\n"
"v_mfma_f32_16x16x32_bf16 v[0:3], v[56:59], v[40:43], v[0:3]\n"
"ds_read_b128 v[120:123], v154 offset:19456\n"
"v_and_b32 v170, 48, v219\n"
"v_mfma_f32_16x16x32_bf16 v[0:3], v[60:63], v[44:47], v[0:3]\n"
"ds_read_b128 v[124:127], v154 offset:28672\n"
"v_add_u32 v171, v169, v170\n"
"v_mfma_f32_16x16x32_bf16 v[12:15], v[64:67], v[48:51], v[12:15]\n"
"v_lshrrev_b32 v170, 1, v170\n"
"v_add_u32 v172, v169, v170\n"
"v_mfma_f32_16x16x32_bf16 v[12:15], v[68:71], v[52:55], v[12:15]\n"
"s_lshl_b32 s54, 1, s53\n"
"s_xor_b32 s47, s54, 15\n"
"s_xor_b32 s54, s53, 1\n"
"s_mul_i32 s44, s54, 9216\n"
"s_xor_b32 s54, s53, 2\n"
"s_mul_i32 s45, s54, 9216\n"
"s_xor_b32 s54, s53, 3\n"
"s_mul_i32 s46, s54, 9216\n"
"s_mul_i32 s54, s76, 1152\n"
"s_lshl_b32 s55, s74, 1\n"
"s_add_u32 s74, s54, s55\n"
"s_lshl_b32 s55, s75, 1\n"
"s_add_u32 s75, s54, s55\n"
"s_xor_b32 s54, s76, 16\n"
"s_mul_i32 s54, s54, 1152\n"
"s_add_u32 s76, s54, s55\n"
"v_mfma_f32_16x16x32_bf16 v[8:11], v[64:67], v[40:43], v[8:11]\n"
"v_mfma_f32_16x16x32_bf16 v[8:11], v[68:71], v[44:47], v[8:11]\n"
"s_waitcnt vmcnt(4)\n"
"s_waitcnt lgkmcnt(0)\n"
"s_barrier\n"
"v_mfma_f32_16x16x32_bf16 v[20:23], v[128:131], v[104:107], v[20:23]\n"
"ds_read_b64 v[40:41], v162 offset:32768\n"
"ds_read_b64 v[42:43], v162 offset:33280\n"
"v_mfma_f32_16x16x32_bf16 v[20:23], v[132:135], v[108:111], v[20:23]\n"
"ds_read_b64 v[44:45], v162 offset:33792\n"
"v_mfma_f32_16x16x32_bf16 v[16:19], v[128:131], v[96:99], v[16:19]\n"
"ds_read_b64 v[46:47], v162 offset:34304\n"
"v_mfma_f32_16x16x32_bf16 v[16:19], v[132:135], v[100:103], v[16:19]\n"
"ds_read_b64 v[48:49], v163 offset:32768\n"
"v_mfma_f32_16x16x32_bf16 v[28:31], v[136:139], v[104:107], v[28:31]\n"
"ds_read_b64 v[50:51], v163 offset:33280\n"
"v_mfma_f32_16x16x32_bf16 v[28:31], v[140:143], v[108:111], v[28:31]\n"
"ds_read_b64 v[52:53], v163 offset:33792\n"
"v_mfma_f32_16x16x32_bf16 v[24:27], v[136:139], v[96:99], v[24:27]\n"
"ds_read_b64 v[54:55], v163 offset:34304\n"
"v_mfma_f32_16x16x32_bf16 v[24:27], v[140:143], v[100:103], v[24:27]\n"
"ds_read_b128 v[72:75], v157 offset:0\n"
"ds_read_b128 v[76:79], v157 offset:9216\n"
"v_mfma_f32_16x16x32_bf16 v[4:7], v[112:115], v[104:107], v[4:7]\n"
"ds_read_b128 v[80:83], v157 offset:1024\n"
"v_mfma_f32_16x16x32_bf16 v[4:7], v[116:119], v[108:111], v[4:7]\n"
"ds_read_b128 v[84:87], v157 offset:10240\n"
"v_mfma_f32_16x16x32_bf16 v[0:3], v[112:115], v[96:99], v[0:3]\n"
"ds_read_b128 v[56:59], v155 offset:0\n"
"v_mfma_f32_16x16x32_bf16 v[0:3], v[116:119], v[100:103], v[0:3]\n"
"ds_read_b128 v[60:63], v155 offset:9216\n"
"v_mfma_f32_16x16x32_bf16 v[12:15], v[120:123], v[104:107], v[12:15]\n"
"ds_read_b128 v[64:67], v155 offset:1024\n"
"v_mfma_f32_16x16x32_bf16 v[12:15], v[124:127], v[108:111], v[12:15]\n"
"ds_read_b128 v[68:71], v155 offset:10240\n"
"v_mfma_f32_16x16x32_bf16 v[8:11], v[120:123], v[96:99], v[8:11]\n"
"v_mfma_f32_16x16x32_bf16 v[8:11], v[124:127], v[100:103], v[8:11]\n"
"s_waitcnt vmcnt(0)\n"
"s_waitcnt lgkmcnt(0)\n"
"s_barrier\n"
"v_mfma_f32_16x16x32_bf16 v[20:23], v[72:75], v[48:51], v[20:23]\n"
"ds_read_b64 v[96:97], v162 offset:49152\n"
"ds_read_b64 v[98:99], v162 offset:49664\n"
"v_mfma_f32_16x16x32_bf16 v[20:23], v[76:79], v[52:55], v[20:23]\n"
"ds_read_b64 v[100:101], v162 offset:50176\n"
"v_mfma_f32_16x16x32_bf16 v[16:19], v[72:75], v[40:43], v[16:19]\n"
"ds_read_b64 v[102:103], v162 offset:50688\n"
"v_mfma_f32_16x16x32_bf16 v[16:19], v[76:79], v[44:47], v[16:19]\n"
"ds_read_b64 v[104:105], v163 offset:49152\n"
"v_mfma_f32_16x16x32_bf16 v[28:31], v[80:83], v[48:51], v[28:31]\n"
"ds_read_b64 v[106:107], v163 offset:49664\n"
"v_mfma_f32_16x16x32_bf16 v[28:31], v[84:87], v[52:55], v[28:31]\n"
"ds_read_b64 v[108:109], v163 offset:50176\n"
"v_mfma_f32_16x16x32_bf16 v[24:27], v[80:83], v[40:43], v[24:27]\n"
"ds_read_b64 v[110:111], v163 offset:50688\n"
"v_mfma_f32_16x16x32_bf16 v[24:27], v[84:87], v[44:47], v[24:27]\n"
"ds_read_b128 v[128:131], v157 offset:18432\n"
"ds_read_b128 v[132:135], v157 offset:27648\n"
"v_mfma_f32_16x16x32_bf16 v[4:7], v[56:59], v[48:51], v[4:7]\n"
"ds_read_b128 v[136:139], v157 offset:19456\n"
"v_mfma_f32_16x16x32_bf16 v[4:7], v[60:63], v[52:55], v[4:7]\n"
"ds_read_b128 v[140:143], v157 offset:28672\n"
"v_mfma_f32_16x16x32_bf16 v[0:3], v[56:59], v[40:43], v[0:3]\n"
"ds_read_b128 v[112:115], v155 offset:18432\n"
"v_mfma_f32_16x16x32_bf16 v[0:3], v[60:63], v[44:47], v[0:3]\n"
"ds_read_b128 v[116:119], v155 offset:27648\n"
"v_mfma_f32_16x16x32_bf16 v[12:15], v[64:67], v[48:51], v[12:15]\n"
"ds_read_b128 v[120:123], v155 offset:19456\n"
"v_mfma_f32_16x16x32_bf16 v[12:15], v[68:71], v[52:55], v[12:15]\n"
"ds_read_b128 v[124:127], v155 offset:28672\n"
"v_mfma_f32_16x16x32_bf16 v[8:11], v[64:67], v[40:43], v[8:11]\n"
"v_mfma_f32_16x16x32_bf16 v[8:11], v[68:71], v[44:47], v[8:11]\n"
"s_waitcnt lgkmcnt(7)\n"
"v_mfma_f32_16x16x32_bf16 v[20:23], v[128:131], v[104:107], v[20:23]\n"
"s_waitcnt lgkmcnt(6)\n"
"v_mfma_f32_16x16x32_bf16 v[20:23], v[132:135], v[108:111], v[20:23]\n"
"s_waitcnt lgkmcnt(5)\n"
"v_mfma_f32_16x16x32_bf16 v[28:31], v[136:139], v[104:107], v[28:31]\n"
"s_waitcnt lgkmcnt(4)\n"
"v_mfma_f32_16x16x32_bf16 v[28:31], v[140:143], v[108:111], v[28:31]\n"
"v_mfma_f32_16x16x32_bf16 v[16:19], v[128:131], v[96:99], v[16:19]\n"
"v_mfma_f32_16x16x32_bf16 v[16:19], v[132:135], v[100:103], v[16:19]\n"
"v_mfma_f32_16x16x32_bf16 v[24:27], v[136:139], v[96:99], v[24:27]\n"
"v_mfma_f32_16x16x32_bf16 v[24:27], v[140:143], v[100:103], v[24:27]\n"
"s_xor_b32 s54, s53, 3\n"
"s_mul_i32 s54, s54, 36864\n"
"s_add_u32 s60, s79, s54\n"
"v_cvt_pk_bf16_f32 v240, v20, v21\n"
"v_cvt_pk_bf16_f32 v241, v22, v23\n"
"v_cvt_pk_bf16_f32 v242, v28, v29\n"
"v_cvt_pk_bf16_f32 v243, v30, v31\n"
"buffer_store_dwordx4 v[240:243], v254, s[28:31], s60 offen offset:0 sc1\n"
"s_waitcnt lgkmcnt(3)\n"
"v_mfma_f32_16x16x32_bf16 v[4:7], v[112:115], v[104:107], v[4:7]\n"
"s_waitcnt lgkmcnt(2)\n"
"v_mfma_f32_16x16x32_bf16 v[4:7], v[116:119], v[108:111], v[4:7]\n"
"s_waitcnt lgkmcnt(1)\n"
"v_mfma_f32_16x16x32_bf16 v[12:15], v[120:123], v[104:107], v[12:15]\n"
"s_waitcnt lgkmcnt(0)\n"
"v_mfma_f32_16x16x32_bf16 v[12:15], v[124:127], v[108:111], v[12:15]\n"
"s_xor_b32 s54, s53, 1\n"
"s_mul_i32 s54, s54, 36864\n"
"s_add_u32 s60, s79, s54\n"
"v_cvt_pk_bf16_f32 v244, v16, v17\n"
"v_cvt_pk_bf16_f32 v245, v18, v19\n"
"v_cvt_pk_bf16_f32 v246, v24, v25\n"
"v_cvt_pk_bf16_f32 v247, v26, v27\n"
"buffer_store_dwordx4 v[244:247], v254, s[28:31], s60 offen offset:0 sc1\n"
"v_mfma_f32_16x16x32_bf16 v[0:3], v[112:115], v[96:99], v[0:3]\n"
"v_mfma_f32_16x16x32_bf16 v[0:3], v[116:119], v[100:103], v[0:3]\n"
"v_mfma_f32_16x16x32_bf16 v[8:11], v[120:123], v[96:99], v[8:11]\n"
"v_mfma_f32_16x16x32_bf16 v[8:11], v[124:127], v[100:103], v[8:11]\n"
"s_xor_b32 s54, s53, 2\n"
"s_mul_i32 s54, s54, 36864\n"
"s_add_u32 s60, s79, s54\n"
"v_cvt_pk_bf16_f32 v240, v4, v5\n"
"v_cvt_pk_bf16_f32 v241, v6, v7\n"
"v_cvt_pk_bf16_f32 v242, v12, v13\n"
"v_cvt_pk_bf16_f32 v243, v14, v15\n"
"buffer_store_dwordx4 v[240:243], v254, s[28:31], s60 offen offset:0 sc1\n"
"s_waitcnt vmcnt(0)\n"
"s_mov_b32 exec_hi, 0\n"
"s_mov_b32 exec_lo, s47\n"
"buffer_atomic_add v167, v166, s[64:67], s98 offen offset:0\n"
"s_mov_b32 exec_lo, 1\n"
"s_movk_i32 s55, 0\n"
".Lf_pollb:\n"
"v_mov_b32 v173, 0\n"
"buffer_atomic_add v173, v168, s[64:67], s99 offen offset:0 sc0\n"
"s_waitcnt vmcnt(0)\n"
"v_readfirstlane_b32 s54, v173\n"
"s_add_u32 s55, s55, 1\n"
"s_nop 0\n"
"s_cmp_eq_u32 s54, 3\n"
"s_cbranch_scc1 .Lf_gob\n"
"s_cmp_lt_u32 s55, 0x20000\n"
"s_cbranch_scc1 .Lf_pollb\n"
".Lf_gob:\n"
"s_mov_b64 exec, -1\n"
"s_add_u32 s60, s59, s44\n"
"buffer_load_dwordx4 v[180:183], v254, s[28:31], s60 offen offset:0 sc1\n"
"s_add_u32 s60, s59, s45\n"
"buffer_load_dwordx4 v[184:187], v254, s[28:31], s60 offen offset:0 sc1\n"
"s_add_u32 s60, s59, s46\n"
"buffer_load_dwordx4 v[188:191], v254, s[28:31], s60 offen offset:0 sc1\n"
"s_waitcnt vmcnt(2)\n"
"v_lshlrev_b32 v174, 16, v180\n"
"v_and_b32 v175, 0xffff0000, v180\n"
"v_pk_add_f32 v[0:1], v[0:1], v[174:175]\n"
"v_lshlrev_b32 v176, 16, v181\n"
"v_and_b32 v177, 0xffff0000, v181\n"
"v_pk_add_f32 v[2:3], v[2:3], v[176:177]\n"
"v_lshlrev_b32 v174, 16, v182\n"
"v_and_b32 v175, 0xffff0000, v182\n"
"v_pk_add_f32 v[8:9], v[8:9], v[174:175]\n"
"v_lshlrev_b32 v176, 16, v183\n"
"v_and_b32 v177, 0xffff0000, v183\n"
"v_pk_add_f32 v[10:11], v[10:11], v[176:177]\n"
"s_waitcnt vmcnt(1)\n"
"v_lshlrev_b32 v174, 16, v184\n"
"v_and_b32 v175, 0xffff0000, v184\n"
"v_pk_add_f32 v[0:1], v[0:1], v[174:175]\n"
"v_lshlrev_b32 v176, 16, v185\n"
"v_and_b32 v177, 0xffff0000, v185\n"
"v_pk_add_f32 v[2:3], v[2:3], v[176:177]\n"
"v_lshlrev_b32 v174, 16, v186\n"
"v_and_b32 v175, 0xffff0000, v186\n"
"v_pk_add_f32 v[8:9], v[8:9], v[174:175]\n"
"v_lshlrev_b32 v176, 16, v187\n"
"v_and_b32 v177, 0xffff0000, v187\n"
"v_pk_add_f32 v[10:11], v[10:11], v[176:177]\n"
"s_waitcnt vmcnt(0)\n"
"v_lshlrev_b32 v174, 16, v188\n"
"v_and_b32 v175, 0xffff0000, v188\n"
"v_pk_add_f32 v[0:1], v[0:1], v[174:175]\n"
"v_lshlrev_b32 v176, 16, v189\n"
"v_and_b32 v177, 0xffff0000, v189\n"
"v_pk_add_f32 v[2:3], v[2:3], v[176:177]\n"
"v_lshlrev_b32 v174, 16, v190\n"
"v_and_b32 v175, 0xffff0000, v190\n"
"v_pk_add_f32 v[8:9], v[8:9], v[174:175]\n"
"v_lshlrev_b32 v176, 16, v191\n"
"v_and_b32 v177, 0xffff0000, v191\n"
"v_pk_add_f32 v[10:11], v[10:11], v[176:177]\n"
"v_cvt_pk_bf16_f32 v240, v0, v1\n"
"v_cvt_pk_bf16_f32 v241, v2, v3\n"
"v_cvt_pk_bf16_f32 v242, v8, v9\n"
"v_cvt_pk_bf16_f32 v243, v10, v11\n"
"buffer_store_dwordx4 v[240:243], v171, s[40:43], s74 offen offset:0 \n"
"s_endpgm\n"
"s_nop 1\n"
".Lf_stslowb:\n"
"s_movk_i32 s55, 0\n"
".Lf_stlb:\n"
"s_sleep 2\n"
"buffer_load_dwordx4 v[220:223], v236, s[64:67], s99 offen offset:0 sc1\n"
"buffer_load_dwordx4 v[224:227], v236, s[64:67], s99 offen offset:512 sc1\n"
"buffer_load_dwordx4 v[228:231], v236, s[64:67], s99 offen offset:1024 sc1\n"
"buffer_load_dwordx4 v[232:235], v236, s[64:67], s99 offen offset:1536 sc1\n"
"s_waitcnt vmcnt(0)\n"
"v_and_b32 v237, v221, v223\n"
"v_and_b32 v238, v225, v227\n"
"v_and_b32 v237, v237, v229\n"
"v_and_b32 v238, v238, v231\n"
"v_and_b32 v237, v237, v233\n"
"v_and_b32 v238, v238, v235\n"
"v_and_b32 v237, v237, v238\n"
"v_cmp_ne_u32 s[96:97], 1, v237\n"
"s_nop 0\n"
"s_cmp_lg_u64 s[96:97], 0\n"
"s_cbranch_scc0 .Lf_stokb\n"
"s_add_u32 s55, s55, 1\n"
"s_cmp_lt_u32 s55, 0x20000\n"
"s_cbranch_scc1 .Lf_stlb\n"
"s_branch .Lf_stokb\n"
"s_nop 1\n"
".Lf_wps:\n"
"s_waitcnt vmcnt(0)\n"
"v_mov_b32 v158, v230\n"
"v_mov_b32 v159, v231\n"
"s_movk_i32 s55, 0\n"
"s_mov_b64 exec, 1\n"
"v_lshlrev_b32 v230, 6, v219\n"
".Lf_pww:\n"
"v_mov_b32 v231, 0\n"
"buffer_atomic_add v231, v230, s[64:67], s98 offen offset:0 sc0\n"
"s_waitcnt vmcnt(0)\n"
"v_cmp_ne_u32 s[96:97], 64, v231\n"
"s_add_u32 s55, s55, 1\n"
"s_nop 0\n"
"s_cmp_lg_u64 s[96:97], 0\n"
"s_cbranch_scc0 .Lf_pdw\n"
"s_cmp_lt_u32 s55, 0x20000\n"
"s_cbranch_scc1 .Lf_pww\n"
".Lf_pdw:\n"
"s_mov_b64 exec, -1\n"
"v_mov_b32 v230, v158\n"
"v_mov_b32 v231, v159\n"
"s_branch .Lf_wpok\n"
".Lk3_end:\n"
  :: "s"(__builtin_amdgcn_kernarg_segment_ptr()), "s"(b), "s"(w), "s"(sb)
  : "v0", "v1", "v2", "v3", "v4", "v5", "v6", "v7", "v8", "v9", "v10", "v11", "v12", "v13", "v14", "v15", "v16", "v17", "v18", "v19", "v20", "v21", "v22", "v23", "v24", "v25", "v26", "v27", "v28", "v29", "v30", "v31", "v32", "v33", "v34", "v35", "v36", "v37", "v38", "v39", "v40", "v41", "v42", "v43", "v44", "v45", "v46", "v47", "v48", "v49", "v50", "v51", "v52", "v53", "v54", "v55", "v56", "v57", "v58", "v59", "v60", "v61", "v62", "v63", "v64", "v65", "v66", "v67", "v68", "v69", "v70", "v71", "v72", "v73", "v74", "v75", "v76", "v77", "v78", "v79", "v80", "v81", "v82", "v83", "v84", "v85", "v86", "v87", "v88", "v89", "v90", "v91", "v92", "v93", "v94", "v95", "v96", "v97", "v98", "v99", "v100", "v101", "v102", "v103", "v104", "v105", "v106", "v107", "v108", "v109", "v110", "v111", "v112", "v113", "v114", "v115", "v116", "v117", "v118", "v119", "v120", "v121", "v122", "v123", "v124", "v125", "v126", "v127", "v128", "v129", "v130", "v131", "v132", "v133", "v134", "v135", "v136", "v137", "v138", "v139", "v140", "v141", "v142", "v143", "v144", "v145", "v146", "v147", "v148", "v149", "v150", "v151", "v152", "v153", "v154", "v155", "v156", "v157", "v158", "v159", "v160", "v161", "v162", "v163", "v164", "v165", "v166", "v167", "v168", "v169", "v170", "v171", "v172", "v173", "v174", "v175", "v176", "v177", "v178", "v179", "v180", "v181", "v182", "v183", "v184", "v185", "v186", "v187", "v188", "v189", "v190", "v191", "v192", "v193", "v194", "v195", "v196", "v197", "v198", "v199", "v200", "v201", "v202", "v203", "v204", "v205", "v206", "v207", "v208", "v209", "v210", "v211", "v212", "v213", "v214", "v215", "v216", "v217", "v218", "v219", "v220", "v221", "v222", "v223", "v224", "v225", "v226", "v227", "v228", "v229", "v230", "v231", "v232", "v233", "v234", "v235", "v236", "v237", "v238", "v239", "v240", "v241", "v242", "v243", "v244", "v245", "v246", "v247", "v248", "v249", "v250", "v251", "v252", "v253", "v254", "s20", "s21", "s22", "s23", "s24", "s25", "s26", "s27", "s28", "s29", "s30", "s31", "s32", "s33", "s34", "s35", "s36", "s37", "s38", "s39", "s40", "s41", "s42", "s43", "s44", "s45", "s46", "s47", "s48", "s49", "s50", "s51", "s52", "s53", "s54", "s55", "s56", "s57", "s58", "s59", "s60", "s61", "s62", "s63", "s64", "s65", "s66", "s67", "s68", "s69", "s70", "s71", "s72", "s73", "s74", "s75", "s76", "s77", "s78", "s79", "s80", "s81", "s82", "s83", "s84", "s85", "s86", "s87", "s88", "s89", "s90", "s91", "s92", "s93", "s94", "s95", "s96", "s97", "s98", "s99", "vcc", "scc", "memory");

}

extern "C" void kernel_launch(void* const* d_in, const int* in_sizes, int n_in, void* d_out, int out_size, void* d_ws, size_t ws_size, hipStream_t stream) {
  s4d_geglu<<<256, 512, 0, stream>>>((const u16*)d_in[0], (const u16*)d_in[1], (const u16*)d_in[2], (const u16*)d_in[3], (const u16*)d_in[4], (const u16*)d_in[5], (const u16*)d_in[6],
                                     (const u16*)d_in[8], (const u16*)d_in[9], (const u16*)d_in[10], (u16*)d_out, (char*)d_ws);
}
```
